```python
import torch

def _softmax(x, axis=-1):
    return torch.softmax(x, dim=axis)
def _gelu(x):
    return torch.nn.functional.gelu(x, approximate="tanh")
def _silu(x):
    return torch.nn.functional.silu(x)
def _sigmoid(x):
    return torch.sigmoid(x)
def _layernorm(x, axis=-1, eps=1e-5):
    m = x.mean(dim=axis, keepdim=True)
    v = x.var(dim=axis, keepdim=True, unbiased=False)
    return (x - m) / torch.sqrt(v + eps)
def _scatter_add(tgt, idx, src, axis=0):
    return tgt.clone().index_add_(axis, idx, src)
def _segment_sum(src, seg, n):
    out = torch.zeros((n,) + src.shape[1:], dtype=src.dtype)
    return out.index_add_(0, seg, src)
def _pad(x, pw):
    flat = tuple(v for pair in reversed(pw) for v in pair)
    return torch.nn.functional.pad(x, flat)

kernel_name = 'recurrent_scan__compressed_kv_cla__geglu_ffn__decode'

def setup_inputs():

    _w = (lambda a: (lambda t: t.to(torch.bfloat16) if t.is_floating_point() else t)(torch.as_tensor(a)))
    def _A_setup():
        B, H, Sq, Sk, D, M = 1, 32, 1, 16384, 128, 512
        Dc = 4 * D
        return {
            "x": _w((lambda *s: torch.randn(*s, dtype=torch.bfloat16) * 0.05)(B, Sq, M)),
            "c_kv": _w((lambda *s: torch.randn(*s, dtype=torch.bfloat16) * 0.8)(B, Sk, Dc)),
            "w_q": _w((lambda *s: torch.randn(*s, dtype=torch.bfloat16) * 1.6)(M, H, D) * (M ** -0.5)),
            "w_uk": _w((lambda *s: torch.randn(*s, dtype=torch.bfloat16) * 1.6)(Dc, H, D) * (Dc ** -0.5)),
            "w_uv": _w((lambda *s: torch.randn(*s, dtype=torch.bfloat16) * 0.8)(Dc, H, D) * (Dc ** -0.5)),
            "w_o": _w((lambda *s: torch.randn(*s, dtype=torch.bfloat16) * 0.8)(H, D, M) * ((H * D) ** -0.5)),
        }
    _A_in = _A_setup()

    def _B_setup():
        B, S, D, F = 1, 1, 512, 1792
        return {
            "x": _w((lambda *s: torch.randn(*s, dtype=torch.bfloat16) * 0.05)(B, S, D)),
            "w_gate": _w((lambda *s: torch.randn(*s, dtype=torch.bfloat16) * 0.2)(D, F)),
            "w_up": _w((lambda *s: torch.randn(*s, dtype=torch.bfloat16) * 0.2)(D, F)),
            "w_down": _w((lambda *s: torch.randn(*s, dtype=torch.bfloat16) * 0.2)(F, D)),
        }
    _B_in = _B_setup()

    return {
        "A_x": _A_in["x"],
        "A_c_kv": _A_in["c_kv"],
        "A_w_q": _A_in["w_q"],
        "A_w_uk": _A_in["w_uk"],
        "A_w_uv": _A_in["w_uv"],
        "A_w_o": _A_in["w_o"],
        "B_x": _B_in["x"],
        "B_w_gate": _B_in["w_gate"],
        "B_w_up": _B_in["w_up"],
        "B_w_down": _B_in["w_down"],
    }

def reference(A_x, A_c_kv, A_w_q, A_w_uk, A_w_uv, A_w_o, B_x, B_w_gate, B_w_up, B_w_down):
    def _A_ref(x, c_kv, w_q, w_uk, w_uv, w_o):
        B, Sq, M = x.shape[0], x.shape[1], x.shape[2]
        Sk = c_kv.shape[1]
        H, D = w_q.shape[1], w_q.shape[2]
        q = torch.einsum("bqm,mhd->bhqd", x, w_q)
        k = torch.einsum("bkc,chd->bhkd", c_kv, w_uk)
        v = torch.einsum("bkc,chd->bhkd", c_kv, w_uv)
        scores = torch.einsum("bhqd,bhkd->bhqk", q, k) * (D ** -0.5)
        off = Sk - Sq
        qi = torch.arange(Sq)[:, None]
        kj = torch.arange(Sk)[None, :]
        cmask = kj <= (qi + off)
        scores = torch.where(cmask[None, None], scores, -1e9)
        p = _softmax(scores, axis=-1)
        ctx = torch.einsum("bhqk,bhkd->bhqd", p, v)
        return torch.einsum("bhqd,hdm->bqm", ctx, w_o)

    def _B_ref(x, w_gate, w_up, w_down):
        return (_gelu(x @ w_gate) * (x @ w_up)) @ w_down

    x = A_x
    xn = _layernorm(x, axis=-1)
    s = _A_ref(xn, A_c_kv, A_w_q, A_w_uk, A_w_uv, A_w_o)
    g = _silu(_B_ref(xn, B_w_gate, B_w_up, B_w_down))
    return x + s * g
```

```cpp
#include <hip/hip_runtime.h>
#include <hip/hip_bf16.h>
#include <stdint.h>

typedef unsigned short u16;
static constexpr int M = 512, H = 32, D = 128, DC = 512, SK = 16384, F = 1792;

__device__ __forceinline__ float bf2f(u16 b) { return __uint_as_float(((unsigned)b) << 16); }
__device__ __forceinline__ u16 f2bf(float f) { unsigned u = __float_as_uint(f); u += 0x7FFFu + ((u >> 16) & 1u); return (u16)(u >> 16); }
__device__ __forceinline__ float rbf(float f) { return bf2f(f2bf(f)); }

__device__ void layernorm_to_lds(const u16* x, float* xn, float* red) {
  int t = threadIdx.x;
  float a = bf2f(x[t]), b = bf2f(x[t + 256]);
  float s = a + b;
  for (int o = 32; o > 0; o >>= 1) s += __shfl_xor(s, o);
  if ((t & 63) == 0) red[t >> 6] = s;
  __syncthreads();
  float mean = (red[0] + red[1] + red[2] + red[3]) * (1.0f / 512);
  __syncthreads();
  float da = a - mean, db = b - mean;
  float v = da * da + db * db;
  for (int o = 32; o > 0; o >>= 1) v += __shfl_xor(v, o);
  if ((t & 63) == 0) red[t >> 6] = v;
  __syncthreads();
  float var = (red[0] + red[1] + red[2] + red[3]) * (1.0f / 512);
  float mb = rbf(mean), vb = rbf(var);
  float den = rbf(sqrtf(rbf(vb + 1e-5f)));
  xn[t] = rbf(rbf(a - mb) / den);
  xn[t + 256] = rbf(rbf(b - mb) / den);
  __syncthreads();
}

__global__ void k1(const u16* x, const u16* wq, const u16* wg, const u16* wu, float* q, float* hid) {
  __shared__ float xn[512];
  __shared__ float red[4];
  layernorm_to_lds(x, xn, red);
  int col = blockIdx.x * 256 + threadIdx.x;
  if (col < 4096) {
    float acc = 0;
    for (int m = 0; m < M; ++m) acc += xn[m] * bf2f(wq[(size_t)m * 4096 + col]);
    q[col] = rbf(acc);
  } else {
    int f = col - 4096;
    if (f < F) {
      float g = 0, u = 0;
      for (int m = 0; m < M; ++m) { g += xn[m] * bf2f(wg[(size_t)m * F + f]); u += xn[m] * bf2f(wu[(size_t)m * F + f]); }
      g = rbf(g); u = rbf(u);
      float ge = 0.5f * g * (1.0f + tanhf(0.7978845608028654f * (g + 0.044715f * g * g * g)));
      hid[f] = rbf(rbf(ge) * u);
    }
  }
}
__global__ void k2(const float* q, const u16* wuk, float* qabs) {
  int idx = blockIdx.x * 256 + threadIdx.x;
  int c = idx >> 5, h = idx & 31;
  float acc = 0;
  for (int d = 0; d < D; ++d) acc += q[h * D + d] * bf2f(wuk[(size_t)c * 4096 + h * D + d]);
  qabs[h * DC + c] = acc * 0.08838834764831845f;
}
__global__ void k3(const float* qabs, const u16* ckv, float* part, float* pm, float* pl) {
  __shared__ float sc[32][64];
  __shared__ u16 kv[64][520];
  int t = threadIdx.x, k0 = blockIdx.x * 64;
  for (int i = t; i < 64 * 512; i += 256) kv[i >> 9][i & 511] = ckv[(size_t)k0 * 512 + i];
  __syncthreads();
  for (int i = t; i < 32 * 64; i += 256) {
    int h = i >> 6, k = i & 63;
    float acc = 0;
    for (int c = 0; c < DC; ++c) acc += qabs[h * DC + c] * bf2f(kv[k][c]);
    sc[h][k] = acc;
  }
  __syncthreads();
  if (t < 32) {
    float m = -1e30f;
    for (int k = 0; k < 64; ++k) m = fmaxf(m, sc[t][k]);
    float l = 0;
    for (int k = 0; k < 64; ++k) { float p = expf(sc[t][k] - m); sc[t][k] = p; l += p; }
    pm[blockIdx.x * 32 + t] = m; pl[blockIdx.x * 32 + t] = l;
  }
  __syncthreads();
  for (int i = t; i < 32 * 512; i += 256) {
    int h = i >> 9, c = i & 511;
    float acc = 0;
    for (int k = 0; k < 64; ++k) acc += sc[h][k] * bf2f(kv[k][c]);
    part[(size_t)blockIdx.x * 32 * 512 + i] = acc;
  }
}
__global__ void k4(const float* part, const float* pm, const float* pl, const u16* wuv, float* ctx) {
  __shared__ float w[256];
  __shared__ float lat[512];
  __shared__ float red[4];
  int h = blockIdx.x, t = threadIdx.x;
  float m = pm[t * 32 + h];
  float mm = m;
  for (int o = 32; o > 0; o >>= 1) mm = fmaxf(mm, __shfl_xor(mm, o));
  if ((t & 63) == 0) red[t >> 6] = mm;
  __syncthreads();
  mm = fmaxf(fmaxf(red[0], red[1]), fmaxf(red[2], red[3]));
  __syncthreads();
  float e = expf(m - mm);
  float l = e * pl[t * 32 + h];
  for (int o = 32; o > 0; o >>= 1) l += __shfl_xor(l, o);
  if ((t & 63) == 0) red[t >> 6] = l;
  __syncthreads();
  l = red[0] + red[1] + red[2] + red[3];
  w[t] = e / l;
  __syncthreads();
  for (int c = t; c < 512; c += 256) {
    float acc = 0;
    for (int s = 0; s < 256; ++s) acc += w[s] * part[(size_t)s * 32 * 512 + h * 512 + c];
    lat[c] = acc;
  }
  __syncthreads();
  if (t < 128) {
    float acc = 0;
    for (int c = 0; c < 512; ++c) acc += lat[c] * bf2f(wuv[(size_t)c * 4096 + h * D + t]);
    ctx[h * D + t] = rbf(acc);
  }
}
__global__ void k5(const u16* x, const float* ctx, const float* hid, const u16* wo, const u16* wd, u16* out) {
  __shared__ float r1[256], r2[256];
  int m = blockIdx.x * 2 + (threadIdx.x & 1);
  int j = threadIdx.x >> 1;
  float s = 0, y = 0;
  for (int i = j; i < 4096; i += 128) s += ctx[i] * bf2f(wo[(size_t)i * 512 + m]);
  for (int i = j; i < F; i += 128) y += hid[i] * bf2f(wd[(size_t)i * 512 + m]);
  r1[threadIdx.x] = s; r2[threadIdx.x] = y;
  __syncthreads();
  if (threadIdx.x < 2) {
    float ss = 0, yy = 0;
    for (int i = 0; i < 128; ++i) { ss += r1[i * 2 + threadIdx.x]; yy += r2[i * 2 + threadIdx.x]; }
    ss = rbf(ss); yy = rbf(yy);
    float g = rbf(yy / (1.0f + expf(-yy)));
    out[m] = f2bf(bf2f(x[m]) + rbf(ss * g));
  }
}

extern "C" void kernel_launch(void* const* d_in, const int* in_sizes, int n_in,
                              void* d_out, int out_size, void* d_ws, size_t ws_size,
                              hipStream_t stream) {
  const u16* x = (const u16*)d_in[0];
  const u16* ckv = (const u16*)d_in[1];
  const u16* wq = (const u16*)d_in[2];
  const u16* wuk = (const u16*)d_in[3];
  const u16* wuv = (const u16*)d_in[4];
  const u16* wo = (const u16*)d_in[5];
  const u16* wg = (const u16*)d_in[7];
  const u16* wu = (const u16*)d_in[8];
  const u16* wd = (const u16*)d_in[9];
  float* ws = (float*)d_ws;
  float* q = ws; float* hid = ws + 4096; float* qabs = ws + 8192; float* pm = qabs + 16384; float* pl = pm + 8192;
  float* ctx = pl + 8192; float* part = ctx + 4096;
  k1<<<(4096 + F + 255) / 256, 256, 0, stream>>>(x, wq, wg, wu, q, hid);
  k2<<<16384 / 256, 256, 0, stream>>>(q, wuk, qabs);
  k3<<<256, 256, 0, stream>>>(qabs, ckv, part, pm, pl);
  k4<<<32, 256, 0, stream>>>(part, pm, pl, wuv, ctx);
  k5<<<256, 256, 0, stream>>>(x, ctx, hid, wo, wd, (u16*)d_out);
}
```

```cpp
#include <hip/hip_runtime.h>
#include <stdint.h>

typedef unsigned short u16;
typedef unsigned long long u64;
typedef unsigned v4u __attribute__((ext_vector_type(4)));
typedef unsigned v2u __attribute__((ext_vector_type(2)));
typedef float f4 __attribute__((ext_vector_type(4)));
typedef float f2 __attribute__((ext_vector_type(2)));
typedef __bf16 bf8 __attribute__((ext_vector_type(8)));
typedef __bf16 bf2 __attribute__((ext_vector_type(2)));
typedef short s4 __attribute__((ext_vector_type(4)));
#define GAS __attribute__((address_space(1)))
#define LAS __attribute__((address_space(3)))

#define NT_AUX 1

constexpr int WS_QP = 4096;
constexpr int WS_QA = WS_QP + 131072;
constexpr int WS_FP = WS_QA + 65536;
constexpr int WS_ST = WS_FP + 131072;
constexpr int WS_SP = WS_ST + 262144;
constexpr int WS_YP = WS_SP + 524288;
constexpr int WS_OP = WS_YP + 524288;
constexpr int WS_END = WS_OP + 4194304;
constexpr int WS_STAMP = 16 << 20;
constexpr int LDS_BYTES = 163840;
constexpr unsigned SPIN_CAP = 1u << 16;
constexpr int AUX_SC1 = 16;

#define BAR() asm volatile("s_waitcnt lgkmcnt(0)\n\ts_barrier" ::: "memory")
#define FENCE() asm volatile("" ::: "memory")
#define SCHED() __builtin_amdgcn_sched_barrier(0)
#define VMWAIT(n) asm volatile("s_waitcnt vmcnt(" #n ")" ::: "memory")

#define STAMP(i) do {} while (0)

__device__ __forceinline__ float bflo(unsigned u) { return __uint_as_float(u << 16); }
__device__ __forceinline__ float bfhi(unsigned u) { return __uint_as_float(u & 0xFFFF0000u); }
__device__ __forceinline__ unsigned pk(float a, float b) { f2 f = {a, b}; return __builtin_bit_cast(unsigned, __builtin_convertvector(f, bf2)); }
__device__ __forceinline__ float rbf(float a) { return bflo(pk(a, a) & 0xFFFFu); }
__device__ __forceinline__ f4 mfma(v4u a, v4u b, f4 c) {
  return __builtin_amdgcn_mfma_f32_16x16x32_bf16(__builtin_bit_cast(bf8, a), __builtin_bit_cast(bf8, b), c, 0, 0, 0);
}
template <int CTRL> __device__ __forceinline__ float dpp(float v) {
  return __builtin_bit_cast(float, __builtin_amdgcn_update_dpp(0, __builtin_bit_cast(int, v), CTRL, 0xf, 0xf, true));
}
__device__ __forceinline__ float add16(float v) { unsigned u = __float_as_uint(v); auto r = __builtin_amdgcn_permlane16_swap(u, u, false, false); return __uint_as_float(r[0]) + __uint_as_float(r[1]); }
__device__ __forceinline__ float add32(float v) { unsigned u = __float_as_uint(v); auto r = __builtin_amdgcn_permlane32_swap(u, u, false, false); return __uint_as_float(r[0]) + __uint_as_float(r[1]); }
__device__ __forceinline__ float max16(float v) { unsigned u = __float_as_uint(v); auto r = __builtin_amdgcn_permlane16_swap(u, u, false, false); return fmaxf(__uint_as_float(r[0]), __uint_as_float(r[1])); }
__device__ __forceinline__ float max32(float v) { unsigned u = __float_as_uint(v); auto r = __builtin_amdgcn_permlane32_swap(u, u, false, false); return fmaxf(__uint_as_float(r[0]), __uint_as_float(r[1])); }
__device__ __forceinline__ float wave_sum(float v) {
  v += dpp<0x128>(v); v += dpp<0x124>(v); v += dpp<0x122>(v); v += dpp<0x121>(v);
  return add32(add16(v));
}
__device__ __forceinline__ v4u ldw(const void* p) {
  return __builtin_nontemporal_load((const GAS v4u*)p);
}
__device__ __forceinline__ void dma16(const void* src, unsigned lds_dst) {
  asm volatile("s_mov_b32 m0, %1\n\ts_nop 0\n\tglobal_load_lds_dwordx4 %0, off nt" :: "v"(src), "s"(lds_dst) : "memory");
}
__device__ __forceinline__ v4u lds128(LAS char* L, int off) { return *(LAS v4u*)(L + off); }
__device__ __forceinline__ v2u ldstr(LAS char* L, int off) { return __builtin_bit_cast(v2u, __builtin_amdgcn_ds_read_tr16_b64_v4i16((LAS s4*)(L + off))); }
__device__ __forceinline__ int swz(int r) { return ((r & 7) << 1) | ((r >> 3) & 1); }
__device__ __forceinline__ v4u xmask(unsigned bits16, const unsigned (&mk)[4]) {
  unsigned xx = bits16 | (bits16 << 16);
  return v4u{xx & mk[0], xx & mk[1], xx & mk[2], xx & mk[3]};
}
__device__ __forceinline__ void ln_stats(v4u xa, float& mb, float& den) {
  float v[8] = {bflo(xa.x), bfhi(xa.x), bflo(xa.y), bfhi(xa.y), bflo(xa.z), bfhi(xa.z), bflo(xa.w), bfhi(xa.w)};
  float s = ((v[0] + v[1]) + (v[2] + v[3])) + ((v[4] + v[5]) + (v[6] + v[7]));
  float mean = wave_sum(s) * (1.0f / 512.0f);
  float q = 0.f;
#pragma unroll
  for (int i = 0; i < 8; ++i) { float d = v[i] - mean; q += d * d; }
  float var = wave_sum(q) * (1.0f / 512.0f);
  mb = rbf(mean);
  den = rbf(__builtin_sqrtf(rbf(rbf(var) + 1e-5f)));
}
__device__ __forceinline__ unsigned ln_apply(float x, float mb, float den) { return pk(rbf(x - mb) / den, 0.f) & 0xFFFFu; }

__device__ __forceinline__ void gemv4_publish(LAS char* L, int red_off, const v4u (&wv)[4], v2u x4, float mb, float den, const unsigned (&mk)[4],
                                              __amdgpu_buffer_rsrc_t rs, int dst_off, unsigned tag, int w, int lane) {
  const int r16 = lane & 15, g = lane >> 4;
  f4 acc = {0.f, 0.f, 0.f, 0.f};
  acc = mfma(xmask(ln_apply(bflo(x4.x), mb, den), mk), wv[0], acc);
  acc = mfma(xmask(ln_apply(bfhi(x4.x), mb, den), mk), wv[1], acc);
  acc = mfma(xmask(ln_apply(bflo(x4.y), mb, den), mk), wv[2], acc);
  acc = mfma(xmask(ln_apply(bfhi(x4.y), mb, den), mk), wv[3], acc);
  if (lane < 32) *(LAS f4*)(L + red_off + (w * 128 + 8 * r16 + 4 * g) * 4) = acc;
  BAR();
  if (w == 0) {
    f2 s = {0.f, 0.f};
#pragma unroll
    for (int i = 0; i < 8; ++i) s += *(LAS f2*)(L + red_off + (i * 128 + 2 * lane) * 4);
    v4u o = {__float_as_uint(s.x), tag, __float_as_uint(s.y), tag};
    __builtin_amdgcn_raw_buffer_store_b128(o, rs, dst_off + lane * 16, 0, AUX_SC1);
  }
}

__global__ void __launch_bounds__(512, 2) mla_decode(const u16* x, const u16* ckv, const u16* wq, const u16* wuk, const u16* wuv, const u16* wo,
                                                      const u16* wg, const u16* wu, const u16* wd, u16* out, unsigned char* ws) {
  extern __shared__ __attribute__((aligned(16))) char smem[];
  LAS char* L = (LAS char*)smem;
  STAMP(0);
  const int b = blockIdx.x, tid = threadIdx.x, lane = tid & 63, w = __builtin_amdgcn_readfirstlane(tid >> 6);
  const int xcd = b & 7, rr = (b >> 3) & 15;
  const bool isA = b >= 128;
  const int r16 = lane & 15, g = lane >> 4;
  const __amdgpu_buffer_rsrc_t rs = __builtin_amdgcn_make_buffer_rsrc(ws, 0, WS_END, 0x00020000);
  const int fl = swz(4 * (w & 3) + g);
  const int dchunk = (r16 ^ fl) * 16;
  const int trrow = 4 * g + (r16 >> 2);
  const int trbase = trrow * 256 + ((((r16 >> 1) & 1) ^ swz(trrow)) * 16) + 8 * (r16 & 1);
  unsigned tag;

  if (!isA) {
    const int h = xcd * 4 + (rr & 3), j = rr >> 2, cons = h * 4 + j;
    const int row0 = 128 * j + 16 * w;
    v4u wqv[4], wkv[4];
    const v4u xa = *(const GAS v4u*)(x + 8 * lane);
    const v2u x4 = *(const GAS v2u*)(x + row0 + 4 * g);
#pragma unroll
    for (int i = 0; i < 4; ++i) wqv[i] = ldw(wq + (size_t)(row0 + 4 * g + i) * 4096 + h * 128 + 8 * r16);
#pragma unroll
    for (int s = 0; s < 4; ++s) wkv[s] = ldw(wuk + (size_t)(row0 + r16) * 4096 + h * 128 + 32 * s + 8 * g);
    SCHED();
    tag = *(const GAS unsigned*)ws + 1u;
    tag = __builtin_amdgcn_readfirstlane(tag);
    unsigned mk[4];
#pragma unroll
    for (int k = 0; k < 4; ++k) mk[k] = ((r16 >> 1) == k) ? ((r16 & 1) ? 0xFFFF0000u : 0x0000FFFFu) : 0u;
    float mb, den;
    ln_stats(xa, mb, den);
    gemv4_publish(L, 0, wqv, x4, mb, den, mk, rs, WS_QP + cons * 1024, tag, w, lane);
    STAMP(1);
    {
      v4u p[4]; unsigned spins = 0; bool ok;
      do {
        FENCE();
#pragma unroll
        for (int i = 0; i < 4; ++i) p[i] = __builtin_amdgcn_raw_buffer_load_b128(rs, WS_QP + (h * 4 + i) * 1024 + lane * 16, 0, AUX_SC1);
        ok = true;
#pragma unroll
        for (int i = 0; i < 4; ++i) ok &= (p[i].y == tag) & (p[i].w == tag);
      } while (!__all(ok) && ++spins < SPIN_CAP);
      float q0 = (__uint_as_float(p[0].x) + __uint_as_float(p[1].x)) + (__uint_as_float(p[2].x) + __uint_as_float(p[3].x));
      float q1 = (__uint_as_float(p[0].z) + __uint_as_float(p[1].z)) + (__uint_as_float(p[2].z) + __uint_as_float(p[3].z));
      *(LAS unsigned*)(L + 4096 + w * 256 + lane * 4) = pk(q0, q1);
    }
    STAMP(2);
    FENCE();
    {
      f4 acc = {0.f, 0.f, 0.f, 0.f};
#pragma unroll
      for (int s = 0; s < 4; ++s) acc = mfma(wkv[s], lds128(L, 4096 + w * 256 + (16 * s + 4 * g) * 4), acc);
      const float sc = 0.08838834764831845f * 1.4426950408889634f;
      v4u o = {pk(acc[0] * sc, acc[1] * sc), tag, pk(acc[2] * sc, acc[3] * sc), tag};
      if (r16 == 0) __builtin_amdgcn_raw_buffer_store_b128(o, rs, WS_QA + cons * 512 + (4 * w + g) * 16, 0, AUX_SC1);
    }
    STAMP(3);
    BAR();
    {
      const char* suv = (const char*)wuv + ((size_t)(128 * j + 64 * (w >> 2) + 16 * g + (w & 3)) * 4096 + h * 128) * 2 + dchunk;
#pragma unroll
      for (int n = 0; n < 4; ++n) dma16(suv + (size_t)n * 4 * 8192, 131072 + (32 * n + 4 * w) * 256);
      const char* so = (const char*)wo + (size_t)(h * 128 + 8 * (w & 3) + 4 * (w >> 2) + g) * 1024 + dchunk;
#pragma unroll
      for (int qq = 0; qq < 4; ++qq) {
        const int q = (qq + j) & 3;
#pragma unroll
        for (int n = 0; n < 4; ++n) dma16(so + n * 32 * 1024 + q * 256, q * 32768 + (32 * n + 4 * w) * 256);
      }
    }
    VMWAIT(0);
    BAR();
    STAMP(4);
    v4u buv[4], bo[4][4];
#pragma unroll
    for (int ks = 0; ks < 4; ++ks) {
      const int a = 131072 + (trbase ^ (32 * w)) + ks * 8192;
      v2u lo = ldstr(L, a), hi = ldstr(L, a + 4096);
      buv[ks] = v4u{lo.x, lo.y, hi.x, hi.y};
    }
#pragma unroll
    for (int t = 0; t < 4; ++t)
#pragma unroll
      for (int ks = 0; ks < 4; ++ks) {
        const int a = (w >> 1) * 32768 + (trbase ^ (32 * (4 * (w & 1) + t))) + ks * 8192;
        v2u lo = ldstr(L, a), hi = ldstr(L, a + 4096);
        bo[t][ks] = v4u{lo.x, lo.y, hi.x, hi.y};
      }
    BAR();
    STAMP(5);
    v4u st[4], od[4];
    {
      unsigned spins = 0; bool ok;
      do {
        FENCE();
#pragma unroll
        for (int i = 0; i < 4; ++i) st[i] = __builtin_amdgcn_raw_buffer_load_b128(rs, WS_ST + (cons * 128 + 16 * w + 4 * g + i) * 16, 0, AUX_SC1);
        ok = true;
#pragma unroll
        for (int i = 0; i < 4; ++i) ok &= (st[i].y == tag) & (st[i].w == tag);
      } while (!__all(ok) && ++spins < SPIN_CAP);
    }
    STAMP(6);
    FENCE();
#pragma unroll
    for (int i = 0; i < 4; ++i) od[i] = __builtin_amdgcn_raw_buffer_load_b128(rs, WS_OP + (cons * 128 + 16 * w + 4 * g + i) * 256 + r16 * 16, 0, AUX_SC1);
    {
      float m[4], mw;
#pragma unroll
      for (int i = 0; i < 4; ++i) m[i] = __uint_as_float(st[i].x);
      mw = max32(max16(fmaxf(fmaxf(m[0], m[1]), fmaxf(m[2], m[3]))));
      unsigned wb[4]; float lw = 0.f;
#pragma unroll
      for (int i = 0; i < 4; ++i) { wb[i] = pk(__builtin_amdgcn_exp2f(m[i] - mw), 0.f) & 0xFFFFu; lw += bflo(wb[i]) * __uint_as_float(st[i].z); }
      lw = add32(add16(lw));
      f4 acc = {0.f, 0.f, 0.f, 0.f};
#pragma unroll
      for (int i = 0; i < 4; ++i) acc = mfma(xmask(wb[i], mk), od[i], acc);
      if (lane < 32) *(LAS v2u*)(L + w * 272 + (8 * r16 + 4 * g) * 2) = v2u{pk(acc[0], acc[1]), pk(acc[2], acc[3])};
      if (lane == 0) *(LAS f2*)(L + 4096 + w * 8) = f2{mw, lw};
    }
    STAMP(7);
    BAR();
    {
      f4 acc = {0.f, 0.f, 0.f, 0.f};
#pragma unroll
      for (int ks = 0; ks < 4; ++ks) acc = mfma(lds128(L, (r16 & 7) * 272 + (32 * ks + 8 * g) * 2), buv[ks], acc);
      const f4 s0 = *(LAS f4*)(L + 4096 + (g & 1) * 32), s1 = *(LAS f4*)(L + 4096 + (g & 1) * 32 + 16);
      const float M = max16(fmaxf(fmaxf(s0[0], s0[2]), fmaxf(s1[0], s1[2])));
      const float f0 = __builtin_amdgcn_exp2f(s0[0] - M), f1 = __builtin_amdgcn_exp2f(s0[2] - M), f2_ = __builtin_amdgcn_exp2f(s1[0] - M), f3 = __builtin_amdgcn_exp2f(s1[2] - M);
      const float Lt = add16((f0 * s0[1] + f1 * s0[3]) + (f2_ * s1[1] + f3 * s1[3]));
      const float c = add16((f0 * acc[0] + f1 * acc[1]) + (f2_ * acc[2] + f3 * acc[3])) / Lt;
      if (lane < 16) *(LAS u16*)(L + 8192 + (16 * w + r16) * 2) = (u16)(pk(c, 0.f) & 0xFFFFu);
    }
    BAR();
    {
      v4u cf[4];
#pragma unroll
      for (int ks = 0; ks < 4; ++ks) cf[ks] = lds128(L, 8192 + (32 * ks + 8 * g) * 2);
      f4 acc[4];
#pragma unroll
      for (int t = 0; t < 4; ++t) acc[t] = f4{0.f, 0.f, 0.f, 0.f};
#pragma unroll
      for (int ks = 0; ks < 4; ++ks)
#pragma unroll
        for (int t = 0; t < 4; ++t) acc[t] = mfma(cf[ks], bo[t][ks], acc[t]);
      const float v = (g == 0) ? acc[0][0] : (g == 1) ? acc[1][0] : (g == 2) ? acc[2][0] : acc[3][0];
      __builtin_amdgcn_raw_buffer_store_b64(v2u{__float_as_uint(v), tag}, rs, WS_SP + ((4 * w + g) * 128 + cons) * 128 + r16 * 8, 0, AUX_SC1);
    }
    STAMP(8);
  } else {
    const int ai = rr * 8 + xcd;
    {
      const char* src = (const char*)ckv + (size_t)(128 * ai + 4 * w + g) * 1024 + dchunk;
#pragma unroll
      for (int q = 0; q < 4; ++q)
#pragma unroll
        for (int n = 0; n < 4; ++n) dma16(src + n * 32 * 1024 + q * 256, q * 32768 + (32 * n + 4 * w) * 256);
    }
    STAMP(1);
    const int ht = w & 1, kt = w >> 1;
    const int rbase = r16 * 256 + ((4 * g) ^ swz(r16)) * 16;
    v4u kf[2][16];
    VMWAIT(8);
    BAR();
#pragma unroll
    for (int s = 0; s < 8; ++s)
#pragma unroll
      for (int u = 0; u < 2; ++u) kf[u][s] = lds128(L, (s >> 2) * 32768 + (32 * kt + 16 * u) * 256 + (rbase ^ ((s & 3) * 16)));
    FENCE();
    tag = __builtin_amdgcn_readfirstlane(*(const GAS unsigned*)ws) + 1u;
    v4u qg[8];
#pragma unroll
    for (int n = 0; n < 8; ++n) qg[n] = __builtin_amdgcn_raw_buffer_load_b128(rs, WS_QA + (n * 512 + tid) * 16, 0, AUX_SC1);
    VMWAIT(8);
    BAR();
    STAMP(2);
#pragma unroll
    for (int s = 8; s < 16; ++s)
#pragma unroll
      for (int u = 0; u < 2; ++u) kf[u][s] = lds128(L, (s >> 2) * 32768 + (32 * kt + 16 * u) * 256 + (rbase ^ ((s & 3) * 16)));
    v2u v00[8];
#pragma unroll
    for (int t = 0; t < 8; ++t) v00[t] = ldstr(L, kt * 32768 + (trbase ^ (32 * t)));
    {
      unsigned spins = 0;
      for (;;) {
        bool ok = true;
#pragma unroll
        for (int n = 0; n < 8; ++n) ok &= (qg[n].y == tag) & (qg[n].w == tag);
        if (__all(ok) || ++spins >= SPIN_CAP) break;
        FENCE();
#pragma unroll
        for (int n = 0; n < 8; ++n) qg[n] = __builtin_amdgcn_raw_buffer_load_b128(rs, WS_QA + (n * 512 + tid) * 16, 0, AUX_SC1);
      }
      const int gam = tid & 31, jq = (tid >> 5) & 3, h0 = tid >> 7;
#pragma unroll
      for (int n = 0; n < 8; ++n) {
        const int hh = 4 * n + h0;
        *(LAS v2u*)(L + 131072 + jq * 8192 + hh * 256 + (((gam >> 1) ^ swz(hh & 15)) * 16) + (gam & 1) * 8) = v2u{qg[n].x, qg[n].z};
      }
    }
    STAMP(3);
    const int mi = ai & 3, fi = (ai < 112) ? (ai >> 2) : 0;
    v4u fw[4], dw[2];
    {
      const u16* p = ((r16 < 8) ? wg : wu) + (size_t)(128 * mi + 16 * w + 4 * g) * 1792 + 64 * fi + 8 * (r16 & 7);
#pragma unroll
      for (int i = 0; i < 4; ++i) fw[i] = ldw(p + i * 1792);
      const u16* pd = wd + (size_t)(64 * fi + 16 * mi + 2 * g + (r16 >> 3)) * 512 + 64 * w + 8 * (r16 & 7);
#pragma unroll
      for (int i = 0; i < 2; ++i) dw[i] = ldw(pd + i * 8 * 512);
    }
    BAR();
    f4 S[2] = {f4{0.f, 0.f, 0.f, 0.f}, f4{0.f, 0.f, 0.f, 0.f}};
#pragma unroll
    for (int s = 0; s < 16; ++s) {
      const v4u qf = lds128(L, 131072 + (s >> 2) * 8192 + ht * 4096 + (rbase ^ ((s & 3) * 16)));
      S[0] = mfma(kf[0][s], qf, S[0]);
      S[1] = mfma(kf[1][s], qf, S[1]);
    }
    {
      float mx = fmaxf(fmaxf(fmaxf(S[0][0], S[0][1]), fmaxf(S[0][2], S[0][3])), fmaxf(fmaxf(S[1][0], S[1][1]), fmaxf(S[1][2], S[1][3])));
      mx = max32(max16(mx));
      if (lane < 16) *(LAS float*)(L + (16 * ht + r16) * 16 + kt * 4) = mx;
    }
    STAMP(4);
    BAR();
    float M;
    {
      const f4 m4 = *(LAS f4*)(L + (16 * ht + r16) * 16);
      M = fmaxf(fmaxf(m4[0], m4[1]), fmaxf(m4[2], m4[3]));
      float p[8], l = 0.f;
#pragma unroll
      for (int i = 0; i < 8; ++i) { p[i] = __builtin_amdgcn_exp2f(S[i >> 2][i & 3] - M); l += p[i]; }
      *(LAS v4u*)(L + 131072 + (ht * 4 + kt) * 1024 + lane * 16) = v4u{pk(p[0], p[1]), pk(p[2], p[3]), pk(p[4], p[5]), pk(p[6], p[7])};
      l = add32(add16(l));
      if (lane < 16) *(LAS float*)(L + 512 + (16 * ht + r16) * 16 + kt * 4) = l;
    }
    BAR();
    STAMP(5);
    {
      f4 O[8];
#pragma unroll
      for (int t = 0; t < 8; ++t) O[t] = f4{0.f, 0.f, 0.f, 0.f};
#pragma unroll
      for (int ks = 0; ks < 4; ++ks) {
        const v4u pf = lds128(L, 131072 + (ht * 4 + ks) * 1024 + lane * 16);
#pragma unroll
        for (int t = 0; t < 8; ++t) {
          const int a = kt * 32768 + (trbase ^ (32 * t)) + ks * 8192;
          const v2u lo = (ks == 0) ? v00[t] : ldstr(L, a), hi = ldstr(L, a + 4096);
          O[t] = mfma(pf, v4u{lo.x, lo.y, hi.x, hi.y}, O[t]);
        }
      }
#pragma unroll
      for (int i = 0; i < 4; ++i) {
        const v4u o = {pk(O[0][i], O[1][i]), pk(O[2][i], O[3][i]), pk(O[4][i], O[5][i]), pk(O[6][i], O[7][i])};
        __builtin_amdgcn_raw_buffer_store_b128(o, rs, WS_OP + (((16 * ht + 4 * g + i) * 4 + kt) * 128 + ai) * 256 + r16 * 16, 0, AUX_SC1);
      }
      const f4 l4 = *(LAS f4*)(L + 512 + (16 * ht + r16) * 16);
      const float Lt = (l4[0] + l4[1]) + (l4[2] + l4[3]);
      STAMP(6);
      VMWAIT(0);
      if (lane < 16) __builtin_amdgcn_raw_buffer_store_b128(v4u{__float_as_uint(M), tag, __float_as_uint(Lt), tag}, rs, WS_ST + (((16 * ht + r16) * 4 + kt) * 128 + ai) * 16, 0, AUX_SC1);
    }
    STAMP(7);
    if (ai < 112) {
      const v4u xa = *(const GAS v4u*)(x + 8 * lane);
      const v2u x4 = *(const GAS v2u*)(x + 128 * mi + 16 * w + 4 * g);
      unsigned mk[4];
#pragma unroll
      for (int k = 0; k < 4; ++k) mk[k] = ((r16 >> 1) == k) ? ((r16 & 1) ? 0xFFFF0000u : 0x0000FFFFu) : 0u;
      float mb, den;
      ln_stats(xa, mb, den);
      gemv4_publish(L, 131072 + 8192, fw, x4, mb, den, mk, rs, WS_FP + (fi * 4 + mi) * 1024, tag, w, lane);
      v4u p; unsigned spins = 0;
      const int gu = (r16 >> 2) & 1, hi = r16 >> 3;
      do {
        FENCE();
        p = __builtin_amdgcn_raw_buffer_load_b128(rs, WS_FP + (fi * 4 + (r16 & 3)) * 1024 + (64 * gu + 16 * mi + 8 * hi + 2 * g) * 8, 0, AUX_SC1);
      } while (!__all((p.y == tag) & (p.w == tag)) && ++spins < SPIN_CAP);
      float v0 = __uint_as_float(p.x), v1 = __uint_as_float(p.z);
      v0 += dpp<0xB1>(v0); v1 += dpp<0xB1>(v1);
      v0 += dpp<0x4E>(v0); v1 += dpp<0x4E>(v1);
      const float o0 = dpp<0x141>(v0), o1 = dpp<0x141>(v1);
      float hv[2];
#pragma unroll
      for (int e = 0; e < 2; ++e) {
        const float gt = rbf(gu ? (e ? o1 : o0) : (e ? v1 : v0)), up = rbf(gu ? (e ? v1 : v0) : (e ? o1 : o0));
        const float z2 = 2.0f * 0.7978845608028654f * 1.4426950408889634f * (gt + 0.044715f * gt * gt * gt);
        const float ge = gt / (1.0f + __builtin_amdgcn_exp2f(-z2));
        hv[e] = rbf(ge) * up;
      }
      const float t0 = dpp<0x128>(hv[0]), t1 = dpp<0x128>(hv[1]);
      unsigned mk2[4];
#pragma unroll
      for (int k = 0; k < 4; ++k) mk2[k] = (((r16 & 7) >> 1) == k) ? ((r16 & 1) ? 0xFFFF0000u : 0x0000FFFFu) : 0u;
      f4 acc = {0.f, 0.f, 0.f, 0.f};
      acc = mfma(xmask(pk(hi ? t1 : hv[0], 0.f) & 0xFFFFu, mk2), dw[0], acc);
      acc = mfma(xmask(pk(hi ? hv[1] : t0, 0.f) & 0xFFFFu, mk2), dw[1], acc);
      float y[4];
#pragma unroll
      for (int i = 0; i < 4; ++i) {
        const unsigned u = __float_as_uint(dpp<0x128>(acc[i]));
        auto r = __builtin_amdgcn_permlane32_swap(u, u, false, false);
        y[i] = acc[i] + __uint_as_float(r[1]);
      }
      if (r16 < 8 && g < 2) {
        const int off = WS_YP + ((4 * w + (r16 >> 1)) * 128 + ai) * 128 + ((r16 & 1) * 8 + 4 * g) * 8;
        __builtin_amdgcn_raw_buffer_store_b128(v4u{__float_as_uint(y[0]), tag, __float_as_uint(y[1]), tag}, rs, off, 0, AUX_SC1);
        __builtin_amdgcn_raw_buffer_store_b128(v4u{__float_as_uint(y[2]), tag, __float_as_uint(y[3]), tag}, rs, off + 16, 0, AUX_SC1);
      }
    }
    STAMP(8);
    if (w == 7) {
      const int G = xcd * 4 + (rr & 3), qd = rr >> 2;
      const int off = (G * 128 + (lane >> 1)) * 128 + qd * 32 + (lane & 1) * 16;
      const unsigned xo = *(const GAS unsigned*)(x + 16 * G + 4 * qd + 2 * (lane & 1));
      float res[2][2];
#pragma unroll
      for (int pass = 0; pass < 2; ++pass) {
        v4u p[4]; unsigned spins = 0; bool ok;
        const bool skip3 = (pass == 0) && (lane >= 32);
        do {
          FENCE();
#pragma unroll
          for (int n = 0; n < 4; ++n) p[n] = __builtin_amdgcn_raw_buffer_load_b128(rs, (pass ? WS_SP : WS_YP) + off + n * 4096, 0, AUX_SC1);
          if (skip3) p[3] = v4u{0u, tag, 0u, tag};
          ok = true;
#pragma unroll
          for (int n = 0; n < 4; ++n) ok &= (p[n].y == tag) & (p[n].w == tag);
        } while (!__all(ok) && ++spins < SPIN_CAP);
        float a0 = (__uint_as_float(p[0].x) + __uint_as_float(p[1].x)) + (__uint_as_float(p[2].x) + __uint_as_float(p[3].x));
        float a1 = (__uint_as_float(p[0].z) + __uint_as_float(p[1].z)) + (__uint_as_float(p[2].z) + __uint_as_float(p[3].z));
        a0 += dpp<0x128>(a0); a1 += dpp<0x128>(a1);
        a0 += dpp<0x124>(a0); a1 += dpp<0x124>(a1);
        a0 += dpp<0x122>(a0); a1 += dpp<0x122>(a1);
        a0 = add32(add16(a0)); a1 = add32(add16(a1));
        if (pass == 0) {
          a0 = rbf(a0); a1 = rbf(a1);
          a0 = rbf(a0 / (1.0f + __builtin_amdgcn_exp2f(-1.4426950408889634f * a0)));
          a1 = rbf(a1 / (1.0f + __builtin_amdgcn_exp2f(-1.4426950408889634f * a1)));
          STAMP(9);
        }
        res[pass][0] = a0; res[pass][1] = a1;
      }
      const unsigned o = pk(bflo(xo) + rbf(rbf(res[1][0]) * res[0][0]), bfhi(xo) + rbf(rbf(res[1][1]) * res[0][1]));
      if (lane < 2) *(GAS unsigned*)(out + 16 * G + 4 * qd + 2 * lane) = o;
      if (ai == 0 && lane == 0) *(GAS unsigned*)ws = tag;
      STAMP(10);
    }
  }
}

extern "C" void kernel_launch(void* const* d_in, const int* in_sizes, int n_in, void* d_out, int out_size, void* d_ws, size_t ws_size, hipStream_t stream) {
  (void)hipFuncSetAttribute((const void*)mla_decode, hipFuncAttributeMaxDynamicSharedMemorySize, LDS_BYTES);
  mla_decode<<<256, 512, LDS_BYTES, stream>>>((const u16*)d_in[0], (const u16*)d_in[1], (const u16*)d_in[2], (const u16*)d_in[3], (const u16*)d_in[4], (const u16*)d_in[5],
                                              (const u16*)d_in[7], (const u16*)d_in[8], (const u16*)d_in[9], (u16*)d_out, (unsigned char*)d_ws);
}
```

```cpp
#include <hip/hip_runtime.h>
#include <stdint.h>

typedef unsigned short u16;
typedef unsigned long long u64;
typedef unsigned v4u __attribute__((ext_vector_type(4)));
typedef unsigned v2u __attribute__((ext_vector_type(2)));
typedef unsigned v8u __attribute__((ext_vector_type(8)));
typedef float f4 __attribute__((ext_vector_type(4)));
typedef float f2 __attribute__((ext_vector_type(2)));
typedef __bf16 bf8 __attribute__((ext_vector_type(8)));
typedef __bf16 bf2 __attribute__((ext_vector_type(2)));
typedef short s4 __attribute__((ext_vector_type(4)));
#define GAS __attribute__((address_space(1)))
#define LAS __attribute__((address_space(3)))
#define CAS __attribute__((address_space(4)))

#define DELAY_A 120
#define DELAY_Q 380
#define DELAY_O 40

#define SLEEP_H1 8
#define DELAY_F 220
#define WO_NT 15
#define DELAY_C 0
#define WS_BASE(n) 0
constexpr int WS_FL = 1024;
constexpr int WS_FL2 = 2048;
constexpr int WS_QP = 4096;
constexpr int WS_QA = WS_QP + 131072;
constexpr int WS_FP = WS_QA + 32768;
constexpr int WS_ST = WS_FP + 131072;
constexpr int WS_SP = WS_ST + 262144;
constexpr int WS_YP = WS_SP + 524288;
constexpr int WS_OP = WS_YP + 524288;
constexpr int WS_END = WS_OP + 4194304;
constexpr int WS_STAMP = 6 << 20;
constexpr int WS_TUNE = 64;
constexpr int LDS_BYTES = 163840;
constexpr int CODE_BYTES = 13824;
constexpr int SPIN_CAP = 1 << 16;
constexpr int AUX_SC1 = 16, AUX_NT = 2;
constexpr int QAI = 131072;
constexpr unsigned EMPTY = 0xFFFFFFFFu;

#define BAR() asm volatile("s_waitcnt lgkmcnt(0)\n\ts_barrier" ::: "memory")
#define FENCE() asm volatile("" ::: "memory")
#define SCHED() __builtin_amdgcn_sched_barrier(0)
#define VMWAIT(n) asm volatile("s_waitcnt vmcnt(" #n ")" ::: "memory")
#define PIN(x) asm volatile("" : "+v"(x))
#define PINS(x) asm volatile("" : "+s"(x))

#define STAMP(i) do {} while (0)

typedef __amdgpu_buffer_rsrc_t rsrc_t;
__device__ __forceinline__ float bflo(unsigned u) { return __uint_as_float(u << 16); }
__device__ __forceinline__ float bfhi(unsigned u) { return __uint_as_float(u & 0xFFFF0000u); }
__device__ __forceinline__ unsigned pk(float a, float b) { f2 f = {a, b}; return __builtin_bit_cast(unsigned, __builtin_convertvector(f, bf2)); }
__device__ __forceinline__ unsigned pkc(float a, float b) { return min(pk(a, b), EMPTY - 1u); }
__device__ __forceinline__ float rbf(float a) { return bflo(pk(a, a)); }
__device__ __forceinline__ float asf(unsigned u) { return __uint_as_float(u); }
__device__ __forceinline__ unsigned asu(float f) { return __float_as_uint(f); }
__device__ __forceinline__ float ex2(float x) { return __builtin_amdgcn_exp2f(x); }
__device__ __forceinline__ float rcp(float x) { return __builtin_amdgcn_rcpf(x); }
__device__ __forceinline__ f4 mfma(v4u a, v4u b, f4 c) {
  return __builtin_amdgcn_mfma_f32_16x16x32_bf16(__builtin_bit_cast(bf8, a), __builtin_bit_cast(bf8, b), c, 0, 0, 0);
}
template <int CTRL> __device__ __forceinline__ float dpp(float v) {
  return __builtin_bit_cast(float, __builtin_amdgcn_update_dpp(0, __builtin_bit_cast(int, v), CTRL, 0xf, 0xf, true));
}
template <int CTRL> __device__ __forceinline__ unsigned dppu(unsigned v) { return (unsigned)__builtin_amdgcn_update_dpp(0, (int)v, CTRL, 0xf, 0xf, true); }
__device__ __forceinline__ float add16(float v) { unsigned u = asu(v); auto r = __builtin_amdgcn_permlane16_swap(u, u, false, false); return asf(r[0]) + asf(r[1]); }
__device__ __forceinline__ float add32(float v) { unsigned u = asu(v); auto r = __builtin_amdgcn_permlane32_swap(u, u, false, false); return asf(r[0]) + asf(r[1]); }
__device__ __forceinline__ float fmx(float a, float b) { return __builtin_fmaxf(a, b); }
__device__ __forceinline__ float mx3(float a, float b, float c) { return fmx(fmx(a, b), c); }
__device__ __forceinline__ float max16(float v) { unsigned u = asu(v); auto r = __builtin_amdgcn_permlane16_swap(u, u, false, false); return fmx(asf(r[0]), asf(r[1])); }
__device__ __forceinline__ float max32(float v) { unsigned u = asu(v); auto r = __builtin_amdgcn_permlane32_swap(u, u, false, false); return fmx(asf(r[0]), asf(r[1])); }
__device__ __forceinline__ float wave_sum(float v) {
  v += dpp<0x128>(v); v += dpp<0x124>(v); v += dpp<0x122>(v); v += dpp<0x121>(v);
  return add32(add16(v));
}
__device__ __forceinline__ unsigned umax4(unsigned acc, v4u v) { return max(max(max(acc, v.x), v.y), max(v.z, v.w)); }
__device__ __forceinline__ rsrc_t mkrs(const void* p, int bytes) { return __builtin_amdgcn_make_buffer_rsrc((void*)p, 0, bytes, 0x00020000); }
__device__ __forceinline__ v4u mksrd(const void* p, unsigned bytes) { const u64 a = (u64)p; return v4u{(unsigned)a, (unsigned)(a >> 32) & 0xFFFFu, bytes, 0x00020000u}; }
__device__ __forceinline__ v4u ldnt(rsrc_t r, int voff, int soff) { return __builtin_amdgcn_raw_buffer_load_b128(r, voff, soff, AUX_NT); }
__device__ __forceinline__ v4u ldsc(rsrc_t r, int voff, int soff) { return __builtin_amdgcn_raw_buffer_load_b128(r, voff, soff, AUX_SC1); }
__device__ __forceinline__ void stsc(v4u v, rsrc_t r, int voff, int soff) { __builtin_amdgcn_raw_buffer_store_b128(v, r, voff, soff, AUX_SC1); }
__device__ __forceinline__ void dma16(v4u srd, int voff, int soff, int lds_dst) {
  asm volatile("s_mov_b32 m0, %3\n\ts_nop 0\n\tbuffer_load_dwordx4 %0, %1, %2 offen nt lds" :: "v"(voff), "s"(srd), "s"(soff), "s"(lds_dst) : "memory");
}
__device__ __forceinline__ void dma16p(v4u srd, int voff, int soff, int lds_dst) {
  asm volatile("s_mov_b32 m0, %3\n\ts_nop 0\n\tbuffer_load_dwordx4 %0, %1, %2 offen lds" :: "v"(voff), "s"(srd), "s"(soff), "s"(lds_dst) : "memory");
}
__device__ __forceinline__ void dma16sc(v4u srd, int voff, int soff, int lds_dst) {
  asm volatile("s_mov_b32 m0, %3\n\ts_nop 0\n\tbuffer_load_dwordx4 %0, %1, %2 offen sc1 lds" :: "v"(voff), "s"(srd), "s"(soff), "s"(lds_dst) : "memory");
}
template <typename T> __device__ __forceinline__ LAS T* lp(int off) { return (LAS T*)(uintptr_t)(unsigned)off; }
__device__ __forceinline__ v4u lds128(int off) { return *lp<v4u>(off); }
__device__ __forceinline__ v2u ldstr(int off) { return __builtin_bit_cast(v2u, __builtin_amdgcn_ds_read_tr16_b64_v4i16(lp<s4>(off))); }
__device__ __forceinline__ int swz(int r) { return ((r & 7) << 1) | ((r >> 3) & 1); }
__device__ __forceinline__ v4u xmask(unsigned xx, const unsigned (&mk)[4]) { return v4u{xx & mk[0], xx & mk[1], xx & mk[2], xx & mk[3]}; }
__device__ __forceinline__ bool all_ok(bool ok) { return __builtin_amdgcn_ballot_w64(!ok) == 0ull; }
__device__ __forceinline__ void wait_until(u64 t) { while ((long long)(__builtin_amdgcn_s_memrealtime() - t) < 0) __builtin_amdgcn_s_sleep(1); }

__device__ __forceinline__ void layernorm4(v4u xa, v2u x4, unsigned (&xx)[4]) {
  float v[8] = {bflo(xa.x), bfhi(xa.x), bflo(xa.y), bfhi(xa.y), bflo(xa.z), bfhi(xa.z), bflo(xa.w), bfhi(xa.w)};
  const float mean = wave_sum(((v[0] + v[1]) + (v[2] + v[3])) + ((v[4] + v[5]) + (v[6] + v[7]))) * (1.0f / 512.0f);
  float q0 = 0.f, q1 = 0.f;
#pragma unroll
  for (int i = 0; i < 4; ++i) { const float d = v[i] - mean, e = v[i + 4] - mean; q0 += d * d; q1 += e * e; }
  const float var = wave_sum(q0 + q1) * (1.0f / 512.0f);
  const float mb = rbf(mean);
  const float ri = rcp(rbf(__builtin_amdgcn_sqrtf(rbf(rbf(var) + 1e-5f))));
  const unsigned d01 = pk(bflo(x4.x) - mb, bfhi(x4.x) - mb), d23 = pk(bflo(x4.y) - mb, bfhi(x4.y) - mb);
  const float n0 = bflo(d01) * ri, n1 = bfhi(d01) * ri, n2 = bflo(d23) * ri, n3 = bfhi(d23) * ri;
  xx[0] = pk(n0, n0); xx[1] = pk(n1, n1); xx[2] = pk(n2, n2); xx[3] = pk(n3, n3);
}

__device__ __forceinline__ void ln_stats(v4u xa, float& mb, float& ri) {
  float v[8] = {bflo(xa.x), bfhi(xa.x), bflo(xa.y), bfhi(xa.y), bflo(xa.z), bfhi(xa.z), bflo(xa.w), bfhi(xa.w)};
  const float mean = wave_sum(((v[0] + v[1]) + (v[2] + v[3])) + ((v[4] + v[5]) + (v[6] + v[7]))) * (1.0f / 512.0f);
  float q0 = 0.f, q1 = 0.f;
#pragma unroll
  for (int i = 0; i < 4; ++i) { const float d = v[i] - mean, e = v[i + 4] - mean; q0 += d * d; q1 += e * e; }
  const float var = wave_sum(q0 + q1) * (1.0f / 512.0f);
  mb = rbf(mean);
  ri = rcp(rbf(__builtin_amdgcn_sqrtf(rbf(rbf(var) + 1e-5f))));
}

__device__ __forceinline__ v4u layernorm8(v4u xa) {
  float mb, ri;
  ln_stats(xa, mb, ri);
  v4u o;
#pragma unroll
  for (int i = 0; i < 4; ++i) { const unsigned d = pk(bflo(xa[i]) - mb, bfhi(xa[i]) - mb); o[i] = pk(bflo(d) * ri, bfhi(d) * ri); }
  return o;
}

template <bool ASMW> __device__ __forceinline__ void gemv4_publish(int red_off, v4u (&wv)[4], const unsigned (&xx)[4], const unsigned (&mk)[4],
                                                                   rsrc_t rs, int dst_off, unsigned tag, int w, int lane) {
  v4u xm[4];
#pragma unroll
  for (int i = 0; i < 4; ++i) { xm[i] = xmask(xx[i], mk); PIN(xm[i]); }
  int wa = red_off + w * 512 + (lane & 15) * 32 + (lane & 16), ra = red_off + lane * 8, so = lane * 16;
  unsigned tv = tag;
  PIN(wa); PIN(ra); PIN(so); PIN(tv); PINS(dst_off);
  SCHED();
  if (ASMW) asm volatile("s_waitcnt vmcnt(4)" : "+v"(wv[0]), "+v"(wv[1]), "+v"(wv[2]), "+v"(wv[3]) :: "memory");
  f4 acc = {0.f, 0.f, 0.f, 0.f};
#pragma unroll
  for (int i = 0; i < 4; ++i) acc = mfma(xm[i], wv[i], acc);
  if (lane < 32) *lp<f4>(wa) = acc;
  BAR();
  if (w == 0) {
    f2 p[8];
#pragma unroll
    for (int i = 0; i < 8; ++i) p[i] = *lp<f2>(ra + i * 512);
    const f2 s = ((p[0] + p[1]) + (p[2] + p[3])) + ((p[4] + p[5]) + (p[6] + p[7]));
    stsc(v4u{asu(s.x), tv, asu(s.y), tv}, rs, so, dst_off);
  }
}

__global__ void __launch_bounds__(512, 2) mla_decode(const u16* x, const u16* wq, unsigned char* ws, const u16* wuk, const u16* ckv, const u16* wuv, const u16* wo,
                                                      const u16* wg, const u16* wu, const u16* wd, u16* out) {
  const u64 t_start = __builtin_amdgcn_s_memrealtime();
#define DLY_A DELAY_A
#define DLY_Q DELAY_Q
#define DLY_O DELAY_O
#define DLY_F DELAY_F
  const int b = blockIdx.x, tid = threadIdx.x, lane = tid & 63, w = __builtin_amdgcn_readfirstlane(tid >> 6);
  const int xcd = b & 7, rr = (b >> 3) & 15;
  const bool isA = b >= 128;
  const int r16 = lane & 15, g = lane >> 4;
  const int h = xcd * 4 + (rr & 3), j = rr >> 2;
  const int ai = rr * 8 + xcd;
  {
    u64 pc; asm volatile("s_getpc_b64 %0" : "=s"(pc));
    asm volatile("s_mov_b32 m0, %2\n\ts_nop 0\n\tglobal_load_lds_dword %0, %1" :: "v"(min(((b >> 3) * 8 + w) * 64, CODE_BYTES - 64) + r16 * 4), "s"(pc & ~255ull), "s"(QAI + 16384) : "memory");
  }
#define COMMON_CONSTS \
  const int dchunk = (r16 ^ swz(4 * (w & 3) + g)) * 16;     \
  const int trrow = 4 * g + (r16 >> 2); \
  const int trbase = trrow * 256 + ((((r16 >> 1) & 1) ^ swz(trrow)) * 16) + 8 * (r16 & 1);     \
  const int rbase = r16 * 256 + ((4 * g) ^ swz(r16)) * 16;                                      \
  const int trbaseK = trbase + trrow * 768, rbaseK = rbase + r16 * 768;                         \
  const int vrow0 = g * 256 + (r16 ^ (2 * w)) * 16, vrow1 = vrow0 ^ 16;                         \
  unsigned mk[4]; \
  _Pragma("unroll") for (int k = 0; k < 4; ++k) mk[k] = ((r16 >> 1) == k) ? ((r16 & 1) ? 0xFFFF0000u : 0x0000FFFFu) : 0u;

  if (!isA) {
    const int cons = h * 4 + j, xrow0 = 128 * j + 16 * w;
    v4u xa, wqv[4];
    {
      int o0 = (xrow0 + 4 * g) * 8192 + h * 256 + r16 * 16, o1 = o0 + 8192, o2 = o0 + 16384, o3 = o0 + 24576, ox = lane * 16, oc = WS_QA + cons * 256 + lane * 4;
      unsigned em = EMPTY;
      PIN(o0); PIN(o1); PIN(o2); PIN(o3); PIN(ox); PIN(oc); PIN(em);
      SCHED();
      if (w == 0) {
        asm volatile("global_load_dwordx4 %0, %1, %2\n\tglobal_store_dword %3, %4, %5 sc1" : "=&v"(xa) : "v"(ox), "s"(x), "v"(oc), "v"(em), "s"(ws) : "memory");
      }
      asm volatile("global_load_dwordx4 %0, %4, %8 nt\n\tglobal_load_dwordx4 %1, %5, %8 nt\n\tglobal_load_dwordx4 %2, %6, %8 nt\n\tglobal_load_dwordx4 %3, %7, %8 nt\n\ts_barrier"
                   : "=&v"(wqv[0]), "=&v"(wqv[1]), "=&v"(wqv[2]), "=&v"(wqv[3]) : "v"(o0), "v"(o1), "v"(o2), "v"(o3), "s"(wq) : "memory");
    }
    STAMP(9);
    unsigned tag = *(const CAS unsigned*)ws;
    v8u xs = *(const CAS v8u*)(x + xrow0);
    SCHED();
    {
      const v4u sk = mksrd(wuk, 4194304);
#pragma unroll
      for (int n = 0; n < 4; ++n) dma16(sk, g * 8192 + (r16 ^ swz(4 * n + g)) * 16, (xrow0 + 4 * n) * 8192 + h * 256, 16384 + w * 4096 + n * 1024);
    }
    SCHED();
    const rsrc_t rs = mkrs(ws, WS_END);
    COMMON_CONSTS
    unsigned mk2[4];
#pragma unroll
    for (int k = 0; k < 4; ++k) { mk2[k] = mk[k] | dppu<0x128>(mk[k]); PIN(mk[k]); PIN(mk2[k]); }
    int a_w0 = w * 1024 + (g >> 1) * 512 + r16 * 32 + (lane & 16), po = lane * 16, a_qp = ((lane & 31) * 2 + (lane >> 5)) * 16, a_wk = 16384 + w * 4096 + rbase,
        a_qw = 8192 + lane * 4, a_qr = 8192 + g * 64, a_qa = g * 8;
    PIN(a_w0); PIN(a_qp); PIN(po); PIN(a_wk); PIN(a_qw); PIN(a_qr); PIN(a_qa);
    PINS(tag); PINS(xs);
    tag += 1u;
    unsigned tv = tag;
    v4u xm[4];
    {
      unsigned xlo = (g & 1) ? ((g & 2) ? xs[6] : xs[2]) : ((g & 2) ? xs[4] : xs[0]), xhi = (g & 1) ? ((g & 2) ? xs[7] : xs[3]) : ((g & 2) ? xs[5] : xs[1]);
      if (r16 >= 8) { xlo = 0x3F803F80u; xhi = 0x3F803F80u; }
      xm[0] = xmask(__builtin_amdgcn_perm(xlo, xlo, 0x01000100u), mk2); xm[1] = xmask(__builtin_amdgcn_perm(xlo, xlo, 0x03020302u), mk2);
      xm[2] = xmask(__builtin_amdgcn_perm(xhi, xhi, 0x01000100u), mk2); xm[3] = xmask(__builtin_amdgcn_perm(xhi, xhi, 0x03020302u), mk2);
#pragma unroll
      for (int i = 0; i < 4; ++i) PIN(xm[i]);
      PIN(tv);
    }
    float mb = 0.f, ri = 0.f;
    if (w == 0) {
      asm volatile("s_waitcnt vmcnt(8)" : "+v"(xa) :: "memory");
      if (lane == 0) __builtin_amdgcn_raw_buffer_store_b64(v2u{tv, tv}, rs, 0, WS_FL + cons * 8, AUX_SC1);
      STAMP(10);
      ln_stats(xa, mb, ri);
      PIN(mb); PIN(ri);
      VMWAIT(5);
    } else {
      VMWAIT(4);
    }
    SCHED();
    asm volatile("" : "+v"(wqv[0]), "+v"(wqv[1]), "+v"(wqv[2]), "+v"(wqv[3]) :: "memory");
    STAMP(12);
    {
      f4 acc = {0.f, 0.f, 0.f, 0.f};
#pragma unroll
      for (int i = 0; i < 4; ++i) acc = mfma(xm[i], wqv[i], acc);
      *lp<f4>(a_w0) = acc;
    }
    BAR();
    if (w == 0) {
      f4 p[8];
#pragma unroll
      for (int i = 0; i < 8; ++i) p[i] = *lp<f4>(po + i * 1024);
      const f4 s = ((p[0] + p[1]) + (p[2] + p[3])) + ((p[4] + p[5]) + (p[6] + p[7]));
      const auto r0 = __builtin_amdgcn_permlane32_swap(asu(s[0]), asu(s[2]), false, false), r1 = __builtin_amdgcn_permlane32_swap(asu(s[1]), asu(s[3]), false, false);
      stsc(v4u{asu((asf(r0[0]) - mb * asf(r0[1])) * ri), tv, asu((asf(r1[0]) - mb * asf(r1[1])) * ri), tv}, rs, a_qp, WS_QP + cons * 1024);
    }
    STAMP(1);
    if (w == 0) VMWAIT(2); else VMWAIT(0);
    STAMP(13);
    v4u wkv[4];
#pragma unroll
    for (int s = 0; s < 4; ++s) wkv[s] = lds128(a_wk ^ (s * 16));
    if (w == 1) {
      v4u p[4]; int spins = 0; unsigned bad;
      __builtin_amdgcn_s_sleep(SLEEP_H1);
      for (;;) {
        FENCE();
#pragma unroll
        for (int i = 0; i < 4; ++i) p[i] = ldsc(rs, po + i * 1024, WS_QP + h * 4096);
        bad = 0;
#pragma unroll
        for (int i = 0; i < 4; ++i) bad |= (p[i].y ^ tv) | (p[i].w ^ tv);
        PIN(bad);
        if (all_ok(bad == 0) || ++spins >= SPIN_CAP) break;
        __builtin_amdgcn_s_sleep(2);
      }
      const float q0 = (asf(p[0].x) + asf(p[1].x)) + (asf(p[2].x) + asf(p[3].x));
      const float q1 = (asf(p[0].z) + asf(p[1].z)) + (asf(p[2].z) + asf(p[3].z));
      *lp<unsigned>(a_qw) = pk(q0, q1);
    }
    BAR();
    STAMP(2);
    {
      v4u qf[4];
#pragma unroll
      for (int s = 0; s < 4; ++s) qf[s] = lds128(a_qr + s * 16);
      SCHED();
      f4 acc = {0.f, 0.f, 0.f, 0.f};
#pragma unroll
      for (int s = 0; s < 4; ++s) acc = mfma(wkv[s], qf[s], acc);
      const float sc = 0.08838834764831845f * 1.4426950408889634f;
      if (r16 == 0) __builtin_amdgcn_raw_buffer_store_b64(v2u{pkc(acc[0] * sc, acc[1] * sc), pkc(acc[2] * sc, acc[3] * sc)}, rs, a_qa, WS_QA + cons * 256 + w * 32, AUX_SC1);
    }
    STAMP(3);
    BAR();
    STAMP(14);
    wait_until(t_start + DLY_Q);
    v4u buv[4], bo[4][4];
    {
      const v4u suv = mksrd(wuv, 4194304), so = mksrd(wo, 4194304);
      v4u fw[4];
      {
        const GAS char* pf = (const GAS char*)((r16 < 8) ? wg : wu) + (xrow0 + 4 * g + 1) * 3584 + (h < 28 ? h : 0) * 128 + (r16 & 7) * 16;
        asm volatile("global_load_dwordx4 %0, %4, off offset:-3584 nt\n\tglobal_load_dwordx4 %1, %4, off nt\n\tglobal_load_dwordx4 %2, %4, off offset:3584 nt\n\tglobal_load_dwordx4 %3, %5, off nt\n\ts_barrier"
                     : "=&v"(fw[0]), "=&v"(fw[1]), "=&v"(fw[2]), "=&v"(fw[3]) : "v"(pf), "v"(pf + 7168) : "memory");
      }
      const int vo1 = g * 16 * 8192 + dchunk, so1 = (128 * j + 2 * (w & 3) + (w >> 2)) * 8192 + h * 256;
#pragma unroll 1
      for (int n = 0; n < 4; ++n) dma16(suv, vo1, so1 + (64 * (n >> 1) + 8 * (n & 1)) * 8192, QAI + (32 * n + 4 * w) * 256);
      const int so2 = (h * 128 + 8 * (w >> 2) + (w & 3)) * 1024;
      int tq[4];
#pragma unroll
      for (int t = 0; t < 4; ++t) tq[t] = (w >> 1) * 256 + (trbaseK ^ (32 * (4 * (w & 1) + t)));
#pragma unroll
      for (int n = 0; n < 6; ++n) {
        if (n < 4) {
#pragma unroll
          for (int i = 0; i < 4; ++i) { if ((WO_NT >> n) & 1) dma16(so, (i & 1) ? vrow1 : vrow0, so2 + (((n + j) & 3) * 32 + 16 * (i & 1) + 4 * (i >> 1)) * 1024, (32 * n + w + 8 * i) * 1024);
            else dma16p(so, (i & 1) ? vrow1 : vrow0, so2 + (((n + j) & 3) * 32 + 16 * (i & 1) + 4 * (i >> 1)) * 1024, (32 * n + w + 8 * i) * 1024); }
        }
        if (n == 0) {
          asm volatile("s_waitcnt vmcnt(8)" : "+v"(fw[0]), "+v"(fw[1]), "+v"(fw[2]), "+v"(fw[3]) :: "memory");
          f4 acc = {0.f, 0.f, 0.f, 0.f};
#pragma unroll
          for (int i = 0; i < 4; ++i) acc = mfma(xm[i], fw[i], acc);
          *lp<f4>(98304 + a_w0) = acc;
          BAR();
          if (w == 0) {
            f4 p[8];
#pragma unroll
            for (int i = 0; i < 8; ++i) p[i] = *lp<f4>(98304 + po + i * 1024);
            const f4 s = ((p[0] + p[1]) + (p[2] + p[3])) + ((p[4] + p[5]) + (p[6] + p[7]));
            const auto r0 = __builtin_amdgcn_permlane32_swap(asu(s[0]), asu(s[2]), false, false), r1 = __builtin_amdgcn_permlane32_swap(asu(s[1]), asu(s[3]), false, false);
            stsc(v4u{asu((asf(r0[0]) - mb * asf(r0[1])) * ri), tv, asu((asf(r1[0]) - mb * asf(r1[1])) * ri), tv}, rs, a_qp, WS_FP + cons * 1024);
          }
          STAMP(15);
        }
        if (n == 1) {
          asm volatile("s_waitcnt vmcnt(8)\n\ts_barrier" ::: "memory");
#pragma unroll
          for (int ks = 0; ks < 4; ++ks) {
            const int a = QAI + (trbase ^ (32 * w)) + ks * 8192;
            const v2u lo = ldstr(a), hi = ldstr(a + 4096);
            buv[ks] = v4u{lo.x, lo.y, hi.x, hi.y};
          }
        }
        if (n >= 2) {
          if (n < 4) asm volatile("s_waitcnt vmcnt(8)\n\ts_barrier" ::: "memory");
          if (n == 4) asm volatile("s_waitcnt vmcnt(4)\n\ts_barrier" ::: "memory");
          if (n == 5) { asm volatile("s_waitcnt vmcnt(0)\n\ts_barrier" ::: "memory"); STAMP(4); }
#pragma unroll
          for (int t = 0; t < 4; ++t) {
            const v2u lo = ldstr(tq[t] + (n - 2) * 32768), hi = ldstr(tq[t] + (n - 2) * 32768 + 16384);
            bo[t][n - 2] = v4u{lo.x, lo.y, hi.x, hi.y};
          }
        }
      }
    }
    int a_st = (4 * g + (r16 & 3)) * 16, a_od = g * 1024 + r16 * 16, a_w1 = w * 272 + r16 * 16 + (g & 1) * 8, a_r1 = (r16 & 7) * 272 + g * 16,
        a_w2 = 8192 + ((16 * w + r16 - 32 * j) & 127) * 2, a_r2 = 8192 + g * 16, a_o = g * 16384 + r16 * 8;
    PIN(a_st); PIN(a_od); PIN(a_w1); PIN(a_r1); PIN(a_w2); PIN(a_r2); PIN(a_o);
    BAR();
    STAMP(5);
    v4u st; u64 t_st;
    {
      int spins = 0;
      do {
        FENCE();
        st = ldsc(rs, a_st, WS_ST + (cons * 128 + 16 * w) * 16);
      } while (!all_ok((st.y == tv) & (st.w == tv)) && ++spins < SPIN_CAP);
      t_st = __builtin_amdgcn_s_memrealtime() + (spins ? DLY_O : 0);
    }
    STAMP(6);
    {
      const float m = asf(st.x);
      float mw = fmx(m, dpp<0xB1>(m)); mw = fmx(mw, dpp<0x4E>(mw)); mw = max32(max16(mw));
      float lw = ex2(m - mw) * asf(st.z);
      lw += dpp<0xB1>(lw); lw += dpp<0x4E>(lw); lw = add32(add16(lw));
      if (lane == 0) *lp<f2>(4096 + w * 8) = f2{mw, lw};
      BAR();
      const f2 sw = *lp<f2>(4096 + (lane & 7) * 8);
      float M = fmx(sw.x, dpp<0xB1>(sw.x)); M = fmx(M, dpp<0x4E>(M)); M = fmx(M, dpp<0x141>(M));
      float Lt = ex2(sw.x - M) * sw.y;
      Lt += dpp<0xB1>(Lt); Lt += dpp<0x4E>(Lt); Lt += dpp<0x141>(Lt);
      const float e = ex2(m - M) * rcp(Lt);
      const unsigned wb = pk(e, e);
      xm[0] = xmask(dppu<0x00>(wb), mk); xm[1] = xmask(dppu<0x55>(wb), mk); xm[2] = xmask(dppu<0xAA>(wb), mk); xm[3] = xmask(dppu<0xFF>(wb), mk);
#pragma unroll
      for (int i = 0; i < 4; ++i) PIN(xm[i]);
    }
    wait_until(t_st);
    {
      int spins = 0; f4 acc; unsigned bad;
      do {
        FENCE();
        v4u od[4];
#pragma unroll
        for (int i = 0; i < 4; ++i) od[i] = ldsc(rs, a_od + i * 256, WS_OP + (cons * 128 + 16 * w) * 256);
        acc = f4{0.f, 0.f, 0.f, 0.f}; bad = 0;
#pragma unroll
        for (int i = 0; i < 4; ++i) { acc = mfma(xm[i], od[i], acc); bad = umax4(bad, od[i]); }
      } while (!all_ok(bad != EMPTY) && ++spins < SPIN_CAP);
      if (lane < 32) *lp<v2u>(a_w1) = v2u{pk(acc[0], acc[1]), pk(acc[2], acc[3])};
    }
    STAMP(7);
    BAR();
    {
      v4u af[4];
#pragma unroll
      for (int ks = 0; ks < 4; ++ks) af[ks] = lds128(a_r1 + ks * 64);
      SCHED();
      f4 acc = {0.f, 0.f, 0.f, 0.f};
#pragma unroll
      for (int ks = 0; ks < 4; ++ks) acc = mfma(af[ks], buv[ks], acc);
      const float c = add16((acc[0] + acc[1]) + (acc[2] + acc[3]));
      if (lane < 16) *lp<u16>(a_w2) = (u16)pk(c, c);
    }
    BAR();
    {
      v4u cf[4];
#pragma unroll
      for (int ks = 0; ks < 4; ++ks) cf[ks] = lds128(a_r2 + ks * 64);
      SCHED();
      f4 acc[4];
#pragma unroll
      for (int t = 0; t < 4; ++t) acc[t] = f4{0.f, 0.f, 0.f, 0.f};
#pragma unroll
      for (int ks = 0; ks < 4; ++ks)
#pragma unroll
        for (int t = 0; t < 4; ++t) acc[t] = mfma(cf[ks], bo[t][ks], acc[t]);
      const float v01 = (g & 1) ? acc[1][0] : acc[0][0], v23 = (g & 1) ? acc[3][0] : acc[2][0];
      const float v = (g & 2) ? v23 : v01;
      __builtin_amdgcn_raw_buffer_store_b64(v2u{asu(v), tv}, rs, a_o, WS_SP + (4 * w * 128 + cons) * 128, AUX_SC1);
    }
    STAMP(8);
  } else {
    FENCE();
    const v4u sws = mksrd(ws, WS_END);
    int po = lane * 16;
    const int ht = w & 1, kt = w >> 1;
    v4u k0[16], k1[16];
    v2u v0[4][4];
    {
      const rsrc_t rs0 = mkrs(ws, WS_END);
      wait_until(t_start + DELAY_C);
#pragma unroll
      for (int i = 0; i < 4; ++i) stsc(v4u{EMPTY, EMPTY, EMPTY, EMPTY}, rs0, (16 * w + g) * 32768 + r16 * 16, WS_OP + i * 131072 + ai * 256);
    }
    STAMP(11);
    if (w < 2) {
      const v4u skv = mksrd(ckv, 16777216);
      const int so = (128 * ai + w) * 1024, vb = po ^ (w * 32);
      wait_until(t_start + DLY_A);
      STAMP(12);
#pragma unroll 1
      for (int i = 0; i < 8; ++i) {
        const int blk = ((2 * i) & 7) + (i >> 2);
        if (i == 6) asm volatile("s_waitcnt vmcnt(16)\n\ts_barrier" ::: "memory");
#pragma unroll
        for (int r = 0; r < 8; ++r) dma16(skv, vb ^ ((r & 3) * 64 + (r >> 2) * 16), so + blk * 16384 + r * 2048, w * 1024 + blk * 16384 + r * 2048);
      }
      STAMP(1);
    }
    SCHED();
    const rsrc_t rx = mkrs(x, 1024), rs = mkrs(ws, WS_END);
    const unsigned tag = *(const CAS unsigned*)ws + 1u;
    PINS(wd); PINS(out);
    COMMON_CONSTS
    const int mi = ai & 3, fi = (ai < 112) ? (ai >> 2) : 0;
    int ka[4], ta[4], vo0 = vrow0, vo1 = vrow1;
    unsigned tv = tag;
#pragma unroll
    for (int s = 0; s < 4; ++s) { ka[s] = kt * 32768 + (rbaseK ^ (s * 16)); ta[s] = (w >> 1) * 256 + (trbaseK ^ (32 * (4 * (w & 1) + s))); PIN(ka[s]); PIN(ta[s]); }
    PIN(vo0); PIN(vo1); PIN(tv);
    v4u fl;
#define RD_U0() _Pragma("unroll") for (int i = 0; i < 4; ++i) _Pragma("unroll") for (int s = 0; s < 4; ++s) { k0[4 * i + s] = lds128(ka[s] + i * 256); v0[i][s] = ldstr(ta[s] + i * 32768); }
#define RD_K1() _Pragma("unroll") for (int s = 0; s < 16; ++s) k1[s] = lds128(ka[s & 3] + (s >> 2) * 256 + 16384)
    if (w >= 2) {
      asm volatile("s_waitcnt vmcnt(0)\n\ts_barrier" ::: "memory");
      if (w == 7) {
        __builtin_amdgcn_s_setprio(3);
        asm volatile("buffer_load_dwordx4 %0, %1, %2, %3 offen sc1" : "=v"(fl) : "v"(po), "s"(sws), "s"(WS_FL) : "memory");
        __builtin_amdgcn_s_setprio(0);
      }
      RD_U0()
      if (w == 7) {
        asm volatile("s_waitcnt vmcnt(0)" : "+v"(fl) :: "memory");
        int spins = 0;
        while (!all_ok(((fl.x ^ tv) | (fl.y ^ tv) | (fl.z ^ tv) | (fl.w ^ tv)) == 0) && ++spins < SPIN_CAP)
          asm volatile("buffer_load_dwordx4 %0, %1, %2, %3 offen sc1\n\ts_waitcnt vmcnt(0)" : "=&v"(fl) : "v"(po), "s"(sws), "s"(WS_FL) : "memory");
      }
    }
    asm volatile("s_waitcnt vmcnt(16)\n\ts_barrier" ::: "memory");
    STAMP(2);
#define QA4() _Pragma("unroll") for (int q = 0; q < 4; ++q) dma16sc(sws, (q & 1) ? vo1 : vo0, WS_QA + (w + 8 * q) * 1024, QAI + (w + 8 * q) * 1024)
    QA4();
    if (w < 2) { RD_U0() }
    if (kt < 2) { RD_K1(); }
    int a_d = (2 * g + (r16 >> 3)) * 1024 + (r16 & 7) * 16, a_mw = (16 * ht + r16) * 16 + kt * 4,
        a_s = (lane < 16) ? ((16 * ht + r16) * 4 + kt) * 2048 : WS_END, a_o = (16 * g + (w >> 1)) * 32768 + (w & 1) * 128 + r16 * 8, a_pq = QAI + po;
    int qa_[4];
#pragma unroll
    for (int s = 0; s < 4; ++s) { qa_[s] = ka[s] + (QAI + ht * 16384 - kt * 32768); PIN(qa_[s]); }
    PIN(a_d); PIN(a_mw); PIN(a_s); PIN(a_o); PIN(a_pq);
    VMWAIT(4);
    BAR();
    STAMP(3);
    if (kt >= 2) { RD_K1(); }
    {
      for (int spins = 0;; ++spins) {
        VMWAIT(0);
        unsigned bad = 0;
#pragma unroll
        for (int q = 0; q < 4; ++q) bad = umax4(bad, lds128(a_pq + w * 1024 + q * 8192));
        if (all_ok(bad != EMPTY) || spins >= SPIN_CAP) break;
        QA4();
      }
    }
    BAR();
    STAMP(4);
    f4 S0 = {0.f, 0.f, 0.f, 0.f}, S1 = {0.f, 0.f, 0.f, 0.f};
    v4u vf[4][4];
    {
      v4u qf[16];
#pragma unroll
      for (int s = 0; s < 4; ++s) qf[s] = lds128(qa_[s]);
      SCHED();
#pragma unroll
      for (int s = 0; s < 16; ++s) {
        if (s < 12) qf[s + 4] = lds128(qa_[s & 3] + ((s + 4) >> 2) * 256);
        { const v2u hi = ldstr(ta[s & 3] + (s >> 2) * 32768 + 16384); vf[s >> 2][s & 3] = v4u{v0[s >> 2][s & 3].x, v0[s >> 2][s & 3].y, hi.x, hi.y}; }
        S0 = mfma(k0[s], qf[s], S0); S1 = mfma(k1[s], qf[s], S1);
        SCHED();
      }
      float m0 = mx3(mx3(mx3(S0[0], S0[1], S0[2]), S0[3], S1[0]), S1[1], S1[2]);
      m0 = max32(max16(fmx(m0, S1[3])));
      if (lane < 16) *lp<float>(a_mw) = m0;
    }
    BAR();
    STAMP(5);
    float M;
    {
      const f4 m4 = *lp<f4>(a_mw & ~15);
      M = fmx(mx3(m4[0], m4[1], m4[2]), m4[3]);
      float p[8];
#pragma unroll
      for (int i = 0; i < 4; ++i) { p[i] = ex2(S0[i] - M); p[4 + i] = ex2(S1[i] - M); }
      *lp<v4u>(a_pq + w * 1024) = v4u{pk(p[0], p[1]), pk(p[2], p[3]), pk(p[4], p[5]), pk(p[6], p[7])};
      const float l = add32(add16(((p[0] + p[1]) + (p[2] + p[3])) + ((p[4] + p[5]) + (p[6] + p[7]))));
      if (lane < 16) *lp<float>(512 + a_mw) = l;
    }
    BAR();
    STAMP(6);
    {
      v4u pf_[8];
      pf_[0] = lds128(a_pq);
      const f4 l4 = *lp<f4>(512 + (a_mw & ~15));
#pragma unroll
      for (int i = 1; i < 8; ++i) pf_[i] = lds128(a_pq + ((i & 3) * 2 + (i >> 2)) * 1024);
      SCHED();
#pragma unroll
      for (int hh = 0; hh < 2; ++hh) {
        f4 O[4];
#pragma unroll
        for (int t = 0; t < 4; ++t) O[t] = f4{0.f, 0.f, 0.f, 0.f};
#pragma unroll
        for (int ks = 0; ks < 4; ++ks) {
#pragma unroll
          for (int t = 0; t < 4; ++t) O[t] = mfma(pf_[4 * hh + ks], vf[ks][t], O[t]);
          if (hh == 0 && ks == 0) { SCHED(); stsc(v4u{asu(M), tv, asu((l4[0] + l4[1]) + (l4[2] + l4[3])), tv}, rs, a_s, WS_ST + ai * 16); SCHED(); }
        }
        if (hh == 1) { PIN(O[3]); STAMP(13); }
#pragma unroll
        for (int i = 0; i < 4; ++i)
          __builtin_amdgcn_raw_buffer_store_b64(v2u{pkc(O[0][i], O[1][i]), pkc(O[2][i], O[3][i])}, rs, a_o, WS_OP + (16 * hh + i) * 131072 + ai * 256, AUX_SC1);
      }
    }
    STAMP(7);
    const u64 t_pv = __builtin_amdgcn_s_memrealtime();
    if (ai < 112) {
      const rsrc_t rd = mkrs(wd, 1835008);
      v4u dw[2];
#pragma unroll
      for (int i = 0; i < 2; ++i) dw[i] = ldnt(rd, a_d, (64 * fi + 16 * mi + 8 * i) * 1024 + w * 128);
      STAMP(14);
      v4u p; int spins = 0;
      const int gu = (r16 >> 2) & 1, hi = r16 >> 3;
      const int po2 = (r16 & 3) * 1024 + (64 * gu + 8 * hi + 2 * g) * 8;
      do {
        FENCE();
        p = ldsc(rs, po2, WS_FP + fi * 4096 + mi * 128);
      } while (!all_ok((p.y == tv) & (p.w == tv)) && ++spins < SPIN_CAP);
      float v0_ = asf(p.x), v1_ = asf(p.z);
      v0_ += dpp<0xB1>(v0_); v1_ += dpp<0xB1>(v1_);
      v0_ += dpp<0x4E>(v0_); v1_ += dpp<0x4E>(v1_);
      const float o0 = dpp<0x141>(v0_), o1 = dpp<0x141>(v1_);
      float hv[2];
#pragma unroll
      for (int e = 0; e < 2; ++e) {
        const float gt = rbf(gu ? (e ? o1 : o0) : (e ? v1_ : v0_)), up = rbf(gu ? (e ? v1_ : v0_) : (e ? o1 : o0));
        const float z2 = 2.0f * 0.7978845608028654f * 1.4426950408889634f * (gt + 0.044715f * gt * gt * gt);
        hv[e] = rbf(gt * rcp(1.0f + ex2(-z2))) * up;
      }
      const float t0 = dpp<0x128>(hv[0]), t1 = dpp<0x128>(hv[1]);
      unsigned mk2[4];
#pragma unroll
      for (int k = 0; k < 4; ++k) mk2[k] = mk[k] | dppu<0x128>(mk[k]);
      const float ha = hi ? t1 : hv[0], hb = hi ? hv[1] : t0;
      f4 acc = {0.f, 0.f, 0.f, 0.f};
      acc = mfma(xmask(pk(ha, ha), mk2), dw[0], acc);
      acc = mfma(xmask(pk(hb, hb), mk2), dw[1], acc);
      float y[4];
#pragma unroll
      for (int i = 0; i < 4; ++i) {
        const unsigned u = asu(dpp<0x128>(acc[i]));
        auto r = __builtin_amdgcn_permlane32_swap(u, u, false, false);
        y[i] = acc[i] + asf(r[1]);
      }
      if (r16 < 8 && g < 2) {
        const int off = (r16 >> 1) * 16384 + ((r16 & 1) * 8 + 4 * g) * 8;
        stsc(v4u{asu(y[0]), tv, asu(y[1]), tv}, rs, off, WS_YP + (4 * w * 128 + ai) * 128);
        stsc(v4u{asu(y[2]), tv, asu(y[3]), tv}, rs, off + 16, WS_YP + (4 * w * 128 + ai) * 128);
      }
    }
    STAMP(8);
    if (w == 7) {
      const int off = (lane >> 1) * 128 + (lane & 1) * 16, so = h * 16384 + j * 32;
      const unsigned xo = __builtin_amdgcn_raw_buffer_load_b32(rx, (lane & 1) * 4, h * 32 + j * 8, 0);
      v4u p[8]; int spins = 0; unsigned bad;
      wait_until(t_pv + DLY_F);
      do {
        FENCE();
#pragma unroll
        for (int n = 0; n < 8; ++n) p[n] = ldsc(rs, off, (n < 4 ? WS_YP : WS_SP) + so + (n & 3) * 4096);
        if (lane >= 32) p[3] = v4u{0u, tv, 0u, tv};
        bad = 0;
#pragma unroll
        for (int n = 0; n < 8; ++n) bad |= (p[n].y ^ tv) | (p[n].w ^ tv);
        PIN(bad);
      } while (!all_ok(bad == 0) && ++spins < SPIN_CAP);
      STAMP(9);
      float a[4] = {(asf(p[0].x) + asf(p[1].x)) + (asf(p[2].x) + asf(p[3].x)), (asf(p[0].z) + asf(p[1].z)) + (asf(p[2].z) + asf(p[3].z)),
                    (asf(p[4].x) + asf(p[5].x)) + (asf(p[6].x) + asf(p[7].x)), (asf(p[4].z) + asf(p[5].z)) + (asf(p[6].z) + asf(p[7].z))};
#pragma unroll
      for (int i = 0; i < 4; ++i) { a[i] += dpp<0x128>(a[i]); a[i] += dpp<0x124>(a[i]); a[i] += dpp<0x122>(a[i]); a[i] = rbf(add32(add16(a[i]))); }
      const float g0 = rbf(a[0] * rcp(1.0f + ex2(-1.4426950408889634f * a[0]))), g1 = rbf(a[1] * rcp(1.0f + ex2(-1.4426950408889634f * a[1])));
      const unsigned o = pk(bflo(xo) + rbf(a[2] * g0), bfhi(xo) + rbf(a[3] * g1));
      if (lane < 2) *(GAS unsigned*)(out + 16 * h + 4 * j + 2 * lane) = o;
      if (ai == 0 && lane == 0) *(GAS unsigned*)ws = tag;
      STAMP(10);
    }
  }
}

extern "C" void kernel_launch(void* const* d_in, const int* in_sizes, int n_in, void* d_out, int out_size, void* d_ws, size_t ws_size, hipStream_t stream) {
  (void)hipFuncSetAttribute((const void*)mla_decode, hipFuncAttributeMaxDynamicSharedMemorySize, LDS_BYTES);
  mla_decode<<<256, 512, LDS_BYTES, stream>>>((const u16*)d_in[0], (const u16*)d_in[2], (unsigned char*)d_ws + WS_BASE(ws_size), (const u16*)d_in[3], (const u16*)d_in[1], (const u16*)d_in[4], (const u16*)d_in[5],
                                              (const u16*)d_in[7], (const u16*)d_in[8], (const u16*)d_in[9], (u16*)d_out);
}
```

```cpp
#include <hip/hip_runtime.h>
#include <stdint.h>

typedef unsigned short u16;
typedef unsigned long long u64;
typedef unsigned v4u __attribute__((ext_vector_type(4)));
typedef unsigned v2u __attribute__((ext_vector_type(2)));
typedef unsigned v8u __attribute__((ext_vector_type(8)));
typedef unsigned v16u __attribute__((ext_vector_type(16)));
typedef float f4 __attribute__((ext_vector_type(4)));
typedef float f2 __attribute__((ext_vector_type(2)));
typedef __bf16 bf8 __attribute__((ext_vector_type(8)));
typedef __bf16 bf2 __attribute__((ext_vector_type(2)));
typedef short s4 __attribute__((ext_vector_type(4)));
#define GAS __attribute__((address_space(1)))
#define LAS __attribute__((address_space(3)))
#define CAS __attribute__((address_space(4)))

#define DELAY_A 0
#define DELAY_Q 300
#define DELAY_O 50

#define SLEEP_H1 8
#define DELAY_F 200
#define WO_NT 0
#define WS_BASE(n) 0
#define DELAY_G 360
#define DELAY_U 0
#define DELAY_H 210
#define ROLE_A_FIRST 1
#define CLEAR_MODE 0
#define FFN_MODE 0
#define DELAY_C 0
constexpr int WS_FL = 1024;
constexpr int WS_FL2 = 2048;
constexpr int WS_QP = 4096;
constexpr int WS_QA = WS_QP + 131072;
constexpr int WS_FP = WS_QA + 32768;
constexpr int WS_ST = WS_FP + 131072;
constexpr int WS_SP = WS_ST + 262144;
constexpr int WS_YP = WS_SP + 524288;
constexpr int WS_OP = WS_YP + 524288;
constexpr int WS_END = WS_OP + 4194304;
constexpr int WS_STAMP = 6 << 20;
constexpr int WS_TUNE = 64;
constexpr int LDS_BYTES = 163840;
constexpr int CODE_BYTES = 16128;
constexpr int SPIN_CAP = 1 << 16;
constexpr int AUX_SC1 = 16, AUX_NT = 2;
constexpr int QAI = 131072;
constexpr unsigned EMPTY = 0xFFFFFFFFu;

#define BAR() asm volatile("s_waitcnt lgkmcnt(0)\n\ts_barrier" ::: "memory")
#define FENCE() asm volatile("" ::: "memory")
#define SCHED() __builtin_amdgcn_sched_barrier(0)
#define VMWAIT(n) asm volatile("s_waitcnt vmcnt(" #n ")" ::: "memory")
#define PIN(x) asm volatile("" : "+v"(x))
#define PINS(x) asm volatile("" : "+s"(x))

#define STAMP(i) do {} while (0)
#define STAMP_OUT()

typedef __amdgpu_buffer_rsrc_t rsrc_t;
__device__ __forceinline__ float bflo(unsigned u) { return __uint_as_float(u << 16); }
__device__ __forceinline__ float bfhi(unsigned u) { return __uint_as_float(u & 0xFFFF0000u); }
__device__ __forceinline__ unsigned pk(float a, float b) { f2 f = {a, b}; return __builtin_bit_cast(unsigned, __builtin_convertvector(f, bf2)); }
__device__ __forceinline__ unsigned pkc(float a, float b) { return min(pk(a, b), EMPTY - 1u); }
__device__ __forceinline__ float rbf(float a) { return bflo(pk(a, a)); }
__device__ __forceinline__ float asf(unsigned u) { return __uint_as_float(u); }
__device__ __forceinline__ unsigned asu(float f) { return __float_as_uint(f); }
__device__ __forceinline__ float ex2(float x) { return __builtin_amdgcn_exp2f(x); }
__device__ __forceinline__ float rcp(float x) { return __builtin_amdgcn_rcpf(x); }
__device__ __forceinline__ f4 mfma(v4u a, v4u b, f4 c) {
  return __builtin_amdgcn_mfma_f32_16x16x32_bf16(__builtin_bit_cast(bf8, a), __builtin_bit_cast(bf8, b), c, 0, 0, 0);
}
template <int CTRL> __device__ __forceinline__ float dpp(float v) {
  return __builtin_bit_cast(float, __builtin_amdgcn_update_dpp(0, __builtin_bit_cast(int, v), CTRL, 0xf, 0xf, true));
}
template <int CTRL> __device__ __forceinline__ unsigned dppu(unsigned v) { return (unsigned)__builtin_amdgcn_update_dpp(0, (int)v, CTRL, 0xf, 0xf, true); }
__device__ __forceinline__ float add16(float v) { unsigned u = asu(v); auto r = __builtin_amdgcn_permlane16_swap(u, u, false, false); return asf(r[0]) + asf(r[1]); }
__device__ __forceinline__ float add32(float v) { unsigned u = asu(v); auto r = __builtin_amdgcn_permlane32_swap(u, u, false, false); return asf(r[0]) + asf(r[1]); }
__device__ __forceinline__ float fmx(float a, float b) { return __builtin_fmaxf(a, b); }
__device__ __forceinline__ float mx3(float a, float b, float c) { return fmx(fmx(a, b), c); }
__device__ __forceinline__ float max16(float v) { unsigned u = asu(v); auto r = __builtin_amdgcn_permlane16_swap(u, u, false, false); return fmx(asf(r[0]), asf(r[1])); }
__device__ __forceinline__ float max32(float v) { unsigned u = asu(v); auto r = __builtin_amdgcn_permlane32_swap(u, u, false, false); return fmx(asf(r[0]), asf(r[1])); }
__device__ __forceinline__ float wave_sum(float v) {
  v += dpp<0x128>(v); v += dpp<0x124>(v); v += dpp<0x122>(v); v += dpp<0x121>(v);
  return add32(add16(v));
}
__device__ __forceinline__ unsigned umax4(unsigned acc, v4u v) { return max(max(max(acc, v.x), v.y), max(v.z, v.w)); }
__device__ __forceinline__ rsrc_t mkrs(const void* p, int bytes) { return __builtin_amdgcn_make_buffer_rsrc((void*)p, 0, bytes, 0x00020000); }
__device__ __forceinline__ v4u mksrd(const void* p, unsigned bytes) { const u64 a = (u64)p; return v4u{(unsigned)a, (unsigned)(a >> 32) & 0xFFFFu, bytes, 0x00020000u}; }
__device__ __forceinline__ v4u ldnt(rsrc_t r, int voff, int soff) { return __builtin_amdgcn_raw_buffer_load_b128(r, voff, soff, AUX_NT); }
__device__ __forceinline__ v4u ldsc(rsrc_t r, int voff, int soff) { return __builtin_amdgcn_raw_buffer_load_b128(r, voff, soff, AUX_SC1); }
__device__ __forceinline__ void stsc(v4u v, rsrc_t r, int voff, int soff) { __builtin_amdgcn_raw_buffer_store_b128(v, r, voff, soff, AUX_SC1); }
__device__ __forceinline__ void dma16(v4u srd, int voff, int soff, int lds_dst) {
  asm volatile("s_mov_b32 m0, %3\n\ts_nop 0\n\tbuffer_load_dwordx4 %0, %1, %2 offen nt lds" :: "v"(voff), "s"(srd), "s"(soff), "s"(lds_dst) : "memory");
}
__device__ __forceinline__ void dma16p(v4u srd, int voff, int soff, int lds_dst) {
  asm volatile("s_mov_b32 m0, %3\n\ts_nop 0\n\tbuffer_load_dwordx4 %0, %1, %2 offen lds" :: "v"(voff), "s"(srd), "s"(soff), "s"(lds_dst) : "memory");
}
__device__ __forceinline__ void dma16sc(v4u srd, int voff, int soff, int lds_dst) {
  asm volatile("s_mov_b32 m0, %3\n\ts_nop 0\n\tbuffer_load_dwordx4 %0, %1, %2 offen sc1 lds" :: "v"(voff), "s"(srd), "s"(soff), "s"(lds_dst) : "memory");
}
template <typename T> __device__ __forceinline__ LAS T* lp(int off) { return (LAS T*)(uintptr_t)(unsigned)off; }
__device__ __forceinline__ v4u lds128(int off) { return *lp<v4u>(off); }
__device__ __forceinline__ v2u ldstr(int off) { return __builtin_bit_cast(v2u, __builtin_amdgcn_ds_read_tr16_b64_v4i16(lp<s4>(off))); }
__device__ __forceinline__ int swz(int r) { return ((r & 7) << 1) | ((r >> 3) & 1); }
__device__ __forceinline__ v4u xmask(unsigned xx, const unsigned (&mk)[4]) { return v4u{xx & mk[0], xx & mk[1], xx & mk[2], xx & mk[3]}; }
__device__ __forceinline__ bool all_ok(bool ok) { return __builtin_amdgcn_ballot_w64(!ok) == 0ull; }
__device__ __forceinline__ void wait_until(u64 t) { while ((long long)(__builtin_amdgcn_s_memrealtime() - t) < 0) __builtin_amdgcn_s_sleep(1); }

__device__ __forceinline__ void ln_stats(v4u xa, float& mb, float& ri) {
  float v[8] = {bflo(xa.x), bfhi(xa.x), bflo(xa.y), bfhi(xa.y), bflo(xa.z), bfhi(xa.z), bflo(xa.w), bfhi(xa.w)};
  const float mean = wave_sum(((v[0] + v[1]) + (v[2] + v[3])) + ((v[4] + v[5]) + (v[6] + v[7]))) * (1.0f / 512.0f);
  float q0 = 0.f, q1 = 0.f;
#pragma unroll
  for (int i = 0; i < 4; ++i) { const float d = v[i] - mean, e = v[i + 4] - mean; q0 += d * d; q1 += e * e; }
  const float var = wave_sum(q0 + q1) * (1.0f / 512.0f);
  mb = rbf(mean);
  ri = rcp(rbf(__builtin_amdgcn_sqrtf(rbf(rbf(var) + 1e-5f))));
}

__device__ __forceinline__ v4u layernorm8(v4u xa) {
  float mb, ri;
  ln_stats(xa, mb, ri);
  v4u o;
#pragma unroll
  for (int i = 0; i < 4; ++i) { const unsigned d = pk(bflo(xa[i]) - mb, bfhi(xa[i]) - mb); o[i] = pk(bflo(d) * ri, bfhi(d) * ri); }
  return o;
}

typedef u64 v8q __attribute__((ext_vector_type(8)));
typedef u64 v4q __attribute__((ext_vector_type(4)));
__global__ void __launch_bounds__(512, 2) mla_decode(const void*, const void*, void*, const void*, const void*, const void*, const void*, const void*,
                                                      const void*, const void*, void*, const void*) {
  v8q ka0; v4q ka1; u64 t_start;
  asm volatile("s_load_dwordx16 %0, %3, 0x0\n\ts_load_dwordx8 %1, %3, 0x40\n\ts_memrealtime %2" : "=&s"(ka0), "=&s"(ka1), "=&s"(t_start) : "s"(__builtin_amdgcn_kernarg_segment_ptr()));
  const int b = blockIdx.x, tid = threadIdx.x, lane = tid & 63, w = __builtin_amdgcn_readfirstlane(tid >> 6);
  const int xcd = b & 7, rr = (b >> 3) & 15;
  const bool isA = b < 128;
  const int r16 = lane & 15, g = lane >> 4;
  const int h = xcd * 4 + (rr & 3), j = rr >> 2;
  const int ai = rr * 8 + xcd;
  {
    u64 pc; asm volatile("s_getpc_b64 %0" : "=s"(pc));
    asm volatile("s_mov_b32 m0, %2\n\ts_nop 0\n\tglobal_load_lds_dword %0, %1" :: "v"(min(((b >> 3) * 8 + w) * 128, CODE_BYTES - 128) + r16 * 8), "s"(pc & ~255ull), "s"(114688) : "memory");
  }
#define COMMON_CONSTS \
  const int dchunk = (r16 ^ swz(4 * (w & 3) + g)) * 16;     \
  const int trrow = 4 * g + (r16 >> 2); \
  const int trbase = trrow * 256 + ((((r16 >> 1) & 1) ^ swz(trrow)) * 16) + 8 * (r16 & 1);     \
  const int rbase = r16 * 256 + ((4 * g) ^ swz(r16)) * 16;                                      \
  const int trbaseK = trbase + trrow * 768, rbaseK = rbase + r16 * 768;                         \
  const int vrow0 = g * 256 + (r16 ^ (2 * w)) * 16, vrow1 = vrow0 ^ 16;                         \
  unsigned mk[4]; \
  _Pragma("unroll") for (int k = 0; k < 4; ++k) mk[k] = ((r16 >> 1) == k) ? ((r16 & 1) ? 0xFFFF0000u : 0x0000FFFFu) : 0u;

  const int cons = h * 4 + j;
  v4u wqv[8], xa, xb;
  int oA = (128 * j + 32 * (w & 3) + 8 * g) * 8192 + h * 256 + r16 * 16, oq[4], ox = (w == 2) ? lane * 16 : (128 * j + 32 * (w & 3) + 8 * g) * 2, oc = WS_QA + cons * 256 + lane * 4;
  unsigned em = EMPTY;
  {
#pragma unroll
    for (int n = 0; n < 4; ++n) { oq[n] = (128 * j + 16 * w + 4 * n + g) * 8192 + h * 256 + (r16 ^ swz(4 * n + g)) * 16; PIN(oq[n]); }
    PIN(oA); PIN(ox); PIN(oc); PIN(em);
    SCHED();
    asm volatile("s_waitcnt lgkmcnt(0)" : "+s"(ka0), "+s"(ka1), "+s"(t_start));
  }
  const u16 *x = (const u16*)ka0[0], *wq = (const u16*)ka0[1], *wuk = (const u16*)ka0[3], *ckv = (const u16*)ka0[4], *wuv = (const u16*)ka0[5], *wo = (const u16*)ka0[6],
            *wg = (const u16*)ka0[7], *wu = (const u16*)ka1[0], *wd = (const u16*)ka1[1];
  unsigned char* ws = (unsigned char*)ka0[2];
  u16* out = (u16*)ka1[2];
  {
    int b2 = b;
    PINS(b2);
    if (b2 >= 128) {
      if (w == 0) asm volatile("global_store_dword %0, %1, %2 sc1" :: "v"(oc), "v"(em), "s"(ws) : "memory");
#pragma unroll
      for (int n = 0; n < 4; ++n)
        asm volatile("s_mov_b32 m0, %2\n\ts_nop 0\n\tglobal_load_lds_dwordx4 %0, %1 nt" :: "v"(oq[n]), "s"(wuk), "s"(QAI + w * 4096 + n * 1024) : "memory");
    } else {
    if (w == 2) asm volatile("global_load_dwordx4 %0, %1, %2" : "=&v"(xa) : "v"(ox), "s"(x) : "memory");
    if (w >= 4) asm volatile("global_load_dwordx4 %0, %1, %2" : "=&v"(xb) : "v"(ox), "s"(x) : "memory");
    if (w >= 4) {
      const v4u sq = mksrd(wq, 4194304);
      asm volatile("buffer_load_dwordx4 %0, %8, %9, 0 offen nt\n\tbuffer_load_dwordx4 %1, %8, %9, %10 offen nt\n\tbuffer_load_dwordx4 %2, %8, %9, %11 offen nt\n\tbuffer_load_dwordx4 %3, %8, %9, %12 offen nt\n\t"
                   "buffer_load_dwordx4 %4, %8, %9, %13 offen nt\n\tbuffer_load_dwordx4 %5, %8, %9, %14 offen nt\n\tbuffer_load_dwordx4 %6, %8, %9, %15 offen nt\n\tbuffer_load_dwordx4 %7, %8, %9, %16 offen nt"
                   : "=&v"(wqv[0]), "=&v"(wqv[1]), "=&v"(wqv[2]), "=&v"(wqv[3]), "=&v"(wqv[4]), "=&v"(wqv[5]), "=&v"(wqv[6]), "=&v"(wqv[7])
                   : "v"(oA), "s"(sq), "s"(8192), "s"(16384), "s"(24576), "s"(32768), "s"(40960), "s"(49152), "s"(57344) : "memory");
    }
    asm volatile("s_barrier" ::: "memory");
    }
  }
#define DLY_G DELAY_G
#define DLY_H DELAY_H
#define DLY_U DELAY_U
#define DLY_A DELAY_A
#define DLY_Q DELAY_Q
#define DLY_O DELAY_O
#define DLY_F DELAY_F
  if (!isA) {
    STAMP(9);
    const int xrow0 = 128 * j + 16 * w;
    const unsigned tag = *(const CAS unsigned*)ws + 1u;
    const rsrc_t rs = mkrs(ws, WS_END);
    COMMON_CONSTS
    int po = lane * 16, a_wk = QAI + w * 4096 + rbase, a_qw = 73728 + lane * 4, a_qr = 73728 + g * 64, a_qa = 74240 + w * 32 + g * 8;
    unsigned tv = tag;
#pragma unroll
    for (int k = 0; k < 4; ++k) PIN(mk[k]);
    PIN(po); PIN(a_wk); PIN(a_qw); PIN(a_qr); PIN(a_qa); PIN(tv);
    if (w == 0) {
      VMWAIT(4);
      if (lane == 0) __builtin_amdgcn_raw_buffer_store_b64(v2u{tv, tv}, rs, 0, WS_FL + cons * 8, AUX_SC1);
      STAMP(10);
    }
    if (w == 2) *lp<v4u>(65536 + po) = layernorm8(*(const GAS v4u*)(x + 8 * lane));
    VMWAIT(0);
    BAR();
    STAMP(13);
    v4u wkv[4], xm[4];
#pragma unroll
    for (int s = 0; s < 4; ++s) wkv[s] = lds128(a_wk ^ (s * 16));
    {
      const v2u x4 = *lp<v2u>(65536 + (xrow0 + 4 * g) * 2);
      xm[0] = xmask(__builtin_amdgcn_perm(x4.x, x4.x, 0x01000100u), mk); xm[1] = xmask(__builtin_amdgcn_perm(x4.x, x4.x, 0x03020302u), mk);
      xm[2] = xmask(__builtin_amdgcn_perm(x4.y, x4.y, 0x01000100u), mk); xm[3] = xmask(__builtin_amdgcn_perm(x4.y, x4.y, 0x03020302u), mk);
#pragma unroll
      for (int i = 0; i < 4; ++i) { PIN(xm[i]); PIN(wkv[i]); }
    }
    if (w == 1) {
      v4u p[4]; int spins = 0; unsigned bad;
      wait_until(t_start + DLY_H);
      for (;;) {
        FENCE();
#pragma unroll
        for (int i = 0; i < 4; ++i) p[i] = ldsc(rs, po + i * 1024, WS_QP + h * 4096);
        bad = 0;
#pragma unroll
        for (int i = 0; i < 4; ++i) bad |= (p[i].y ^ tv) | (p[i].w ^ tv);
        PIN(bad);
        if (all_ok(bad == 0) || ++spins >= SPIN_CAP) break;
        __builtin_amdgcn_s_sleep(1);
      }
      const float q0 = (asf(p[0].x) + asf(p[1].x)) + (asf(p[2].x) + asf(p[3].x));
      const float q1 = (asf(p[0].z) + asf(p[1].z)) + (asf(p[2].z) + asf(p[3].z));
      *lp<unsigned>(a_qw) = pk(q0, q1);
    }
    BAR();
    STAMP(2);
    {
      v4u qf[4];
#pragma unroll
      for (int s = 0; s < 4; ++s) qf[s] = lds128(a_qr + s * 16);
      SCHED();
      f4 acc = {0.f, 0.f, 0.f, 0.f};
#pragma unroll
      for (int s = 0; s < 4; ++s) acc = mfma(wkv[s], qf[s], acc);
      const float sc = 0.08838834764831845f * 1.4426950408889634f;
      if (r16 == 0) *lp<v2u>(a_qa) = v2u{pkc(acc[0] * sc, acc[1] * sc), pkc(acc[2] * sc, acc[3] * sc)};
    }
    BAR();
    if (w == 0 && lane < 16) stsc(lds128(74240 + po), rs, po, WS_QA + cons * 256);
    STAMP(3);
    BAR();
    wait_until(t_start + DLY_Q);
    v4u buv[4], bo[4][4];
    {
      const v4u suv = mksrd(wuv, 4194304), so = mksrd(wo, 4194304);
      const int so2 = (h * 128 + 8 * (w >> 2) + (w & 3)) * 1024;
      int tq[4];
#pragma unroll
      for (int t = 0; t < 4; ++t) tq[t] = (w >> 1) * 256 + (trbaseK ^ (32 * (4 * (w & 1) + t)));
#define WO4(n) _Pragma("unroll") for (int i = 0; i < 4; ++i) dma16p(so, (i & 1) ? vrow1 : vrow0, so2 + ((((n) + j) & 3) * 32 + 16 * (i & 1) + 4 * (i >> 1)) * 1024, (32 * (n) + w + 8 * i) * 1024)
#define RD_BO(n) _Pragma("unroll") for (int t = 0; t < 4; ++t) { const v2u lo = ldstr(tq[t] + (n) * 32768), hi = ldstr(tq[t] + (n) * 32768 + 16384); bo[t][n] = v4u{lo.x, lo.y, hi.x, hi.y}; }
      WO4(0);
      asm volatile("s_barrier" ::: "memory");
      WO4(1);
      asm volatile("s_barrier" ::: "memory");
      WO4(2);
      asm volatile("s_waitcnt vmcnt(8)\n\ts_barrier" ::: "memory");
      WO4(3);
      RD_BO(0)
      asm volatile("s_waitcnt vmcnt(8)\n\ts_barrier" ::: "memory");
      STAMP(14);
      wait_until(t_start + DLY_U);
      v4u fw[4];
      {
        const GAS char* pf = (const GAS char*)((r16 < 8) ? wg : wu) + (xrow0 + 4 * g + 1) * 3584 + (h < 28 ? h : 0) * 128 + (r16 & 7) * 16;
        asm volatile("global_load_dwordx4 %0, %4, off offset:-3584 nt\n\tglobal_load_dwordx4 %1, %4, off nt\n\tglobal_load_dwordx4 %2, %4, off offset:3584 nt\n\tglobal_load_dwordx4 %3, %5, off nt"
                     : "=&v"(fw[0]), "=&v"(fw[1]), "=&v"(fw[2]), "=&v"(fw[3]) : "v"(pf), "v"(pf + 7168) : "memory");
      }
      RD_BO(1)
      asm volatile("s_waitcnt vmcnt(4) lgkmcnt(0)\n\ts_barrier" ::: "memory");
      const int vo1 = g * 16 * 8192 + dchunk, so1 = (128 * j + 2 * (w & 3) + (w >> 2)) * 8192 + h * 256;
#pragma unroll 1
      for (int n = 0; n < 4; ++n) dma16(suv, vo1, so1 + (64 * (n >> 1) + 8 * (n & 1)) * 8192, QAI + (32 * n + 4 * w) * 256);
      RD_BO(2)
      RD_BO(3)
      asm volatile("s_waitcnt vmcnt(4)" : "+v"(fw[0]), "+v"(fw[1]), "+v"(fw[2]), "+v"(fw[3]) :: "memory");
      f4 acc = {0.f, 0.f, 0.f, 0.f};
#pragma unroll
      for (int i = 0; i < 4; ++i) acc = mfma(xm[i], fw[i], acc);
      if (lane < 32) *lp<f4>(32768 + w * 512 + r16 * 32 + (lane & 16)) = acc;
      BAR();
      if (w == 0) {
        f2 p[8];
#pragma unroll
        for (int i = 0; i < 8; ++i) p[i] = *lp<f2>(32768 + lane * 8 + i * 512);
        const f2 s = ((p[0] + p[1]) + (p[2] + p[3])) + ((p[4] + p[5]) + (p[6] + p[7]));
        stsc(v4u{asu(s.x), tv, asu(s.y), tv}, rs, po, WS_FP + cons * 1024);
        STAMP(15);
        VMWAIT(1);
      } else {
        VMWAIT(0);
      }
      asm volatile("s_barrier" ::: "memory");
      STAMP(4);
#pragma unroll
      for (int ks = 0; ks < 4; ++ks) {
        const int a = QAI + (trbase ^ (32 * w)) + ks * 8192;
        const v2u lo = ldstr(a), hi = ldstr(a + 4096);
        buv[ks] = v4u{lo.x, lo.y, hi.x, hi.y};
      }
    }
    int a_st = (4 * g + (r16 & 3)) * 512, a_od = g * 1024 + r16 * 16, a_w1 = w * 272 + r16 * 16 + (g & 1) * 8, a_r1 = (r16 & 7) * 272 + g * 16,
        a_w2 = 8192 + ((16 * w + r16 - 32 * j) & 127) * 2, a_r2 = 8192 + g * 16, a_o = g * 16384 + r16 * 8;
    PIN(a_st); PIN(a_od); PIN(a_w1); PIN(a_r1); PIN(a_w2); PIN(a_r2); PIN(a_o);
    STAMP(5);
    v4u st, od[4];
    {
      int spins = 0;
      do {
        FENCE();
        st = ldsc(rs, a_st, WS_ST + w * 8192 + h * 16);
      } while (!all_ok((st.y == tv) & (st.w == tv)) && ++spins < SPIN_CAP);
      const u64 t_st = __builtin_amdgcn_s_memrealtime() + (spins ? DLY_O : 0);
      STAMP(6);
      const float m = asf(st.x);
      float mw = fmx(m, dpp<0xB1>(m)); mw = fmx(mw, dpp<0x4E>(mw)); mw = max32(max16(mw));
      float lw = ex2(m - mw) * asf(st.z);
      lw += dpp<0xB1>(lw); lw += dpp<0x4E>(lw); lw = add32(add16(lw));
      if (lane == 0) *lp<f2>(4096 + w * 8) = f2{mw, lw};
      wait_until(t_st);
      FENCE();
#pragma unroll
      for (int i = 0; i < 4; ++i) od[i] = ldsc(rs, a_od + i * 256, WS_OP + (cons * 128 + 16 * w) * 256);
      BAR();
      const f2 sw = *lp<f2>(4096 + (lane & 7) * 8);
      float M = fmx(sw.x, dpp<0xB1>(sw.x)); M = fmx(M, dpp<0x4E>(M)); M = fmx(M, dpp<0x141>(M));
      float Lt = ex2(sw.x - M) * sw.y;
      Lt += dpp<0xB1>(Lt); Lt += dpp<0x4E>(Lt); Lt += dpp<0x141>(Lt);
      const float e = ex2(m - M) * rcp(Lt);
      const unsigned wb = pk(e, e);
      xm[0] = xmask(dppu<0x00>(wb), mk); xm[1] = xmask(dppu<0x55>(wb), mk); xm[2] = xmask(dppu<0xAA>(wb), mk); xm[3] = xmask(dppu<0xFF>(wb), mk);
#pragma unroll
      for (int i = 0; i < 4; ++i) PIN(xm[i]);
      SCHED();
    }
    {
      int spins = 0; f4 acc;
      for (;;) {
        unsigned bad = 0;
        acc = f4{0.f, 0.f, 0.f, 0.f};
#pragma unroll
        for (int i = 0; i < 4; ++i) { acc = mfma(xm[i], od[i], acc); bad = umax4(bad, od[i]); }
        if (all_ok(bad != EMPTY) || ++spins >= SPIN_CAP) break;
        FENCE();
#pragma unroll
        for (int i = 0; i < 4; ++i) od[i] = ldsc(rs, a_od + i * 256, WS_OP + (cons * 128 + 16 * w) * 256);
      }
      if (lane < 32) *lp<v2u>(a_w1) = v2u{pk(acc[0], acc[1]), pk(acc[2], acc[3])};
    }
    STAMP(7);
    BAR();
    {
      v4u af[4];
#pragma unroll
      for (int ks = 0; ks < 4; ++ks) af[ks] = lds128(a_r1 + ks * 64);
      SCHED();
      f4 acc = {0.f, 0.f, 0.f, 0.f};
#pragma unroll
      for (int ks = 0; ks < 4; ++ks) acc = mfma(af[ks], buv[ks], acc);
      const float c = add16((acc[0] + acc[1]) + (acc[2] + acc[3]));
      if (lane < 16) *lp<u16>(a_w2) = (u16)pk(c, c);
    }
    BAR();
    {
      v4u cf[4];
#pragma unroll
      for (int ks = 0; ks < 4; ++ks) cf[ks] = lds128(a_r2 + ks * 64);
      SCHED();
      f4 acc[4];
#pragma unroll
      for (int t = 0; t < 4; ++t) acc[t] = f4{0.f, 0.f, 0.f, 0.f};
#pragma unroll
      for (int ks = 0; ks < 4; ++ks)
#pragma unroll
        for (int t = 0; t < 4; ++t) acc[t] = mfma(cf[ks], bo[t][ks], acc[t]);
      const float v01 = (g & 1) ? acc[1][0] : acc[0][0], v23 = (g & 1) ? acc[3][0] : acc[2][0];
      const float v = (g & 2) ? v23 : v01;
      __builtin_amdgcn_raw_buffer_store_b64(v2u{asu(v), tv}, rs, a_o, WS_SP + (4 * w * 128 + cons) * 128, AUX_SC1);
    }
    STAMP(8);
    STAMP_OUT();
    __builtin_amdgcn_endpgm();
  }
  {
    unsigned tagA;
    asm volatile("s_load_dword %0, %1, 0x0" : "=&s"(tagA) : "s"(ws));
    const v4u sws = mksrd(ws, WS_END);
    int po = lane * 16;
    const int ht = w & 1, kt = w >> 1;
    v4u k0[16], k1[16];
    v2u v0[4][4];
    if (w < 2) {
      const v4u skv = mksrd(ckv, 16777216);
      const int so = (128 * ai + w) * 1024, vb = po ^ (w * 32);
      for (int i = 0; i < (int)(DLY_A); ++i) __builtin_amdgcn_s_sleep(1);
#pragma unroll 1
      for (int i = 0; i < 8; ++i) {
        const int blk = ((2 * i) & 7) + (i >> 2);
        if (i == 2) asm volatile("s_barrier" ::: "memory");
        if (i == 6) asm volatile("s_waitcnt vmcnt(16)\n\ts_barrier" ::: "memory");
#pragma unroll
        for (int r = 0; r < 8; ++r) dma16(skv, vb ^ ((r & 3) * 64 + (r >> 2) * 16), so + blk * 16384 + r * 2048, w * 1024 + blk * 16384 + r * 2048);
      }
      STAMP(15);
    }
    SCHED();
    const rsrc_t rx = mkrs(x, 1024), rs = mkrs(ws, WS_END);
    COMMON_CONSTS
#pragma unroll
    for (int k = 0; k < 4; ++k) PIN(mk[k]);
    asm volatile("s_waitcnt lgkmcnt(0)" : "+s"(tagA));
    const unsigned tag = tagA + 1u;
    unsigned tv = tag;
    PIN(tv);
    v4u xn = {0u, 0u, 0u, 0u};
    if (w >= 4) {
      unsigned mk2[4];
      v4u xm[8];
#pragma unroll
      for (int k = 0; k < 4; ++k) { mk2[k] = mk[k] | dppu<0x128>(mk[k]); PIN(mk2[k]); }
      int a_w0 = QAI + (w - 4) * 1024 + (g >> 1) * 512 + r16 * 32 + (lane & 16);
      PIN(a_w0);
      asm volatile("s_waitcnt vmcnt(8)" : "+v"(xb) :: "memory");
#pragma unroll
      for (int k = 0; k < 4; ++k) {
        const unsigned xp = (r16 >= 8) ? 0x3F803F80u : xb[k];
        xm[2 * k] = xmask(__builtin_amdgcn_perm(xp, xp, 0x01000100u), mk2); xm[2 * k + 1] = xmask(__builtin_amdgcn_perm(xp, xp, 0x03020302u), mk2);
        PIN(xm[2 * k]); PIN(xm[2 * k + 1]);
      }
      SCHED();
      f4 acc = {0.f, 0.f, 0.f, 0.f};
      asm volatile("s_waitcnt vmcnt(2)" : "+v"(wqv[0]), "+v"(wqv[1]), "+v"(wqv[2]), "+v"(wqv[3]), "+v"(wqv[4]), "+v"(wqv[5]) :: "memory");
#pragma unroll
      for (int i = 0; i < 6; ++i) acc = mfma(xm[i], wqv[i], acc);
      SCHED();
      asm volatile("s_waitcnt vmcnt(0)" : "+v"(wqv[6]), "+v"(wqv[7]) :: "memory");
      acc = mfma(xm[6], wqv[6], acc); acc = mfma(xm[7], wqv[7], acc);
      STAMP(12);
      *lp<f4>(a_w0) = acc;
      BAR();
    } else if (w == 2) {
      __builtin_amdgcn_s_setprio(3);
      float mb, ri;
      asm volatile("s_waitcnt vmcnt(0)" : "+v"(xa) :: "memory");
      ln_stats(xa, mb, ri);
      int a_qp = ((lane & 31) * 2 + (lane >> 5)) * 16, a_r0 = QAI + po;
      PIN(mb); PIN(ri); PIN(a_qp); PIN(a_r0);
      BAR();
      f4 p[4];
#pragma unroll
      for (int i = 0; i < 4; ++i) p[i] = *lp<f4>(a_r0 + i * 1024);
      const f4 s = (p[0] + p[1]) + (p[2] + p[3]);
      const auto r0 = __builtin_amdgcn_permlane32_swap(asu(s[0]), asu(s[2]), false, false), r1 = __builtin_amdgcn_permlane32_swap(asu(s[1]), asu(s[3]), false, false);
      stsc(v4u{asu((asf(r0[0]) - mb * asf(r0[1])) * ri), tv, asu((asf(r1[0]) - mb * asf(r1[1])) * ri), tv}, rs, a_qp, WS_QP + cons * 1024);
      STAMP(1);
      __builtin_amdgcn_s_setprio(0);
    } else if (w == 3) {
      BAR();
    }
    const int mi = ai & 3, fi = (ai < 112) ? (ai >> 2) : 0;
    int ka[4], ta[4], qa_[4], vo0 = vrow0, vo1 = vrow1;
#pragma unroll
    for (int s = 0; s < 4; ++s) {
      ka[s] = kt * 32768 + (rbaseK ^ (s * 16)); ta[s] = (w >> 1) * 256 + (trbaseK ^ (32 * (4 * (w & 1) + s))); qa_[s] = ka[s] + (QAI + ht * 16384 - kt * 32768);
      PIN(ka[s]); PIN(ta[s]); PIN(qa_[s]);
    }
    int a_d = (2 * g + (r16 >> 3)) * 1024 + (r16 & 7) * 16, a_mw = (16 * ht + r16) * 16 + kt * 4,
        a_s = (lane < 16) ? (16 * ht + r16) * 16 : WS_END, a_o = (16 * g + (w >> 1)) * 32768 + (w & 1) * 128 + r16 * 8, a_pq = QAI + po,
        a_c = (16 * w + g) * 32768 + r16 * 16;
    PIN(a_d); PIN(a_mw); PIN(a_s); PIN(a_o); PIN(a_pq); PIN(a_c); PIN(vo0); PIN(vo1);
#define RD_U0() _Pragma("unroll") for (int i = 0; i < 4; ++i) _Pragma("unroll") for (int s = 0; s < 4; ++s) { k0[4 * i + s] = lds128(ka[s] + i * 256); v0[i][s] = ldstr(ta[s] + i * 32768); }
#define RD_K1() _Pragma("unroll") for (int s = 0; s < 16; ++s) k1[s] = lds128(ka[s & 3] + (s >> 2) * 256 + 16384)
#define FLAGS_OK() all_ok(((fl.x ^ tv) | (fl.y ^ tv) | (fl.z ^ tv) | (fl.w ^ tv)) == 0)
#define QA4() _Pragma("unroll") for (int q = 0; q < 4; ++q) dma16sc(sws, (q & 1) ? vo1 : vo0, WS_QA + (w + 8 * q) * 1024, QAI + (w + 8 * q) * 1024)
    if (w >= 2) {
      v4u fl;
      if (w == 3) {
        __builtin_amdgcn_s_setprio(3);
        asm volatile("s_waitcnt vmcnt(0)\n\tbuffer_load_dwordx4 %0, %1, %2, %3 offen sc1" : "=&v"(fl) : "v"(po), "s"(sws), "s"(WS_FL) : "memory");
      }
      asm volatile("s_barrier" ::: "memory");
      RD_U0()
      if (w == 3) {
        asm volatile("s_waitcnt vmcnt(0)" : "+v"(fl) :: "memory");
        int spins = 0;
        while (!FLAGS_OK() && ++spins < SPIN_CAP)
          asm volatile("buffer_load_dwordx4 %0, %1, %2, %3 offen sc1\n\ts_waitcnt vmcnt(0)" : "=&v"(fl) : "v"(po), "s"(sws), "s"(WS_FL) : "memory");
        __builtin_amdgcn_s_setprio(0);
      }
    }
    asm volatile("s_waitcnt vmcnt(16)\n\ts_barrier" ::: "memory");
    STAMP(2);
    const bool early = (long long)(__builtin_amdgcn_s_memrealtime() - (t_start + DLY_G)) >= 0;
    if (early) { QA4(); }
    if (w < 2) { RD_U0() }
    if (kt < 2) { RD_K1(); }
    if (!early) { wait_until(t_start + DLY_G); QA4(); }
    VMWAIT(4);
    BAR();
    STAMP(3);
    if (kt >= 2) { RD_K1(); }
    for (int spins = 0;; ++spins) {
      VMWAIT(0);
      unsigned bad = 0;
#pragma unroll
      for (int q = 0; q < 4; ++q) bad = umax4(bad, lds128(a_pq + w * 1024 + q * 8192));
      if (all_ok(bad != EMPTY) || spins >= SPIN_CAP) break;
      QA4();
    }
    BAR();
    STAMP(4);
    f4 S0 = {0.f, 0.f, 0.f, 0.f}, S1 = {0.f, 0.f, 0.f, 0.f};
    v4u vf[4][4];
    {
      v4u qf[16];
#pragma unroll
      for (int s = 0; s < 4; ++s) qf[s] = lds128(qa_[s]);
      SCHED();
#pragma unroll
      for (int s = 0; s < 16; ++s) {
        if (s < 12) qf[s + 4] = lds128(qa_[s & 3] + ((s + 4) >> 2) * 256);
        { const v2u hi = ldstr(ta[s & 3] + (s >> 2) * 32768 + 16384); vf[s >> 2][s & 3] = v4u{v0[s >> 2][s & 3].x, v0[s >> 2][s & 3].y, hi.x, hi.y}; }
        if (s < 8 && !(s & 1)) stsc(v4u{EMPTY, EMPTY, EMPTY, EMPTY}, rs, a_c, WS_OP + (s >> 1) * 131072 + ai * 256);
        S0 = mfma(k0[s], qf[s], S0); S1 = mfma(k1[s], qf[s], S1);
        SCHED();
      }
      float m0 = mx3(mx3(mx3(S0[0], S0[1], S0[2]), S0[3], S1[0]), S1[1], S1[2]);
      m0 = max32(max16(fmx(m0, S1[3])));
      if (lane < 16) *lp<float>(a_mw) = m0;
    }
    BAR();
    STAMP(5);
    float M;
    {
      const f4 m4 = *lp<f4>(a_mw & ~15);
      M = fmx(mx3(m4[0], m4[1], m4[2]), m4[3]);
      float p[8];
#pragma unroll
      for (int i = 0; i < 4; ++i) { p[i] = ex2(S0[i] - M); p[4 + i] = ex2(S1[i] - M); }
      *lp<v4u>(a_pq + w * 1024) = v4u{pk(p[0], p[1]), pk(p[2], p[3]), pk(p[4], p[5]), pk(p[6], p[7])};
      const float l = add32(add16(((p[0] + p[1]) + (p[2] + p[3])) + ((p[4] + p[5]) + (p[6] + p[7]))));
      if (lane < 16) *lp<float>(512 + a_mw) = l;
    }
    VMWAIT(0);
    BAR();
    STAMP(6);
    {
      v4u pf_[8];
      pf_[0] = lds128(a_pq);
      const f4 l4 = *lp<f4>(512 + (a_mw & ~15));
#pragma unroll
      for (int i = 1; i < 8; ++i) pf_[i] = lds128(a_pq + ((i & 3) * 2 + (i >> 2)) * 1024);
      SCHED();
#pragma unroll
      for (int hh = 0; hh < 2; ++hh) {
        f4 O[4];
#pragma unroll
        for (int t = 0; t < 4; ++t) O[t] = f4{0.f, 0.f, 0.f, 0.f};
#pragma unroll
        for (int ks = 0; ks < 4; ++ks) {
#pragma unroll
          for (int t = 0; t < 4; ++t) O[t] = mfma(pf_[4 * hh + ks], vf[ks][t], O[t]);
          if (hh == 0 && ks == 0) { SCHED(); if (kt == 0) stsc(v4u{asu(M), tv, asu((l4[0] + l4[1]) + (l4[2] + l4[3])), tv}, rs, a_s, WS_ST + ai * 512); SCHED(); }
        }
        if (hh == 1) { PIN(O[3]); STAMP(13); }
#pragma unroll
        for (int i = 0; i < 4; ++i)
          __builtin_amdgcn_raw_buffer_store_b64(v2u{pkc(O[0][i], O[1][i]), pkc(O[2][i], O[3][i])}, rs, a_o, WS_OP + (16 * hh + i) * 131072 + ai * 256, AUX_SC1);
      }
    }
    STAMP(7);
    const u64 t_pv = __builtin_amdgcn_s_memrealtime();
    if (ai < 112) {
      const rsrc_t rd = mkrs(wd, 1835008);
      v4u dw[2];
#pragma unroll
      for (int i = 0; i < 2; ++i) dw[i] = ldnt(rd, a_d, (64 * fi + 16 * mi + 8 * i) * 1024 + w * 128);
      STAMP(14);
      v4u p; int spins = 0;
      const int gu = (r16 >> 2) & 1, hi = r16 >> 3;
      const int po2 = (r16 & 3) * 1024 + (64 * gu + 8 * hi + 2 * g) * 8;
      do {
        FENCE();
        p = ldsc(rs, po2, WS_FP + fi * 4096 + mi * 128);
      } while (!all_ok((p.y == tv) & (p.w == tv)) && ++spins < SPIN_CAP);
      float v0_ = asf(p.x), v1_ = asf(p.z);
      v0_ += dpp<0xB1>(v0_); v1_ += dpp<0xB1>(v1_);
      v0_ += dpp<0x4E>(v0_); v1_ += dpp<0x4E>(v1_);
      const float o0 = dpp<0x141>(v0_), o1 = dpp<0x141>(v1_);
      float hv[2];
#pragma unroll
      for (int e = 0; e < 2; ++e) {
        const float gt = rbf(gu ? (e ? o1 : o0) : (e ? v1_ : v0_)), up = rbf(gu ? (e ? v1_ : v0_) : (e ? o1 : o0));
        const float z2 = 2.0f * 0.7978845608028654f * 1.4426950408889634f * (gt + 0.044715f * gt * gt * gt);
        hv[e] = rbf(gt * rcp(1.0f + ex2(-z2))) * up;
      }
      const float t0 = dpp<0x128>(hv[0]), t1 = dpp<0x128>(hv[1]);
      unsigned mk2[4];
#pragma unroll
      for (int k = 0; k < 4; ++k) mk2[k] = mk[k] | dppu<0x128>(mk[k]);
      const float ha = hi ? t1 : hv[0], hb = hi ? hv[1] : t0;
      f4 acc = {0.f, 0.f, 0.f, 0.f};
      acc = mfma(xmask(pk(ha, ha), mk2), dw[0], acc);
      acc = mfma(xmask(pk(hb, hb), mk2), dw[1], acc);
      float y[4];
#pragma unroll
      for (int i = 0; i < 4; ++i) {
        const unsigned u = asu(dpp<0x128>(acc[i]));
        auto r = __builtin_amdgcn_permlane32_swap(u, u, false, false);
        y[i] = acc[i] + asf(r[1]);
      }
      const auto ya = __builtin_amdgcn_permlane32_swap(asu(y[0]), asu(y[2]), false, false), yb = __builtin_amdgcn_permlane32_swap(asu(y[1]), asu(y[3]), false, false);
      if (r16 < 8) stsc(v4u{ya[0], tv, yb[0], tv}, rs, (r16 >> 1) * 16384 + ((r16 & 1) * 8 + 4 * (g & 1) + 2 * (g >> 1)) * 8, WS_YP + (4 * w * 128 + ai) * 128);
    }
    STAMP(8);
    if (w >= 6) {
      const int off = (lane >> 1) * 128 + (lane & 1) * 16, so = ((w == 7) ? WS_SP : WS_YP) + h * 16384 + j * 32;
      const unsigned xo = __builtin_amdgcn_raw_buffer_load_b32(rx, (lane & 1) * 4, h * 32 + j * 8, 0);
      const bool skip3 = (w == 6) && (lane >= 32);
      v4u p[4]; int spins = 0; unsigned bad;
      wait_until(t_pv + DLY_F);
      do {
        FENCE();
#pragma unroll
        for (int n = 0; n < 4; ++n) p[n] = ldsc(rs, off, so + n * 4096);
        if (skip3) p[3] = v4u{0u, tv, 0u, tv};
        bad = 0;
#pragma unroll
        for (int n = 0; n < 4; ++n) bad |= (p[n].y ^ tv) | (p[n].w ^ tv);
        PIN(bad);
      } while (!all_ok(bad == 0) && ++spins < SPIN_CAP);
      float a0 = (asf(p[0].x) + asf(p[1].x)) + (asf(p[2].x) + asf(p[3].x));
      float a1 = (asf(p[0].z) + asf(p[1].z)) + (asf(p[2].z) + asf(p[3].z));
      a0 += dpp<0x128>(a0); a1 += dpp<0x128>(a1);
      a0 += dpp<0x124>(a0); a1 += dpp<0x124>(a1);
      a0 += dpp<0x122>(a0); a1 += dpp<0x122>(a1);
      a0 = rbf(add32(add16(a0))); a1 = rbf(add32(add16(a1)));
      if (w == 6) {
        a0 = rbf(a0 * rcp(1.0f + ex2(-1.4426950408889634f * a0)));
        a1 = rbf(a1 * rcp(1.0f + ex2(-1.4426950408889634f * a1)));
        if (lane < 2) *lp<f2>(1024 + lane * 8) = f2{a0, a1};
        STAMP(9);
        BAR();
      } else {
        BAR();
        const f2 gt = *lp<f2>(1024 + (lane & 1) * 8);
        const unsigned o = pk(bflo(xo) + rbf(a0 * gt.x), bfhi(xo) + rbf(a1 * gt.y));
        if (lane < 2) *(GAS unsigned*)(out + 16 * h + 4 * j + 2 * lane) = o;
        if (ai == 0 && lane == 0) *(GAS unsigned*)ws = tag;
        STAMP(10);
      }
    }
  }
  STAMP_OUT();
}

extern "C" void kernel_launch(void* const* d_in, const int* in_sizes, int n_in, void* d_out, int out_size, void* d_ws, size_t ws_size, hipStream_t stream) {
  (void)hipFuncSetAttribute((const void*)mla_decode, hipFuncAttributeMaxDynamicSharedMemorySize, LDS_BYTES);
  mla_decode<<<256, 512, LDS_BYTES, stream>>>((const u16*)d_in[0], (const u16*)d_in[2], (unsigned char*)d_ws + WS_BASE(ws_size), (const u16*)d_in[3], (const u16*)d_in[1], (const u16*)d_in[4], (const u16*)d_in[5],
                                              (const u16*)d_in[7], (const u16*)d_in[8], (const u16*)d_in[9], (u16*)d_out, d_in[0]);
}
```

```cpp
#define PADX 7
#include <hip/hip_runtime.h>
#include <stdint.h>

typedef unsigned short u16;
typedef unsigned long long u64;
typedef unsigned v4u __attribute__((ext_vector_type(4)));
typedef unsigned v2u __attribute__((ext_vector_type(2)));
typedef unsigned v8u __attribute__((ext_vector_type(8)));
typedef unsigned v16u __attribute__((ext_vector_type(16)));
typedef float f4 __attribute__((ext_vector_type(4)));
typedef float f2 __attribute__((ext_vector_type(2)));
typedef __bf16 bf8 __attribute__((ext_vector_type(8)));
typedef __bf16 bf2 __attribute__((ext_vector_type(2)));
typedef short s4 __attribute__((ext_vector_type(4)));
#define GAS __attribute__((address_space(1)))
#define LAS __attribute__((address_space(3)))
#define CAS __attribute__((address_space(4)))

#define DELAY_A 0
#define DELAY_Q 300
#define DELAY_O 50

#define SLEEP_H1 8
#define DELAY_F 200
#define WO_NT 0
#define WS_BASE(n) 0
#define DELAY_G 400
#define DELAY_U 0
#define DELAY_H 240
#define ROLE_A_FIRST 1
#define CLEAR_MODE 0
#define FFN_MODE 0
#define DELAY_C 0
constexpr int WS_FL = 1024;
constexpr int WS_FL2 = 2048;
constexpr int WS_QP = 4096;
constexpr int WS_QA = WS_QP + 131072;
constexpr int WS_FP = WS_QA + 32768;
constexpr int WS_ST = WS_FP + 131072;
constexpr int WS_SP = WS_ST + 262144;
constexpr int WS_YP = WS_SP + 524288;
constexpr int WS_OP = WS_YP + 524288;
constexpr int WS_END = WS_OP + 4194304;
constexpr int WS_STAMP = 6 << 20;
constexpr int WS_TUNE = 64;
constexpr int LDS_BYTES = 163840;
constexpr int CODE_BYTES = 16128;
constexpr int SPIN_CAP = 1 << 16;
constexpr int AUX_SC1 = 16, AUX_NT = 2;
constexpr int QAI = 131072;
constexpr unsigned EMPTY = 0xFFFFFFFFu;

#define BAR() asm volatile("s_waitcnt lgkmcnt(0)\n\ts_barrier" ::: "memory")
#define FENCE() asm volatile("" ::: "memory")
#define SCHED() __builtin_amdgcn_sched_barrier(0)
#define VMWAIT(n) asm volatile("s_waitcnt vmcnt(" #n ")" ::: "memory")
#define PIN(x) asm volatile("" : "+v"(x))
#define PINS(x) asm volatile("" : "+s"(x))

#define STAMP(i) do {} while (0)
#define STAMP_OUT() { const unsigned xcc_ = (unsigned)__builtin_amdgcn_s_getreg(20 | (3 << 11)) & 0xfu; if (xcc_ == PADX && !isA) wait_until(t_start + 3000); }

typedef __amdgpu_buffer_rsrc_t rsrc_t;
__device__ __forceinline__ float bflo(unsigned u) { return __uint_as_float(u << 16); }
__device__ __forceinline__ float bfhi(unsigned u) { return __uint_as_float(u & 0xFFFF0000u); }
__device__ __forceinline__ unsigned pk(float a, float b) { f2 f = {a, b}; return __builtin_bit_cast(unsigned, __builtin_convertvector(f, bf2)); }
__device__ __forceinline__ unsigned pkc(float a, float b) { return min(pk(a, b), EMPTY - 1u); }
__device__ __forceinline__ float rbf(float a) { return bflo(pk(a, a)); }
__device__ __forceinline__ float asf(unsigned u) { return __uint_as_float(u); }
__device__ __forceinline__ unsigned asu(float f) { return __float_as_uint(f); }
__device__ __forceinline__ float ex2(float x) { return __builtin_amdgcn_exp2f(x); }
__device__ __forceinline__ float rcp(float x) { return __builtin_amdgcn_rcpf(x); }
__device__ __forceinline__ f4 mfma(v4u a, v4u b, f4 c) {
  return __builtin_amdgcn_mfma_f32_16x16x32_bf16(__builtin_bit_cast(bf8, a), __builtin_bit_cast(bf8, b), c, 0, 0, 0);
}
template <int CTRL> __device__ __forceinline__ float dpp(float v) {
  return __builtin_bit_cast(float, __builtin_amdgcn_update_dpp(0, __builtin_bit_cast(int, v), CTRL, 0xf, 0xf, true));
}
template <int CTRL> __device__ __forceinline__ unsigned dppu(unsigned v) { return (unsigned)__builtin_amdgcn_update_dpp(0, (int)v, CTRL, 0xf, 0xf, true); }
__device__ __forceinline__ float add16(float v) { unsigned u = asu(v); auto r = __builtin_amdgcn_permlane16_swap(u, u, false, false); return asf(r[0]) + asf(r[1]); }
__device__ __forceinline__ float add32(float v) { unsigned u = asu(v); auto r = __builtin_amdgcn_permlane32_swap(u, u, false, false); return asf(r[0]) + asf(r[1]); }
__device__ __forceinline__ float fmx(float a, float b) { return __builtin_fmaxf(a, b); }
__device__ __forceinline__ float mx3(float a, float b, float c) { return fmx(fmx(a, b), c); }
__device__ __forceinline__ float max16(float v) { unsigned u = asu(v); auto r = __builtin_amdgcn_permlane16_swap(u, u, false, false); return fmx(asf(r[0]), asf(r[1])); }
__device__ __forceinline__ float max32(float v) { unsigned u = asu(v); auto r = __builtin_amdgcn_permlane32_swap(u, u, false, false); return fmx(asf(r[0]), asf(r[1])); }
__device__ __forceinline__ float wave_sum(float v) {
  v += dpp<0x128>(v); v += dpp<0x124>(v); v += dpp<0x122>(v); v += dpp<0x121>(v);
  return add32(add16(v));
}
__device__ __forceinline__ unsigned umax4(unsigned acc, v4u v) { return max(max(max(acc, v.x), v.y), max(v.z, v.w)); }
__device__ __forceinline__ rsrc_t mkrs(const void* p, int bytes) { return __builtin_amdgcn_make_buffer_rsrc((void*)p, 0, bytes, 0x00020000); }
__device__ __forceinline__ v4u mksrd(const void* p, unsigned bytes) { const u64 a = (u64)p; return v4u{(unsigned)a, (unsigned)(a >> 32) & 0xFFFFu, bytes, 0x00020000u}; }
__device__ __forceinline__ v4u ldnt(rsrc_t r, int voff, int soff) { return __builtin_amdgcn_raw_buffer_load_b128(r, voff, soff, AUX_NT); }
__device__ __forceinline__ v4u ldsc(rsrc_t r, int voff, int soff) { return __builtin_amdgcn_raw_buffer_load_b128(r, voff, soff, AUX_SC1); }
__device__ __forceinline__ void stsc(v4u v, rsrc_t r, int voff, int soff) { __builtin_amdgcn_raw_buffer_store_b128(v, r, voff, soff, AUX_SC1); }
__device__ __forceinline__ void dma16(v4u srd, int voff, int soff, int lds_dst) {
  asm volatile("s_mov_b32 m0, %3\n\ts_nop 0\n\tbuffer_load_dwordx4 %0, %1, %2 offen nt lds" :: "v"(voff), "s"(srd), "s"(soff), "s"(lds_dst) : "memory");
}
__device__ __forceinline__ void dma16p(v4u srd, int voff, int soff, int lds_dst) {
  asm volatile("s_mov_b32 m0, %3\n\ts_nop 0\n\tbuffer_load_dwordx4 %0, %1, %2 offen lds" :: "v"(voff), "s"(srd), "s"(soff), "s"(lds_dst) : "memory");
}
__device__ __forceinline__ void dma16sc(v4u srd, int voff, int soff, int lds_dst) {
  asm volatile("s_mov_b32 m0, %3\n\ts_nop 0\n\tbuffer_load_dwordx4 %0, %1, %2 offen sc1 lds" :: "v"(voff), "s"(srd), "s"(soff), "s"(lds_dst) : "memory");
}
template <typename T> __device__ __forceinline__ LAS T* lp(int off) { return (LAS T*)(uintptr_t)(unsigned)off; }
__device__ __forceinline__ v4u lds128(int off) { return *lp<v4u>(off); }
__device__ __forceinline__ v2u ldstr(int off) { return __builtin_bit_cast(v2u, __builtin_amdgcn_ds_read_tr16_b64_v4i16(lp<s4>(off))); }
__device__ __forceinline__ int swz(int r) { return ((r & 7) << 1) | ((r >> 3) & 1); }
__device__ __forceinline__ v4u xmask(unsigned xx, const unsigned (&mk)[4]) { return v4u{xx & mk[0], xx & mk[1], xx & mk[2], xx & mk[3]}; }
__device__ __forceinline__ bool all_ok(bool ok) { return __builtin_amdgcn_ballot_w64(!ok) == 0ull; }
__device__ __forceinline__ void wait_until(u64 t) { while ((long long)(__builtin_amdgcn_s_memrealtime() - t) < 0) __builtin_amdgcn_s_sleep(1); }

__device__ __forceinline__ void ln_stats(v4u xa, float& mb, float& ri) {
  float v[8] = {bflo(xa.x), bfhi(xa.x), bflo(xa.y), bfhi(xa.y), bflo(xa.z), bfhi(xa.z), bflo(xa.w), bfhi(xa.w)};
  const float mean = wave_sum(((v[0] + v[1]) + (v[2] + v[3])) + ((v[4] + v[5]) + (v[6] + v[7]))) * (1.0f / 512.0f);
  float q0 = 0.f, q1 = 0.f;
#pragma unroll
  for (int i = 0; i < 4; ++i) { const float d = v[i] - mean, e = v[i + 4] - mean; q0 += d * d; q1 += e * e; }
  const float var = wave_sum(q0 + q1) * (1.0f / 512.0f);
  mb = rbf(mean);
  ri = rcp(rbf(__builtin_amdgcn_sqrtf(rbf(rbf(var) + 1e-5f))));
}

__device__ __forceinline__ v4u layernorm8(v4u xa) {
  float mb, ri;
  ln_stats(xa, mb, ri);
  v4u o;
#pragma unroll
  for (int i = 0; i < 4; ++i) { const unsigned d = pk(bflo(xa[i]) - mb, bfhi(xa[i]) - mb); o[i] = pk(bflo(d) * ri, bfhi(d) * ri); }
  return o;
}

typedef u64 v8q __attribute__((ext_vector_type(8)));
typedef u64 v4q __attribute__((ext_vector_type(4)));
__global__ void __launch_bounds__(512, 2) mla_decode(const void*, const void*, void*, const void*, const void*, const void*, const void*, const void*,
                                                      const void*, const void*, void*, const void*) {
  v8q ka0; v4q ka1; u64 t_start;
  asm volatile("s_load_dwordx16 %0, %3, 0x0\n\ts_load_dwordx8 %1, %3, 0x40\n\ts_memrealtime %2" : "=&s"(ka0), "=&s"(ka1), "=&s"(t_start) : "s"(__builtin_amdgcn_kernarg_segment_ptr()));
  const int b = blockIdx.x, tid = threadIdx.x, lane = tid & 63, w = __builtin_amdgcn_readfirstlane(tid >> 6);
  const int xcd = b & 7, rr = (b >> 3) & 15;
  const bool isA = b < 128;
  const int r16 = lane & 15, g = lane >> 4;
  const int h = xcd * 4 + (rr & 3), j = rr >> 2;
  const int ai = rr * 8 + xcd;
  {
    u64 pc; asm volatile("s_getpc_b64 %0" : "=s"(pc));
    asm volatile("s_mov_b32 m0, %2\n\ts_nop 0\n\tglobal_load_lds_dword %0, %1" :: "v"(min(((b >> 3) * 8 + w) * 128, CODE_BYTES - 128) + r16 * 8), "s"(pc & ~255ull), "s"(114688) : "memory");
  }
#define COMMON_CONSTS \
  const int dchunk = (r16 ^ swz(4 * (w & 3) + g)) * 16;     \
  const int trrow = 4 * g + (r16 >> 2); \
  const int trbase = trrow * 256 + ((((r16 >> 1) & 1) ^ swz(trrow)) * 16) + 8 * (r16 & 1);     \
  const int rbase = r16 * 256 + ((4 * g) ^ swz(r16)) * 16;                                      \
  const int trbaseK = trbase + trrow * 768, rbaseK = rbase + r16 * 768;                         \
  const int vrow0 = g * 256 + (r16 ^ (2 * w)) * 16, vrow1 = vrow0 ^ 16;                         \
  unsigned mk[4]; \
  _Pragma("unroll") for (int k = 0; k < 4; ++k) mk[k] = ((r16 >> 1) == k) ? ((r16 & 1) ? 0xFFFF0000u : 0x0000FFFFu) : 0u;

  const int cons = h * 4 + j;
  v4u wqv[8], xa, xb;
  int oA = (128 * j + 32 * (w & 3) + 8 * g) * 8192 + h * 256 + r16 * 16, oq[4], ox = (w == 2) ? lane * 16 : (128 * j + 32 * (w & 3) + 8 * g) * 2, oc = WS_QA + cons * 256 + lane * 4;
  unsigned em = EMPTY;
  {
#pragma unroll
    for (int n = 0; n < 4; ++n) { oq[n] = (128 * j + 16 * w + 4 * n + g) * 8192 + h * 256 + (r16 ^ swz(4 * n + g)) * 16; PIN(oq[n]); }
    PIN(oA); PIN(ox); PIN(oc); PIN(em);
    SCHED();
    asm volatile("s_waitcnt lgkmcnt(0)" : "+s"(ka0), "+s"(ka1), "+s"(t_start));
  }
  const u16 *x = (const u16*)ka0[0], *wq = (const u16*)ka0[1], *wuk = (const u16*)ka0[3], *ckv = (const u16*)ka0[4], *wuv = (const u16*)ka0[5], *wo = (const u16*)ka0[6],
            *wg = (const u16*)ka0[7], *wu = (const u16*)ka1[0], *wd = (const u16*)ka1[1];
  unsigned char* ws = (unsigned char*)ka0[2];
  u16* out = (u16*)ka1[2];
  {
    int b2 = b;
    PINS(b2);
    if (b2 >= 128) {
      if (w == 0) asm volatile("global_store_dword %0, %1, %2 sc1" :: "v"(oc), "v"(em), "s"(ws) : "memory");
#pragma unroll
      for (int n = 0; n < 4; ++n)
        asm volatile("s_mov_b32 m0, %2\n\ts_nop 0\n\tglobal_load_lds_dwordx4 %0, %1 nt" :: "v"(oq[n]), "s"(wuk), "s"(QAI + w * 4096 + n * 1024) : "memory");
    } else {
    if (w == 2) asm volatile("global_load_dwordx4 %0, %1, %2" : "=&v"(xa) : "v"(ox), "s"(x) : "memory");
    if (w >= 4) asm volatile("global_load_dwordx4 %0, %1, %2" : "=&v"(xb) : "v"(ox), "s"(x) : "memory");
    if (w >= 4) {
      const v4u sq = mksrd(wq, 4194304);
      asm volatile("buffer_load_dwordx4 %0, %8, %9, 0 offen nt\n\tbuffer_load_dwordx4 %1, %8, %9, %10 offen nt\n\tbuffer_load_dwordx4 %2, %8, %9, %11 offen nt\n\tbuffer_load_dwordx4 %3, %8, %9, %12 offen nt\n\t"
                   "buffer_load_dwordx4 %4, %8, %9, %13 offen nt\n\tbuffer_load_dwordx4 %5, %8, %9, %14 offen nt\n\tbuffer_load_dwordx4 %6, %8, %9, %15 offen nt\n\tbuffer_load_dwordx4 %7, %8, %9, %16 offen nt"
                   : "=&v"(wqv[0]), "=&v"(wqv[1]), "=&v"(wqv[2]), "=&v"(wqv[3]), "=&v"(wqv[4]), "=&v"(wqv[5]), "=&v"(wqv[6]), "=&v"(wqv[7])
                   : "v"(oA), "s"(sq), "s"(8192), "s"(16384), "s"(24576), "s"(32768), "s"(40960), "s"(49152), "s"(57344) : "memory");
    }
    asm volatile("s_barrier" ::: "memory");
    }
  }
#define DLY_G DELAY_G
#define DLY_H DELAY_H
#define DLY_U DELAY_U
#define DLY_A DELAY_A
#define DLY_Q DELAY_Q
#define DLY_O DELAY_O
#define DLY_F DELAY_F
  if (!isA) {
    STAMP(9);
    const int xrow0 = 128 * j + 16 * w;
    const unsigned tag = *(const CAS unsigned*)ws + 1u;
    const rsrc_t rs = mkrs(ws, WS_END);
    COMMON_CONSTS
    int po = lane * 16, a_wk = QAI + w * 4096 + rbase, a_qw = 73728 + lane * 4, a_qr = 73728 + g * 64, a_qa = 74240 + w * 32 + g * 8;
    unsigned tv = tag;
#pragma unroll
    for (int k = 0; k < 4; ++k) PIN(mk[k]);
    PIN(po); PIN(a_wk); PIN(a_qw); PIN(a_qr); PIN(a_qa); PIN(tv);
    if (w == 0) {
      VMWAIT(4);
      if (lane == 0) __builtin_amdgcn_raw_buffer_store_b64(v2u{tv, tv}, rs, 0, WS_FL + cons * 8, AUX_SC1);
      STAMP(10);
    }
    if (w == 2) *lp<v4u>(65536 + po) = layernorm8(*(const GAS v4u*)(x + 8 * lane));
    VMWAIT(0);
    BAR();
    STAMP(13);
    v4u wkv[4], xm[4];
#pragma unroll
    for (int s = 0; s < 4; ++s) wkv[s] = lds128(a_wk ^ (s * 16));
    {
      const v2u x4 = *lp<v2u>(65536 + (xrow0 + 4 * g) * 2);
      xm[0] = xmask(__builtin_amdgcn_perm(x4.x, x4.x, 0x01000100u), mk); xm[1] = xmask(__builtin_amdgcn_perm(x4.x, x4.x, 0x03020302u), mk);
      xm[2] = xmask(__builtin_amdgcn_perm(x4.y, x4.y, 0x01000100u), mk); xm[3] = xmask(__builtin_amdgcn_perm(x4.y, x4.y, 0x03020302u), mk);
#pragma unroll
      for (int i = 0; i < 4; ++i) { PIN(xm[i]); PIN(wkv[i]); }
    }
    if (w == 1) {
      v4u p[4]; int spins = 0; unsigned bad;
      wait_until(t_start + DLY_H);
      for (;;) {
        FENCE();
#pragma unroll
        for (int i = 0; i < 4; ++i) p[i] = ldsc(rs, po + i * 1024, WS_QP + h * 4096);
        bad = 0;
#pragma unroll
        for (int i = 0; i < 4; ++i) bad |= (p[i].y ^ tv) | (p[i].w ^ tv);
        PIN(bad);
        if (all_ok(bad == 0) || ++spins >= SPIN_CAP) break;
        __builtin_amdgcn_s_sleep(1);
      }
      const float q0 = (asf(p[0].x) + asf(p[1].x)) + (asf(p[2].x) + asf(p[3].x));
      const float q1 = (asf(p[0].z) + asf(p[1].z)) + (asf(p[2].z) + asf(p[3].z));
      *lp<unsigned>(a_qw) = pk(q0, q1);
    }
    BAR();
    STAMP(2);
    {
      v4u qf[4];
#pragma unroll
      for (int s = 0; s < 4; ++s) qf[s] = lds128(a_qr + s * 16);
      SCHED();
      f4 acc = {0.f, 0.f, 0.f, 0.f};
#pragma unroll
      for (int s = 0; s < 4; ++s) acc = mfma(wkv[s], qf[s], acc);
      const float sc = 0.08838834764831845f * 1.4426950408889634f;
      if (r16 == 0) *lp<v2u>(a_qa) = v2u{pkc(acc[0] * sc, acc[1] * sc), pkc(acc[2] * sc, acc[3] * sc)};
    }
    BAR();
    if (w == 0 && lane < 16) stsc(lds128(74240 + po), rs, po, WS_QA + cons * 256);
    STAMP(3);
    BAR();
    wait_until(t_start + DLY_Q);
    v4u buv[4], bo[4][4];
    {
      const v4u suv = mksrd(wuv, 4194304), so = mksrd(wo, 4194304);
      const int so2 = (h * 128 + 8 * (w >> 2) + (w & 3)) * 1024;
      int tq[4];
#pragma unroll
      for (int t = 0; t < 4; ++t) tq[t] = (w >> 1) * 256 + (trbaseK ^ (32 * (4 * (w & 1) + t)));
#define WO4(n) _Pragma("unroll") for (int i = 0; i < 4; ++i) dma16p(so, (i & 1) ? vrow1 : vrow0, so2 + ((((n) + j) & 3) * 32 + 16 * (i & 1) + 4 * (i >> 1)) * 1024, (32 * (n) + w + 8 * i) * 1024)
#define RD_BO(n) _Pragma("unroll") for (int t = 0; t < 4; ++t) { const v2u lo = ldstr(tq[t] + (n) * 32768), hi = ldstr(tq[t] + (n) * 32768 + 16384); bo[t][n] = v4u{lo.x, lo.y, hi.x, hi.y}; }
      WO4(0);
      asm volatile("s_barrier" ::: "memory");
      WO4(1);
      asm volatile("s_barrier" ::: "memory");
      WO4(2);
      asm volatile("s_waitcnt vmcnt(8)\n\ts_barrier" ::: "memory");
      WO4(3);
      RD_BO(0)
      asm volatile("s_waitcnt vmcnt(8)\n\ts_barrier" ::: "memory");
      STAMP(14);
      wait_until(t_start + DLY_U);
      v4u fw[4];
      {
        const GAS char* pf = (const GAS char*)((r16 < 8) ? wg : wu) + (xrow0 + 4 * g + 1) * 3584 + (h < 28 ? h : 0) * 128 + (r16 & 7) * 16;
        asm volatile("global_load_dwordx4 %0, %4, off offset:-3584 nt\n\tglobal_load_dwordx4 %1, %4, off nt\n\tglobal_load_dwordx4 %2, %4, off offset:3584 nt\n\tglobal_load_dwordx4 %3, %5, off nt"
                     : "=&v"(fw[0]), "=&v"(fw[1]), "=&v"(fw[2]), "=&v"(fw[3]) : "v"(pf), "v"(pf + 7168) : "memory");
      }
      RD_BO(1)
      asm volatile("s_waitcnt vmcnt(4) lgkmcnt(0)\n\ts_barrier" ::: "memory");
      const int vo1 = g * 16 * 8192 + dchunk, so1 = (128 * j + 2 * (w & 3) + (w >> 2)) * 8192 + h * 256;
#pragma unroll 1
      for (int n = 0; n < 4; ++n) dma16(suv, vo1, so1 + (64 * (n >> 1) + 8 * (n & 1)) * 8192, QAI + (32 * n + 4 * w) * 256);
      RD_BO(2)
      RD_BO(3)
      asm volatile("s_waitcnt vmcnt(4)" : "+v"(fw[0]), "+v"(fw[1]), "+v"(fw[2]), "+v"(fw[3]) :: "memory");
      f4 acc = {0.f, 0.f, 0.f, 0.f};
#pragma unroll
      for (int i = 0; i < 4; ++i) acc = mfma(xm[i], fw[i], acc);
      if (lane < 32) *lp<f4>(32768 + w * 512 + r16 * 32 + (lane & 16)) = acc;
      BAR();
      if (w == 0) {
        f2 p[8];
#pragma unroll
        for (int i = 0; i < 8; ++i) p[i] = *lp<f2>(32768 + lane * 8 + i * 512);
        const f2 s = ((p[0] + p[1]) + (p[2] + p[3])) + ((p[4] + p[5]) + (p[6] + p[7]));
        stsc(v4u{asu(s.x), tv, asu(s.y), tv}, rs, po, WS_FP + cons * 1024);
        STAMP(15);
        VMWAIT(1);
      } else {
        VMWAIT(0);
      }
      asm volatile("s_barrier" ::: "memory");
      STAMP(4);
#pragma unroll
      for (int ks = 0; ks < 4; ++ks) {
        const int a = QAI + (trbase ^ (32 * w)) + ks * 8192;
        const v2u lo = ldstr(a), hi = ldstr(a + 4096);
        buv[ks] = v4u{lo.x, lo.y, hi.x, hi.y};
      }
    }
    int a_st = (4 * g + (r16 & 3)) * 512, a_od = g * 1024 + r16 * 16, a_w1 = w * 272 + r16 * 16 + (g & 1) * 8, a_r1 = (r16 & 7) * 272 + g * 16,
        a_w2 = 8192 + ((16 * w + r16 - 32 * j) & 127) * 2, a_r2 = 8192 + g * 16, a_o = g * 16384 + r16 * 8;
    PIN(a_st); PIN(a_od); PIN(a_w1); PIN(a_r1); PIN(a_w2); PIN(a_r2); PIN(a_o);
    STAMP(5);
    v4u st, od[4];
    {
      int spins = 0;
      do {
        FENCE();
        st = ldsc(rs, a_st, WS_ST + w * 8192 + h * 16);
      } while (!all_ok((st.y == tv) & (st.w == tv)) && ++spins < SPIN_CAP);
      const u64 t_st = __builtin_amdgcn_s_memrealtime() + (spins ? DLY_O : 0);
      STAMP(6);
      const float m = asf(st.x);
      float mw = fmx(m, dpp<0xB1>(m)); mw = fmx(mw, dpp<0x4E>(mw)); mw = max32(max16(mw));
      float lw = ex2(m - mw) * asf(st.z);
      lw += dpp<0xB1>(lw); lw += dpp<0x4E>(lw); lw = add32(add16(lw));
      if (lane == 0) *lp<f2>(4096 + w * 8) = f2{mw, lw};
      wait_until(t_st);
      FENCE();
#pragma unroll
      for (int i = 0; i < 4; ++i) od[i] = ldsc(rs, a_od + i * 256, WS_OP + (cons * 128 + 16 * w) * 256);
      BAR();
      const f2 sw = *lp<f2>(4096 + (lane & 7) * 8);
      float M = fmx(sw.x, dpp<0xB1>(sw.x)); M = fmx(M, dpp<0x4E>(M)); M = fmx(M, dpp<0x141>(M));
      float Lt = ex2(sw.x - M) * sw.y;
      Lt += dpp<0xB1>(Lt); Lt += dpp<0x4E>(Lt); Lt += dpp<0x141>(Lt);
      const float e = ex2(m - M) * rcp(Lt);
      const unsigned wb = pk(e, e);
      xm[0] = xmask(dppu<0x00>(wb), mk); xm[1] = xmask(dppu<0x55>(wb), mk); xm[2] = xmask(dppu<0xAA>(wb), mk); xm[3] = xmask(dppu<0xFF>(wb), mk);
#pragma unroll
      for (int i = 0; i < 4; ++i) PIN(xm[i]);
      SCHED();
    }
    {
      int spins = 0; f4 acc;
      for (;;) {
        unsigned bad = 0;
        acc = f4{0.f, 0.f, 0.f, 0.f};
#pragma unroll
        for (int i = 0; i < 4; ++i) { acc = mfma(xm[i], od[i], acc); bad = umax4(bad, od[i]); }
        if (all_ok(bad != EMPTY) || ++spins >= SPIN_CAP) break;
        FENCE();
#pragma unroll
        for (int i = 0; i < 4; ++i) od[i] = ldsc(rs, a_od + i * 256, WS_OP + (cons * 128 + 16 * w) * 256);
      }
      if (lane < 32) *lp<v2u>(a_w1) = v2u{pk(acc[0], acc[1]), pk(acc[2], acc[3])};
    }
    STAMP(7);
    BAR();
    {
      v4u af[4];
#pragma unroll
      for (int ks = 0; ks < 4; ++ks) af[ks] = lds128(a_r1 + ks * 64);
      SCHED();
      f4 acc = {0.f, 0.f, 0.f, 0.f};
#pragma unroll
      for (int ks = 0; ks < 4; ++ks) acc = mfma(af[ks], buv[ks], acc);
      const float c = add16((acc[0] + acc[1]) + (acc[2] + acc[3]));
      if (lane < 16) *lp<u16>(a_w2) = (u16)pk(c, c);
    }
    BAR();
    {
      v4u cf[4];
#pragma unroll
      for (int ks = 0; ks < 4; ++ks) cf[ks] = lds128(a_r2 + ks * 64);
      SCHED();
      f4 acc[4];
#pragma unroll
      for (int t = 0; t < 4; ++t) acc[t] = f4{0.f, 0.f, 0.f, 0.f};
#pragma unroll
      for (int ks = 0; ks < 4; ++ks)
#pragma unroll
        for (int t = 0; t < 4; ++t) acc[t] = mfma(cf[ks], bo[t][ks], acc[t]);
      const float v01 = (g & 1) ? acc[1][0] : acc[0][0], v23 = (g & 1) ? acc[3][0] : acc[2][0];
      const float v = (g & 2) ? v23 : v01;
      __builtin_amdgcn_raw_buffer_store_b64(v2u{asu(v), tv}, rs, a_o, WS_SP + (4 * w * 128 + cons) * 128, AUX_SC1);
    }
    STAMP(8);
    STAMP_OUT();
    __builtin_amdgcn_endpgm();
  }
  {
    unsigned tagA;
    asm volatile("s_load_dword %0, %1, 0x0" : "=&s"(tagA) : "s"(ws));
    const v4u sws = mksrd(ws, WS_END);
    int po = lane * 16;
    const int ht = w & 1, kt = w >> 1;
    v4u k0[16], k1[16];
    v2u v0[4][4];
    if (w < 2) {
      const v4u skv = mksrd(ckv, 16777216);
      const int so = (128 * ai + w) * 1024, vb = po ^ (w * 32);
      for (int i = 0; i < (int)(DLY_A); ++i) __builtin_amdgcn_s_sleep(1);
#pragma unroll 1
      for (int i = 0; i < 8; ++i) {
        const int blk = ((2 * i) & 7) + (i >> 2);
        if (i == 2) asm volatile("s_barrier" ::: "memory");
        if (i == 6) asm volatile("s_waitcnt vmcnt(16)\n\ts_barrier" ::: "memory");
#pragma unroll
        for (int r = 0; r < 8; ++r) dma16(skv, vb ^ ((r & 3) * 64 + (r >> 2) * 16), so + blk * 16384 + r * 2048, w * 1024 + blk * 16384 + r * 2048);
      }
      STAMP(15);
    }
    SCHED();
    const rsrc_t rx = mkrs(x, 1024), rs = mkrs(ws, WS_END);
    COMMON_CONSTS
#pragma unroll
    for (int k = 0; k < 4; ++k) PIN(mk[k]);
    asm volatile("s_waitcnt lgkmcnt(0)" : "+s"(tagA));
    const unsigned tag = tagA + 1u;
    unsigned tv = tag;
    PIN(tv);
    v4u xn = {0u, 0u, 0u, 0u};
    if (w >= 4) {
      unsigned mk2[4];
      v4u xm[8];
#pragma unroll
      for (int k = 0; k < 4; ++k) { mk2[k] = mk[k] | dppu<0x128>(mk[k]); PIN(mk2[k]); }
      int a_w0 = QAI + (w - 4) * 1024 + (g >> 1) * 512 + r16 * 32 + (lane & 16);
      PIN(a_w0);
      asm volatile("s_waitcnt vmcnt(8)" : "+v"(xb) :: "memory");
#pragma unroll
      for (int k = 0; k < 4; ++k) {
        const unsigned xp = (r16 >= 8) ? 0x3F803F80u : xb[k];
        xm[2 * k] = xmask(__builtin_amdgcn_perm(xp, xp, 0x01000100u), mk2); xm[2 * k + 1] = xmask(__builtin_amdgcn_perm(xp, xp, 0x03020302u), mk2);
        PIN(xm[2 * k]); PIN(xm[2 * k + 1]);
      }
      SCHED();
      f4 acc = {0.f, 0.f, 0.f, 0.f};
      asm volatile("s_waitcnt vmcnt(2)" : "+v"(wqv[0]), "+v"(wqv[1]), "+v"(wqv[2]), "+v"(wqv[3]), "+v"(wqv[4]), "+v"(wqv[5]) :: "memory");
#pragma unroll
      for (int i = 0; i < 6; ++i) acc = mfma(xm[i], wqv[i], acc);
      SCHED();
      asm volatile("s_waitcnt vmcnt(0)" : "+v"(wqv[6]), "+v"(wqv[7]) :: "memory");
      acc = mfma(xm[6], wqv[6], acc); acc = mfma(xm[7], wqv[7], acc);
      STAMP(12);
      *lp<f4>(a_w0) = acc;
      BAR();
    } else if (w == 2) {
      __builtin_amdgcn_s_setprio(3);
      float mb, ri;
      asm volatile("s_waitcnt vmcnt(0)" : "+v"(xa) :: "memory");
      ln_stats(xa, mb, ri);
      int a_qp = ((lane & 31) * 2 + (lane >> 5)) * 16, a_r0 = QAI + po;
      PIN(mb); PIN(ri); PIN(a_qp); PIN(a_r0);
      BAR();
      f4 p[4];
#pragma unroll
      for (int i = 0; i < 4; ++i) p[i] = *lp<f4>(a_r0 + i * 1024);
      const f4 s = (p[0] + p[1]) + (p[2] + p[3]);
      const auto r0 = __builtin_amdgcn_permlane32_swap(asu(s[0]), asu(s[2]), false, false), r1 = __builtin_amdgcn_permlane32_swap(asu(s[1]), asu(s[3]), false, false);
      stsc(v4u{asu((asf(r0[0]) - mb * asf(r0[1])) * ri), tv, asu((asf(r1[0]) - mb * asf(r1[1])) * ri), tv}, rs, a_qp, WS_QP + cons * 1024);
      STAMP(1);
      __builtin_amdgcn_s_setprio(0);
    } else if (w == 3) {
      BAR();
    }
    const int mi = ai & 3, fi = (ai < 112) ? (ai >> 2) : 0;
    int ka[4], ta[4], qa_[4], vo0 = vrow0, vo1 = vrow1;
#pragma unroll
    for (int s = 0; s < 4; ++s) {
      ka[s] = kt * 32768 + (rbaseK ^ (s * 16)); ta[s] = (w >> 1) * 256 + (trbaseK ^ (32 * (4 * (w & 1) + s))); qa_[s] = ka[s] + (QAI + ht * 16384 - kt * 32768);
      PIN(ka[s]); PIN(ta[s]); PIN(qa_[s]);
    }
    int a_d = (2 * g + (r16 >> 3)) * 1024 + (r16 & 7) * 16, a_mw = (16 * ht + r16) * 16 + kt * 4,
        a_s = (lane < 16) ? (16 * ht + r16) * 16 : WS_END, a_o = (16 * g + (w >> 1)) * 32768 + (w & 1) * 128 + r16 * 8, a_pq = QAI + po,
        a_c = (16 * w + g) * 32768 + r16 * 16;
    PIN(a_d); PIN(a_mw); PIN(a_s); PIN(a_o); PIN(a_pq); PIN(a_c); PIN(vo0); PIN(vo1);
#define RD_U0() _Pragma("unroll") for (int i = 0; i < 4; ++i) _Pragma("unroll") for (int s = 0; s < 4; ++s) { k0[4 * i + s] = lds128(ka[s] + i * 256); v0[i][s] = ldstr(ta[s] + i * 32768); }
#define RD_K1() _Pragma("unroll") for (int s = 0; s < 16; ++s) k1[s] = lds128(ka[s & 3] + (s >> 2) * 256 + 16384)
#define FLAGS_OK() all_ok(((fl.x ^ tv) | (fl.y ^ tv) | (fl.z ^ tv) | (fl.w ^ tv)) == 0)
#define QA4() _Pragma("unroll") for (int q = 0; q < 4; ++q) dma16sc(sws, (q & 1) ? vo1 : vo0, WS_QA + (w + 8 * q) * 1024, QAI + (w + 8 * q) * 1024)
    if (w >= 2) {
      v4u fl;
      if (w == 3) {
        __builtin_amdgcn_s_setprio(3);
        asm volatile("s_waitcnt vmcnt(0)\n\tbuffer_load_dwordx4 %0, %1, %2, %3 offen sc1" : "=&v"(fl) : "v"(po), "s"(sws), "s"(WS_FL) : "memory");
      }
      asm volatile("s_barrier" ::: "memory");
      RD_U0()
      if (w == 3) {
        asm volatile("s_waitcnt vmcnt(0)" : "+v"(fl) :: "memory");
        int spins = 0;
        while (!FLAGS_OK() && ++spins < SPIN_CAP)
          asm volatile("buffer_load_dwordx4 %0, %1, %2, %3 offen sc1\n\ts_waitcnt vmcnt(0)" : "=&v"(fl) : "v"(po), "s"(sws), "s"(WS_FL) : "memory");
        __builtin_amdgcn_s_setprio(0);
      }
    }
    asm volatile("s_waitcnt vmcnt(16)\n\ts_barrier" ::: "memory");
    STAMP(2);
    const bool early = (long long)(__builtin_amdgcn_s_memrealtime() - (t_start + DLY_G)) >= 0;
    if (early) { QA4(); }
    if (w < 2) { RD_U0() }
    if (kt < 2) { RD_K1(); }
    if (!early) { wait_until(t_start + DLY_G); QA4(); }
    VMWAIT(4);
    BAR();
    STAMP(3);
    if (kt >= 2) { RD_K1(); }
    for (int spins = 0;; ++spins) {
      VMWAIT(0);
      unsigned bad = 0;
#pragma unroll
      for (int q = 0; q < 4; ++q) bad = umax4(bad, lds128(a_pq + w * 1024 + q * 8192));
      if (all_ok(bad != EMPTY) || spins >= SPIN_CAP) break;
      QA4();
    }
    BAR();
    STAMP(4);
    f4 S0 = {0.f, 0.f, 0.f, 0.f}, S1 = {0.f, 0.f, 0.f, 0.f};
    v4u vf[4][4];
    {
      v4u qf[16];
#pragma unroll
      for (int s = 0; s < 4; ++s) qf[s] = lds128(qa_[s]);
      SCHED();
#pragma unroll
      for (int s = 0; s < 16; ++s) {
        if (s < 12) qf[s + 4] = lds128(qa_[s & 3] + ((s + 4) >> 2) * 256);
        { const v2u hi = ldstr(ta[s & 3] + (s >> 2) * 32768 + 16384); vf[s >> 2][s & 3] = v4u{v0[s >> 2][s & 3].x, v0[s >> 2][s & 3].y, hi.x, hi.y}; }
        if (s < 8 && !(s & 1)) stsc(v4u{EMPTY, EMPTY, EMPTY, EMPTY}, rs, a_c, WS_OP + (s >> 1) * 131072 + ai * 256);
        S0 = mfma(k0[s], qf[s], S0); S1 = mfma(k1[s], qf[s], S1);
        SCHED();
      }
      float m0 = mx3(mx3(mx3(S0[0], S0[1], S0[2]), S0[3], S1[0]), S1[1], S1[2]);
      m0 = max32(max16(fmx(m0, S1[3])));
      if (lane < 16) *lp<float>(a_mw) = m0;
    }
    BAR();
    STAMP(5);
    float M;
    {
      const f4 m4 = *lp<f4>(a_mw & ~15);
      M = fmx(mx3(m4[0], m4[1], m4[2]), m4[3]);
      float p[8];
#pragma unroll
      for (int i = 0; i < 4; ++i) { p[i] = ex2(S0[i] - M); p[4 + i] = ex2(S1[i] - M); }
      *lp<v4u>(a_pq + w * 1024) = v4u{pk(p[0], p[1]), pk(p[2], p[3]), pk(p[4], p[5]), pk(p[6], p[7])};
      const float l = add32(add16(((p[0] + p[1]) + (p[2] + p[3])) + ((p[4] + p[5]) + (p[6] + p[7]))));
      if (lane < 16) *lp<float>(512 + a_mw) = l;
    }
    VMWAIT(0);
    BAR();
    STAMP(6);
    {
      v4u pf_[8];
      pf_[0] = lds128(a_pq);
      const f4 l4 = *lp<f4>(512 + (a_mw & ~15));
#pragma unroll
      for (int i = 1; i < 8; ++i) pf_[i] = lds128(a_pq + ((i & 3) * 2 + (i >> 2)) * 1024);
      SCHED();
#pragma unroll
      for (int hh = 0; hh < 2; ++hh) {
        f4 O[4];
#pragma unroll
        for (int t = 0; t < 4; ++t) O[t] = f4{0.f, 0.f, 0.f, 0.f};
#pragma unroll
        for (int ks = 0; ks < 4; ++ks) {
#pragma unroll
          for (int t = 0; t < 4; ++t) O[t] = mfma(pf_[4 * hh + ks], vf[ks][t], O[t]);
          if (hh == 0 && ks == 0) { SCHED(); if (kt == 0) stsc(v4u{asu(M), tv, asu((l4[0] + l4[1]) + (l4[2] + l4[3])), tv}, rs, a_s, WS_ST + ai * 512); SCHED(); }
        }
        if (hh == 1) { PIN(O[3]); STAMP(13); }
#pragma unroll
        for (int i = 0; i < 4; ++i)
          __builtin_amdgcn_raw_buffer_store_b64(v2u{pkc(O[0][i], O[1][i]), pkc(O[2][i], O[3][i])}, rs, a_o, WS_OP + (16 * hh + i) * 131072 + ai * 256, AUX_SC1);
      }
    }
    STAMP(7);
    const u64 t_pv = __builtin_amdgcn_s_memrealtime();
    if (ai < 112) {
      const rsrc_t rd = mkrs(wd, 1835008);
      v4u dw[2];
#pragma unroll
      for (int i = 0; i < 2; ++i) dw[i] = ldnt(rd, a_d, (64 * fi + 16 * mi + 8 * i) * 1024 + w * 128);
      STAMP(14);
      v4u p; int spins = 0;
      const int gu = (r16 >> 2) & 1, hi = r16 >> 3;
      const int po2 = (r16 & 3) * 1024 + (64 * gu + 8 * hi + 2 * g) * 8;
      do {
        FENCE();
        p = ldsc(rs, po2, WS_FP + fi * 4096 + mi * 128);
      } while (!all_ok((p.y == tv) & (p.w == tv)) && ++spins < SPIN_CAP);
      float v0_ = asf(p.x), v1_ = asf(p.z);
      v0_ += dpp<0xB1>(v0_); v1_ += dpp<0xB1>(v1_);
      v0_ += dpp<0x4E>(v0_); v1_ += dpp<0x4E>(v1_);
      const float o0 = dpp<0x141>(v0_), o1 = dpp<0x141>(v1_);
      float hv[2];
#pragma unroll
      for (int e = 0; e < 2; ++e) {
        const float gt = rbf(gu ? (e ? o1 : o0) : (e ? v1_ : v0_)), up = rbf(gu ? (e ? v1_ : v0_) : (e ? o1 : o0));
        const float z2 = 2.0f * 0.7978845608028654f * 1.4426950408889634f * (gt + 0.044715f * gt * gt * gt);
        hv[e] = rbf(gt * rcp(1.0f + ex2(-z2))) * up;
      }
      const float t0 = dpp<0x128>(hv[0]), t1 = dpp<0x128>(hv[1]);
      unsigned mk2[4];
#pragma unroll
      for (int k = 0; k < 4; ++k) mk2[k] = mk[k] | dppu<0x128>(mk[k]);
      const float ha = hi ? t1 : hv[0], hb = hi ? hv[1] : t0;
      f4 acc = {0.f, 0.f, 0.f, 0.f};
      acc = mfma(xmask(pk(ha, ha), mk2), dw[0], acc);
      acc = mfma(xmask(pk(hb, hb), mk2), dw[1], acc);
      float y[4];
#pragma unroll
      for (int i = 0; i < 4; ++i) {
        const unsigned u = asu(dpp<0x128>(acc[i]));
        auto r = __builtin_amdgcn_permlane32_swap(u, u, false, false);
        y[i] = acc[i] + asf(r[1]);
      }
      const auto ya = __builtin_amdgcn_permlane32_swap(asu(y[0]), asu(y[2]), false, false), yb = __builtin_amdgcn_permlane32_swap(asu(y[1]), asu(y[3]), false, false);
      if (r16 < 8) stsc(v4u{ya[0], tv, yb[0], tv}, rs, (r16 >> 1) * 16384 + ((r16 & 1) * 8 + 4 * (g & 1) + 2 * (g >> 1)) * 8, WS_YP + (4 * w * 128 + ai) * 128);
    }
    STAMP(8);
    if (w >= 6) {
      const int off = (lane >> 1) * 128 + (lane & 1) * 16, so = ((w == 7) ? WS_SP : WS_YP) + h * 16384 + j * 32;
      const unsigned xo = __builtin_amdgcn_raw_buffer_load_b32(rx, (lane & 1) * 4, h * 32 + j * 8, 0);
      const bool skip3 = (w == 6) && (lane >= 32);
      v4u p[4]; int spins = 0; unsigned bad;
      wait_until(t_pv + DLY_F);
      do {
        FENCE();
#pragma unroll
        for (int n = 0; n < 4; ++n) p[n] = ldsc(rs, off, so + n * 4096);
        if (skip3) p[3] = v4u{0u, tv, 0u, tv};
        bad = 0;
#pragma unroll
        for (int n = 0; n < 4; ++n) bad |= (p[n].y ^ tv) | (p[n].w ^ tv);
        PIN(bad);
      } while (!all_ok(bad == 0) && ++spins < SPIN_CAP);
      float a0 = (asf(p[0].x) + asf(p[1].x)) + (asf(p[2].x) + asf(p[3].x));
      float a1 = (asf(p[0].z) + asf(p[1].z)) + (asf(p[2].z) + asf(p[3].z));
      a0 += dpp<0x128>(a0); a1 += dpp<0x128>(a1);
      a0 += dpp<0x124>(a0); a1 += dpp<0x124>(a1);
      a0 += dpp<0x122>(a0); a1 += dpp<0x122>(a1);
      a0 = rbf(add32(add16(a0))); a1 = rbf(add32(add16(a1)));
      if (w == 6) {
        a0 = rbf(a0 * rcp(1.0f + ex2(-1.4426950408889634f * a0)));
        a1 = rbf(a1 * rcp(1.0f + ex2(-1.4426950408889634f * a1)));
        if (lane < 2) *lp<f2>(1024 + lane * 8) = f2{a0, a1};
        STAMP(9);
        BAR();
      } else {
        BAR();
        const f2 gt = *lp<f2>(1024 + (lane & 1) * 8);
        const unsigned o = pk(bflo(xo) + rbf(a0 * gt.x), bfhi(xo) + rbf(a1 * gt.y));
        if (lane < 2) *(GAS unsigned*)(out + 16 * h + 4 * j + 2 * lane) = o;
        if (ai == 0 && lane == 0) *(GAS unsigned*)ws = tag;
        STAMP(10);
      }
    }
  }
  STAMP_OUT();
}

extern "C" void kernel_launch(void* const* d_in, const int* in_sizes, int n_in, void* d_out, int out_size, void* d_ws, size_t ws_size, hipStream_t stream) {
  (void)hipFuncSetAttribute((const void*)mla_decode, hipFuncAttributeMaxDynamicSharedMemorySize, LDS_BYTES);
  mla_decode<<<256, 512, LDS_BYTES, stream>>>((const u16*)d_in[0], (const u16*)d_in[2], (unsigned char*)d_ws + WS_BASE(ws_size), (const u16*)d_in[3], (const u16*)d_in[1], (const u16*)d_in[4], (const u16*)d_in[5],
                                              (const u16*)d_in[7], (const u16*)d_in[8], (const u16*)d_in[9], (u16*)d_out, d_in[0]);
}
```

```cpp
#define PADA 1
#define PADI 12
#include <hip/hip_runtime.h>
#include <stdint.h>

typedef unsigned short u16;
typedef unsigned long long u64;
typedef unsigned v4u __attribute__((ext_vector_type(4)));
typedef unsigned v2u __attribute__((ext_vector_type(2)));
typedef unsigned v8u __attribute__((ext_vector_type(8)));
typedef unsigned v16u __attribute__((ext_vector_type(16)));
typedef float f4 __attribute__((ext_vector_type(4)));
typedef float f2 __attribute__((ext_vector_type(2)));
typedef __bf16 bf8 __attribute__((ext_vector_type(8)));
typedef __bf16 bf2 __attribute__((ext_vector_type(2)));
typedef short s4 __attribute__((ext_vector_type(4)));
#define GAS __attribute__((address_space(1)))
#define LAS __attribute__((address_space(3)))
#define CAS __attribute__((address_space(4)))

#define DELAY_A 0
#define DELAY_Q 300
#define DELAY_O 50

#define SLEEP_H1 8
#define DELAY_F 200
#define WO_NT 0
#define WS_BASE(n) 0
#define DELAY_G 400
#define DELAY_U 0
#define DELAY_H 240
#define ROLE_A_FIRST 1
#define CLEAR_MODE 0
#define FFN_MODE 0
#define DELAY_C 0
constexpr int WS_FL = 1024;
constexpr int WS_FL2 = 2048;
constexpr int WS_QP = 4096;
constexpr int WS_QA = WS_QP + 131072;
constexpr int WS_FP = WS_QA + 32768;
constexpr int WS_ST = WS_FP + 131072;
constexpr int WS_SP = WS_ST + 262144;
constexpr int WS_YP = WS_SP + 524288;
constexpr int WS_OP = WS_YP + 524288;
constexpr int WS_END = WS_OP + 4194304;
constexpr int WS_STAMP = 6 << 20;
constexpr int WS_TUNE = 64;
constexpr int LDS_BYTES = 163840;
constexpr int CODE_BYTES = 16128;
constexpr int SPIN_CAP = 1 << 16;
constexpr int AUX_SC1 = 16, AUX_NT = 2;
constexpr int QAI = 131072;
constexpr unsigned EMPTY = 0xFFFFFFFFu;

#define BAR() asm volatile("s_waitcnt lgkmcnt(0)\n\ts_barrier" ::: "memory")
#define FENCE() asm volatile("" ::: "memory")
#define SCHED() __builtin_amdgcn_sched_barrier(0)
#define VMWAIT(n) asm volatile("s_waitcnt vmcnt(" #n ")" ::: "memory")
#define PIN(x) asm volatile("" : "+v"(x))
#define PINS(x) asm volatile("" : "+s"(x))

#define STAMP(i) do { if ((i) == PADI && isA == PADA) tpad_ = __builtin_amdgcn_s_memrealtime(); } while (0)
#define STAMP_OUT() { if (tpad_ && (!isA || w >= 6 || PADI == 1)) wait_until(tpad_ + 3000); }

typedef __amdgpu_buffer_rsrc_t rsrc_t;
__device__ __forceinline__ float bflo(unsigned u) { return __uint_as_float(u << 16); }
__device__ __forceinline__ float bfhi(unsigned u) { return __uint_as_float(u & 0xFFFF0000u); }
__device__ __forceinline__ unsigned pk(float a, float b) { f2 f = {a, b}; return __builtin_bit_cast(unsigned, __builtin_convertvector(f, bf2)); }
__device__ __forceinline__ unsigned pkc(float a, float b) { return min(pk(a, b), EMPTY - 1u); }
__device__ __forceinline__ float rbf(float a) { return bflo(pk(a, a)); }
__device__ __forceinline__ float asf(unsigned u) { return __uint_as_float(u); }
__device__ __forceinline__ unsigned asu(float f) { return __float_as_uint(f); }
__device__ __forceinline__ float ex2(float x) { return __builtin_amdgcn_exp2f(x); }
__device__ __forceinline__ float rcp(float x) { return __builtin_amdgcn_rcpf(x); }
__device__ __forceinline__ f4 mfma(v4u a, v4u b, f4 c) {
  return __builtin_amdgcn_mfma_f32_16x16x32_bf16(__builtin_bit_cast(bf8, a), __builtin_bit_cast(bf8, b), c, 0, 0, 0);
}
template <int CTRL> __device__ __forceinline__ float dpp(float v) {
  return __builtin_bit_cast(float, __builtin_amdgcn_update_dpp(0, __builtin_bit_cast(int, v), CTRL, 0xf, 0xf, true));
}
template <int CTRL> __device__ __forceinline__ unsigned dppu(unsigned v) { return (unsigned)__builtin_amdgcn_update_dpp(0, (int)v, CTRL, 0xf, 0xf, true); }
__device__ __forceinline__ float add16(float v) { unsigned u = asu(v); auto r = __builtin_amdgcn_permlane16_swap(u, u, false, false); return asf(r[0]) + asf(r[1]); }
__device__ __forceinline__ float add32(float v) { unsigned u = asu(v); auto r = __builtin_amdgcn_permlane32_swap(u, u, false, false); return asf(r[0]) + asf(r[1]); }
__device__ __forceinline__ float fmx(float a, float b) { return __builtin_fmaxf(a, b); }
__device__ __forceinline__ float mx3(float a, float b, float c) { return fmx(fmx(a, b), c); }
__device__ __forceinline__ float max16(float v) { unsigned u = asu(v); auto r = __builtin_amdgcn_permlane16_swap(u, u, false, false); return fmx(asf(r[0]), asf(r[1])); }
__device__ __forceinline__ float max32(float v) { unsigned u = asu(v); auto r = __builtin_amdgcn_permlane32_swap(u, u, false, false); return fmx(asf(r[0]), asf(r[1])); }
__device__ __forceinline__ float wave_sum(float v) {
  v += dpp<0x128>(v); v += dpp<0x124>(v); v += dpp<0x122>(v); v += dpp<0x121>(v);
  return add32(add16(v));
}
__device__ __forceinline__ unsigned umax4(unsigned acc, v4u v) { return max(max(max(acc, v.x), v.y), max(v.z, v.w)); }
__device__ __forceinline__ rsrc_t mkrs(const void* p, int bytes) { return __builtin_amdgcn_make_buffer_rsrc((void*)p, 0, bytes, 0x00020000); }
__device__ __forceinline__ v4u mksrd(const void* p, unsigned bytes) { const u64 a = (u64)p; return v4u{(unsigned)a, (unsigned)(a >> 32) & 0xFFFFu, bytes, 0x00020000u}; }
__device__ __forceinline__ v4u ldnt(rsrc_t r, int voff, int soff) { return __builtin_amdgcn_raw_buffer_load_b128(r, voff, soff, AUX_NT); }
__device__ __forceinline__ v4u ldsc(rsrc_t r, int voff, int soff) { return __builtin_amdgcn_raw_buffer_load_b128(r, voff, soff, AUX_SC1); }
__device__ __forceinline__ void stsc(v4u v, rsrc_t r, int voff, int soff) { __builtin_amdgcn_raw_buffer_store_b128(v, r, voff, soff, AUX_SC1); }
__device__ __forceinline__ void dma16(v4u srd, int voff, int soff, int lds_dst) {
  asm volatile("s_mov_b32 m0, %3\n\ts_nop 0\n\tbuffer_load_dwordx4 %0, %1, %2 offen nt lds" :: "v"(voff), "s"(srd), "s"(soff), "s"(lds_dst) : "memory");
}
__device__ __forceinline__ void dma16p(v4u srd, int voff, int soff, int lds_dst) {
  asm volatile("s_mov_b32 m0, %3\n\ts_nop 0\n\tbuffer_load_dwordx4 %0, %1, %2 offen lds" :: "v"(voff), "s"(srd), "s"(soff), "s"(lds_dst) : "memory");
}
__device__ __forceinline__ void dma16sc(v4u srd, int voff, int soff, int lds_dst) {
  asm volatile("s_mov_b32 m0, %3\n\ts_nop 0\n\tbuffer_load_dwordx4 %0, %1, %2 offen sc1 lds" :: "v"(voff), "s"(srd), "s"(soff), "s"(lds_dst) : "memory");
}
template <typename T> __device__ __forceinline__ LAS T* lp(int off) { return (LAS T*)(uintptr_t)(unsigned)off; }
__device__ __forceinline__ v4u lds128(int off) { return *lp<v4u>(off); }
__device__ __forceinline__ v2u ldstr(int off) { return __builtin_bit_cast(v2u, __builtin_amdgcn_ds_read_tr16_b64_v4i16(lp<s4>(off))); }
__device__ __forceinline__ int swz(int r) { return ((r & 7) << 1) | ((r >> 3) & 1); }
__device__ __forceinline__ v4u xmask(unsigned xx, const unsigned (&mk)[4]) { return v4u{xx & mk[0], xx & mk[1], xx & mk[2], xx & mk[3]}; }
__device__ __forceinline__ bool all_ok(bool ok) { return __builtin_amdgcn_ballot_w64(!ok) == 0ull; }
__device__ __forceinline__ void wait_until(u64 t) { while ((long long)(__builtin_amdgcn_s_memrealtime() - t) < 0) __builtin_amdgcn_s_sleep(1); }

__device__ __forceinline__ void ln_stats(v4u xa, float& mb, float& ri) {
  float v[8] = {bflo(xa.x), bfhi(xa.x), bflo(xa.y), bfhi(xa.y), bflo(xa.z), bfhi(xa.z), bflo(xa.w), bfhi(xa.w)};
  const float mean = wave_sum(((v[0] + v[1]) + (v[2] + v[3])) + ((v[4] + v[5]) + (v[6] + v[7]))) * (1.0f / 512.0f);
  float q0 = 0.f, q1 = 0.f;
#pragma unroll
  for (int i = 0; i < 4; ++i) { const float d = v[i] - mean, e = v[i + 4] - mean; q0 += d * d; q1 += e * e; }
  const float var = wave_sum(q0 + q1) * (1.0f / 512.0f);
  mb = rbf(mean);
  ri = rcp(rbf(__builtin_amdgcn_sqrtf(rbf(rbf(var) + 1e-5f))));
}

__device__ __forceinline__ v4u layernorm8(v4u xa) {
  float mb, ri;
  ln_stats(xa, mb, ri);
  v4u o;
#pragma unroll
  for (int i = 0; i < 4; ++i) { const unsigned d = pk(bflo(xa[i]) - mb, bfhi(xa[i]) - mb); o[i] = pk(bflo(d) * ri, bfhi(d) * ri); }
  return o;
}

typedef u64 v8q __attribute__((ext_vector_type(8)));
typedef u64 v4q __attribute__((ext_vector_type(4)));
__global__ void __launch_bounds__(512, 2) mla_decode(const void*, const void*, void*, const void*, const void*, const void*, const void*, const void*,
                                                      const void*, const void*, void*, const void*) {
  v8q ka0; v4q ka1; u64 t_start;
  asm volatile("s_load_dwordx16 %0, %3, 0x0\n\ts_load_dwordx8 %1, %3, 0x40\n\ts_memrealtime %2" : "=&s"(ka0), "=&s"(ka1), "=&s"(t_start) : "s"(__builtin_amdgcn_kernarg_segment_ptr()));
  u64 tpad_ = 0;
  const int b = blockIdx.x, tid = threadIdx.x, lane = tid & 63, w = __builtin_amdgcn_readfirstlane(tid >> 6);
  const int xcd = b & 7, rr = (b >> 3) & 15;
  const bool isA = b < 128;
  const int r16 = lane & 15, g = lane >> 4;
  const int h = xcd * 4 + (rr & 3), j = rr >> 2;
  const int ai = rr * 8 + xcd;
  {
    u64 pc; asm volatile("s_getpc_b64 %0" : "=s"(pc));
    asm volatile("s_mov_b32 m0, %2\n\ts_nop 0\n\tglobal_load_lds_dword %0, %1" :: "v"(min(((b >> 3) * 8 + w) * 128, CODE_BYTES - 128) + r16 * 8), "s"(pc & ~255ull), "s"(114688) : "memory");
  }
#define COMMON_CONSTS \
  const int dchunk = (r16 ^ swz(4 * (w & 3) + g)) * 16;     \
  const int trrow = 4 * g + (r16 >> 2); \
  const int trbase = trrow * 256 + ((((r16 >> 1) & 1) ^ swz(trrow)) * 16) + 8 * (r16 & 1);     \
  const int rbase = r16 * 256 + ((4 * g) ^ swz(r16)) * 16;                                      \
  const int trbaseK = trbase + trrow * 768, rbaseK = rbase + r16 * 768;                         \
  const int vrow0 = g * 256 + (r16 ^ (2 * w)) * 16, vrow1 = vrow0 ^ 16;                         \
  unsigned mk[4]; \
  _Pragma("unroll") for (int k = 0; k < 4; ++k) mk[k] = ((r16 >> 1) == k) ? ((r16 & 1) ? 0xFFFF0000u : 0x0000FFFFu) : 0u;

  const int cons = h * 4 + j;
  v4u wqv[8], xa, xb;
  int oA = (128 * j + 32 * (w & 3) + 8 * g) * 8192 + h * 256 + r16 * 16, oq[4], ox = (w == 2) ? lane * 16 : (128 * j + 32 * (w & 3) + 8 * g) * 2, oc = WS_QA + cons * 256 + lane * 4;
  unsigned em = EMPTY;
  {
#pragma unroll
    for (int n = 0; n < 4; ++n) { oq[n] = (128 * j + 16 * w + 4 * n + g) * 8192 + h * 256 + (r16 ^ swz(4 * n + g)) * 16; PIN(oq[n]); }
    PIN(oA); PIN(ox); PIN(oc); PIN(em);
    SCHED();
    asm volatile("s_waitcnt lgkmcnt(0)" : "+s"(ka0), "+s"(ka1), "+s"(t_start));
  }
  const u16 *x = (const u16*)ka0[0], *wq = (const u16*)ka0[1], *wuk = (const u16*)ka0[3], *ckv = (const u16*)ka0[4], *wuv = (const u16*)ka0[5], *wo = (const u16*)ka0[6],
            *wg = (const u16*)ka0[7], *wu = (const u16*)ka1[0], *wd = (const u16*)ka1[1];
  unsigned char* ws = (unsigned char*)ka0[2];
  u16* out = (u16*)ka1[2];
  {
    int b2 = b;
    PINS(b2);
    if (b2 >= 128) {
      if (w == 0) asm volatile("global_store_dword %0, %1, %2 sc1" :: "v"(oc), "v"(em), "s"(ws) : "memory");
#pragma unroll
      for (int n = 0; n < 4; ++n)
        asm volatile("s_mov_b32 m0, %2\n\ts_nop 0\n\tglobal_load_lds_dwordx4 %0, %1 nt" :: "v"(oq[n]), "s"(wuk), "s"(QAI + w * 4096 + n * 1024) : "memory");
    } else {
    if (w == 2) asm volatile("global_load_dwordx4 %0, %1, %2" : "=&v"(xa) : "v"(ox), "s"(x) : "memory");
    if (w >= 4) asm volatile("global_load_dwordx4 %0, %1, %2" : "=&v"(xb) : "v"(ox), "s"(x) : "memory");
    if (w >= 4) {
      const v4u sq = mksrd(wq, 4194304);
      asm volatile("buffer_load_dwordx4 %0, %8, %9, 0 offen nt\n\tbuffer_load_dwordx4 %1, %8, %9, %10 offen nt\n\tbuffer_load_dwordx4 %2, %8, %9, %11 offen nt\n\tbuffer_load_dwordx4 %3, %8, %9, %12 offen nt\n\t"
                   "buffer_load_dwordx4 %4, %8, %9, %13 offen nt\n\tbuffer_load_dwordx4 %5, %8, %9, %14 offen nt\n\tbuffer_load_dwordx4 %6, %8, %9, %15 offen nt\n\tbuffer_load_dwordx4 %7, %8, %9, %16 offen nt"
                   : "=&v"(wqv[0]), "=&v"(wqv[1]), "=&v"(wqv[2]), "=&v"(wqv[3]), "=&v"(wqv[4]), "=&v"(wqv[5]), "=&v"(wqv[6]), "=&v"(wqv[7])
                   : "v"(oA), "s"(sq), "s"(8192), "s"(16384), "s"(24576), "s"(32768), "s"(40960), "s"(49152), "s"(57344) : "memory");
    }
    asm volatile("s_barrier" ::: "memory");
    }
  }
#define DLY_G DELAY_G
#define DLY_H DELAY_H
#define DLY_U DELAY_U
#define DLY_A DELAY_A
#define DLY_Q DELAY_Q
#define DLY_O DELAY_O
#define DLY_F DELAY_F
  if (!isA) {
    STAMP(9);
    const int xrow0 = 128 * j + 16 * w;
    const unsigned tag = *(const CAS unsigned*)ws + 1u;
    const rsrc_t rs = mkrs(ws, WS_END);
    COMMON_CONSTS
    int po = lane * 16, a_wk = QAI + w * 4096 + rbase, a_qw = 73728 + lane * 4, a_qr = 73728 + g * 64, a_qa = 74240 + w * 32 + g * 8;
    unsigned tv = tag;
#pragma unroll
    for (int k = 0; k < 4; ++k) PIN(mk[k]);
    PIN(po); PIN(a_wk); PIN(a_qw); PIN(a_qr); PIN(a_qa); PIN(tv);
    if (w == 0) {
      VMWAIT(4);
      if (lane == 0) __builtin_amdgcn_raw_buffer_store_b64(v2u{tv, tv}, rs, 0, WS_FL + cons * 8, AUX_SC1);
      STAMP(10);
    }
    if (w == 2) *lp<v4u>(65536 + po) = layernorm8(*(const GAS v4u*)(x + 8 * lane));
    VMWAIT(0);
    BAR();
    STAMP(13);
    v4u wkv[4], xm[4];
#pragma unroll
    for (int s = 0; s < 4; ++s) wkv[s] = lds128(a_wk ^ (s * 16));
    {
      const v2u x4 = *lp<v2u>(65536 + (xrow0 + 4 * g) * 2);
      xm[0] = xmask(__builtin_amdgcn_perm(x4.x, x4.x, 0x01000100u), mk); xm[1] = xmask(__builtin_amdgcn_perm(x4.x, x4.x, 0x03020302u), mk);
      xm[2] = xmask(__builtin_amdgcn_perm(x4.y, x4.y, 0x01000100u), mk); xm[3] = xmask(__builtin_amdgcn_perm(x4.y, x4.y, 0x03020302u), mk);
#pragma unroll
      for (int i = 0; i < 4; ++i) { PIN(xm[i]); PIN(wkv[i]); }
    }
    if (w == 1) {
      v4u p[4]; int spins = 0; unsigned bad;
      wait_until(t_start + DLY_H);
      for (;;) {
        FENCE();
#pragma unroll
        for (int i = 0; i < 4; ++i) p[i] = ldsc(rs, po + i * 1024, WS_QP + h * 4096);
        bad = 0;
#pragma unroll
        for (int i = 0; i < 4; ++i) bad |= (p[i].y ^ tv) | (p[i].w ^ tv);
        PIN(bad);
        if (all_ok(bad == 0) || ++spins >= SPIN_CAP) break;
        __builtin_amdgcn_s_sleep(1);
      }
      const float q0 = (asf(p[0].x) + asf(p[1].x)) + (asf(p[2].x) + asf(p[3].x));
      const float q1 = (asf(p[0].z) + asf(p[1].z)) + (asf(p[2].z) + asf(p[3].z));
      *lp<unsigned>(a_qw) = pk(q0, q1);
    }
    BAR();
    STAMP(2);
    {
      v4u qf[4];
#pragma unroll
      for (int s = 0; s < 4; ++s) qf[s] = lds128(a_qr + s * 16);
      SCHED();
      f4 acc = {0.f, 0.f, 0.f, 0.f};
#pragma unroll
      for (int s = 0; s < 4; ++s) acc = mfma(wkv[s], qf[s], acc);
      const float sc = 0.08838834764831845f * 1.4426950408889634f;
      if (r16 == 0) *lp<v2u>(a_qa) = v2u{pkc(acc[0] * sc, acc[1] * sc), pkc(acc[2] * sc, acc[3] * sc)};
    }
    BAR();
    if (w == 0 && lane < 16) stsc(lds128(74240 + po), rs, po, WS_QA + cons * 256);
    STAMP(3);
    BAR();
    wait_until(t_start + DLY_Q);
    v4u buv[4], bo[4][4];
    {
      const v4u suv = mksrd(wuv, 4194304), so = mksrd(wo, 4194304);
      const int so2 = (h * 128 + 8 * (w >> 2) + (w & 3)) * 1024;
      int tq[4];
#pragma unroll
      for (int t = 0; t < 4; ++t) tq[t] = (w >> 1) * 256 + (trbaseK ^ (32 * (4 * (w & 1) + t)));
#define WO4(n) _Pragma("unroll") for (int i = 0; i < 4; ++i) dma16p(so, (i & 1) ? vrow1 : vrow0, so2 + ((((n) + j) & 3) * 32 + 16 * (i & 1) + 4 * (i >> 1)) * 1024, (32 * (n) + w + 8 * i) * 1024)
#define RD_BO(n) _Pragma("unroll") for (int t = 0; t < 4; ++t) { const v2u lo = ldstr(tq[t] + (n) * 32768), hi = ldstr(tq[t] + (n) * 32768 + 16384); bo[t][n] = v4u{lo.x, lo.y, hi.x, hi.y}; }
      WO4(0);
      asm volatile("s_barrier" ::: "memory");
      WO4(1);
      asm volatile("s_barrier" ::: "memory");
      WO4(2);
      asm volatile("s_waitcnt vmcnt(8)\n\ts_barrier" ::: "memory");
      WO4(3);
      RD_BO(0)
      asm volatile("s_waitcnt vmcnt(8)\n\ts_barrier" ::: "memory");
      STAMP(14);
      wait_until(t_start + DLY_U);
      v4u fw[4];
      {
        const GAS char* pf = (const GAS char*)((r16 < 8) ? wg : wu) + (xrow0 + 4 * g + 1) * 3584 + (h < 28 ? h : 0) * 128 + (r16 & 7) * 16;
        asm volatile("global_load_dwordx4 %0, %4, off offset:-3584 nt\n\tglobal_load_dwordx4 %1, %4, off nt\n\tglobal_load_dwordx4 %2, %4, off offset:3584 nt\n\tglobal_load_dwordx4 %3, %5, off nt"
                     : "=&v"(fw[0]), "=&v"(fw[1]), "=&v"(fw[2]), "=&v"(fw[3]) : "v"(pf), "v"(pf + 7168) : "memory");
      }
      RD_BO(1)
      asm volatile("s_waitcnt vmcnt(4) lgkmcnt(0)\n\ts_barrier" ::: "memory");
      const int vo1 = g * 16 * 8192 + dchunk, so1 = (128 * j + 2 * (w & 3) + (w >> 2)) * 8192 + h * 256;
#pragma unroll 1
      for (int n = 0; n < 4; ++n) dma16(suv, vo1, so1 + (64 * (n >> 1) + 8 * (n & 1)) * 8192, QAI + (32 * n + 4 * w) * 256);
      RD_BO(2)
      RD_BO(3)
      asm volatile("s_waitcnt vmcnt(4)" : "+v"(fw[0]), "+v"(fw[1]), "+v"(fw[2]), "+v"(fw[3]) :: "memory");
      f4 acc = {0.f, 0.f, 0.f, 0.f};
#pragma unroll
      for (int i = 0; i < 4; ++i) acc = mfma(xm[i], fw[i], acc);
      if (lane < 32) *lp<f4>(32768 + w * 512 + r16 * 32 + (lane & 16)) = acc;
      BAR();
      if (w == 0) {
        f2 p[8];
#pragma unroll
        for (int i = 0; i < 8; ++i) p[i] = *lp<f2>(32768 + lane * 8 + i * 512);
        const f2 s = ((p[0] + p[1]) + (p[2] + p[3])) + ((p[4] + p[5]) + (p[6] + p[7]));
        stsc(v4u{asu(s.x), tv, asu(s.y), tv}, rs, po, WS_FP + cons * 1024);
        STAMP(15);
        VMWAIT(1);
      } else {
        VMWAIT(0);
      }
      asm volatile("s_barrier" ::: "memory");
      STAMP(4);
#pragma unroll
      for (int ks = 0; ks < 4; ++ks) {
        const int a = QAI + (trbase ^ (32 * w)) + ks * 8192;
        const v2u lo = ldstr(a), hi = ldstr(a + 4096);
        buv[ks] = v4u{lo.x, lo.y, hi.x, hi.y};
      }
    }
    int a_st = (4 * g + (r16 & 3)) * 512, a_od = g * 1024 + r16 * 16, a_w1 = w * 272 + r16 * 16 + (g & 1) * 8, a_r1 = (r16 & 7) * 272 + g * 16,
        a_w2 = 8192 + ((16 * w + r16 - 32 * j) & 127) * 2, a_r2 = 8192 + g * 16, a_o = g * 16384 + r16 * 8;
    PIN(a_st); PIN(a_od); PIN(a_w1); PIN(a_r1); PIN(a_w2); PIN(a_r2); PIN(a_o);
    STAMP(5);
    v4u st, od[4];
    {
      int spins = 0;
      do {
        FENCE();
        st = ldsc(rs, a_st, WS_ST + w * 8192 + h * 16);
      } while (!all_ok((st.y == tv) & (st.w == tv)) && ++spins < SPIN_CAP);
      const u64 t_st = __builtin_amdgcn_s_memrealtime() + (spins ? DLY_O : 0);
      STAMP(6);
      const float m = asf(st.x);
      float mw = fmx(m, dpp<0xB1>(m)); mw = fmx(mw, dpp<0x4E>(mw)); mw = max32(max16(mw));
      float lw = ex2(m - mw) * asf(st.z);
      lw += dpp<0xB1>(lw); lw += dpp<0x4E>(lw); lw = add32(add16(lw));
      if (lane == 0) *lp<f2>(4096 + w * 8) = f2{mw, lw};
      wait_until(t_st);
      FENCE();
#pragma unroll
      for (int i = 0; i < 4; ++i) od[i] = ldsc(rs, a_od + i * 256, WS_OP + (cons * 128 + 16 * w) * 256);
      BAR();
      const f2 sw = *lp<f2>(4096 + (lane & 7) * 8);
      float M = fmx(sw.x, dpp<0xB1>(sw.x)); M = fmx(M, dpp<0x4E>(M)); M = fmx(M, dpp<0x141>(M));
      float Lt = ex2(sw.x - M) * sw.y;
      Lt += dpp<0xB1>(Lt); Lt += dpp<0x4E>(Lt); Lt += dpp<0x141>(Lt);
      const float e = ex2(m - M) * rcp(Lt);
      const unsigned wb = pk(e, e);
      xm[0] = xmask(dppu<0x00>(wb), mk); xm[1] = xmask(dppu<0x55>(wb), mk); xm[2] = xmask(dppu<0xAA>(wb), mk); xm[3] = xmask(dppu<0xFF>(wb), mk);
#pragma unroll
      for (int i = 0; i < 4; ++i) PIN(xm[i]);
      SCHED();
    }
    {
      int spins = 0; f4 acc;
      for (;;) {
        unsigned bad = 0;
        acc = f4{0.f, 0.f, 0.f, 0.f};
#pragma unroll
        for (int i = 0; i < 4; ++i) { acc = mfma(xm[i], od[i], acc); bad = umax4(bad, od[i]); }
        if (all_ok(bad != EMPTY) || ++spins >= SPIN_CAP) break;
        FENCE();
#pragma unroll
        for (int i = 0; i < 4; ++i) od[i] = ldsc(rs, a_od + i * 256, WS_OP + (cons * 128 + 16 * w) * 256);
      }
      if (lane < 32) *lp<v2u>(a_w1) = v2u{pk(acc[0], acc[1]), pk(acc[2], acc[3])};
    }
    STAMP(7);
    BAR();
    {
      v4u af[4];
#pragma unroll
      for (int ks = 0; ks < 4; ++ks) af[ks] = lds128(a_r1 + ks * 64);
      SCHED();
      f4 acc = {0.f, 0.f, 0.f, 0.f};
#pragma unroll
      for (int ks = 0; ks < 4; ++ks) acc = mfma(af[ks], buv[ks], acc);
      const float c = add16((acc[0] + acc[1]) + (acc[2] + acc[3]));
      if (lane < 16) *lp<u16>(a_w2) = (u16)pk(c, c);
    }
    BAR();
    {
      v4u cf[4];
#pragma unroll
      for (int ks = 0; ks < 4; ++ks) cf[ks] = lds128(a_r2 + ks * 64);
      SCHED();
      f4 acc[4];
#pragma unroll
      for (int t = 0; t < 4; ++t) acc[t] = f4{0.f, 0.f, 0.f, 0.f};
#pragma unroll
      for (int ks = 0; ks < 4; ++ks)
#pragma unroll
        for (int t = 0; t < 4; ++t) acc[t] = mfma(cf[ks], bo[t][ks], acc[t]);
      const float v01 = (g & 1) ? acc[1][0] : acc[0][0], v23 = (g & 1) ? acc[3][0] : acc[2][0];
      const float v = (g & 2) ? v23 : v01;
      __builtin_amdgcn_raw_buffer_store_b64(v2u{asu(v), tv}, rs, a_o, WS_SP + (4 * w * 128 + cons) * 128, AUX_SC1);
    }
    STAMP(8);
    STAMP_OUT();
    __builtin_amdgcn_endpgm();
  }
  {
    unsigned tagA;
    asm volatile("s_load_dword %0, %1, 0x0" : "=&s"(tagA) : "s"(ws));
    const v4u sws = mksrd(ws, WS_END);
    int po = lane * 16;
    const int ht = w & 1, kt = w >> 1;
    v4u k0[16], k1[16];
    v2u v0[4][4];
    if (w < 2) {
      const v4u skv = mksrd(ckv, 16777216);
      const int so = (128 * ai + w) * 1024, vb = po ^ (w * 32);
      for (int i = 0; i < (int)(DLY_A); ++i) __builtin_amdgcn_s_sleep(1);
#pragma unroll 1
      for (int i = 0; i < 8; ++i) {
        const int blk = ((2 * i) & 7) + (i >> 2);
        if (i == 2) asm volatile("s_barrier" ::: "memory");
        if (i == 6) asm volatile("s_waitcnt vmcnt(16)\n\ts_barrier" ::: "memory");
#pragma unroll
        for (int r = 0; r < 8; ++r) dma16(skv, vb ^ ((r & 3) * 64 + (r >> 2) * 16), so + blk * 16384 + r * 2048, w * 1024 + blk * 16384 + r * 2048);
      }
      STAMP(15);
    }
    SCHED();
    const rsrc_t rx = mkrs(x, 1024), rs = mkrs(ws, WS_END);
    COMMON_CONSTS
#pragma unroll
    for (int k = 0; k < 4; ++k) PIN(mk[k]);
    asm volatile("s_waitcnt lgkmcnt(0)" : "+s"(tagA));
    const unsigned tag = tagA + 1u;
    unsigned tv = tag;
    PIN(tv);
    v4u xn = {0u, 0u, 0u, 0u};
    if (w >= 4) {
      unsigned mk2[4];
      v4u xm[8];
#pragma unroll
      for (int k = 0; k < 4; ++k) { mk2[k] = mk[k] | dppu<0x128>(mk[k]); PIN(mk2[k]); }
      int a_w0 = QAI + (w - 4) * 1024 + (g >> 1) * 512 + r16 * 32 + (lane & 16);
      PIN(a_w0);
      asm volatile("s_waitcnt vmcnt(8)" : "+v"(xb) :: "memory");
#pragma unroll
      for (int k = 0; k < 4; ++k) {
        const unsigned xp = (r16 >= 8) ? 0x3F803F80u : xb[k];
        xm[2 * k] = xmask(__builtin_amdgcn_perm(xp, xp, 0x01000100u), mk2); xm[2 * k + 1] = xmask(__builtin_amdgcn_perm(xp, xp, 0x03020302u), mk2);
        PIN(xm[2 * k]); PIN(xm[2 * k + 1]);
      }
      SCHED();
      f4 acc = {0.f, 0.f, 0.f, 0.f};
      asm volatile("s_waitcnt vmcnt(2)" : "+v"(wqv[0]), "+v"(wqv[1]), "+v"(wqv[2]), "+v"(wqv[3]), "+v"(wqv[4]), "+v"(wqv[5]) :: "memory");
#pragma unroll
      for (int i = 0; i < 6; ++i) acc = mfma(xm[i], wqv[i], acc);
      SCHED();
      asm volatile("s_waitcnt vmcnt(0)" : "+v"(wqv[6]), "+v"(wqv[7]) :: "memory");
      acc = mfma(xm[6], wqv[6], acc); acc = mfma(xm[7], wqv[7], acc);
      STAMP(12);
      *lp<f4>(a_w0) = acc;
      BAR();
    } else if (w == 2) {
      __builtin_amdgcn_s_setprio(3);
      float mb, ri;
      asm volatile("s_waitcnt vmcnt(0)" : "+v"(xa) :: "memory");
      ln_stats(xa, mb, ri);
      int a_qp = ((lane & 31) * 2 + (lane >> 5)) * 16, a_r0 = QAI + po;
      PIN(mb); PIN(ri); PIN(a_qp); PIN(a_r0);
      BAR();
      f4 p[4];
#pragma unroll
      for (int i = 0; i < 4; ++i) p[i] = *lp<f4>(a_r0 + i * 1024);
      const f4 s = (p[0] + p[1]) + (p[2] + p[3]);
      const auto r0 = __builtin_amdgcn_permlane32_swap(asu(s[0]), asu(s[2]), false, false), r1 = __builtin_amdgcn_permlane32_swap(asu(s[1]), asu(s[3]), false, false);
      stsc(v4u{asu((asf(r0[0]) - mb * asf(r0[1])) * ri), tv, asu((asf(r1[0]) - mb * asf(r1[1])) * ri), tv}, rs, a_qp, WS_QP + cons * 1024);
      STAMP(1);
      __builtin_amdgcn_s_setprio(0);
    } else if (w == 3) {
      BAR();
    }
    const int mi = ai & 3, fi = (ai < 112) ? (ai >> 2) : 0;
    int ka[4], ta[4], qa_[4], vo0 = vrow0, vo1 = vrow1;
#pragma unroll
    for (int s = 0; s < 4; ++s) {
      ka[s] = kt * 32768 + (rbaseK ^ (s * 16)); ta[s] = (w >> 1) * 256 + (trbaseK ^ (32 * (4 * (w & 1) + s))); qa_[s] = ka[s] + (QAI + ht * 16384 - kt * 32768);
      PIN(ka[s]); PIN(ta[s]); PIN(qa_[s]);
    }
    int a_d = (2 * g + (r16 >> 3)) * 1024 + (r16 & 7) * 16, a_mw = (16 * ht + r16) * 16 + kt * 4,
        a_s = (lane < 16) ? (16 * ht + r16) * 16 : WS_END, a_o = (16 * g + (w >> 1)) * 32768 + (w & 1) * 128 + r16 * 8, a_pq = QAI + po,
        a_c = (16 * w + g) * 32768 + r16 * 16;
    PIN(a_d); PIN(a_mw); PIN(a_s); PIN(a_o); PIN(a_pq); PIN(a_c); PIN(vo0); PIN(vo1);
#define RD_U0() _Pragma("unroll") for (int i = 0; i < 4; ++i) _Pragma("unroll") for (int s = 0; s < 4; ++s) { k0[4 * i + s] = lds128(ka[s] + i * 256); v0[i][s] = ldstr(ta[s] + i * 32768); }
#define RD_K1() _Pragma("unroll") for (int s = 0; s < 16; ++s) k1[s] = lds128(ka[s & 3] + (s >> 2) * 256 + 16384)
#define FLAGS_OK() all_ok(((fl.x ^ tv) | (fl.y ^ tv) | (fl.z ^ tv) | (fl.w ^ tv)) == 0)
#define QA4() _Pragma("unroll") for (int q = 0; q < 4; ++q) dma16sc(sws, (q & 1) ? vo1 : vo0, WS_QA + (w + 8 * q) * 1024, QAI + (w + 8 * q) * 1024)
    if (w >= 2) {
      v4u fl;
      if (w == 3) {
        __builtin_amdgcn_s_setprio(3);
        asm volatile("s_waitcnt vmcnt(0)\n\tbuffer_load_dwordx4 %0, %1, %2, %3 offen sc1" : "=&v"(fl) : "v"(po), "s"(sws), "s"(WS_FL) : "memory");
      }
      asm volatile("s_barrier" ::: "memory");
      RD_U0()
      if (w == 3) {
        asm volatile("s_waitcnt vmcnt(0)" : "+v"(fl) :: "memory");
        int spins = 0;
        while (!FLAGS_OK() && ++spins < SPIN_CAP)
          asm volatile("buffer_load_dwordx4 %0, %1, %2, %3 offen sc1\n\ts_waitcnt vmcnt(0)" : "=&v"(fl) : "v"(po), "s"(sws), "s"(WS_FL) : "memory");
        __builtin_amdgcn_s_setprio(0);
      }
    }
    asm volatile("s_waitcnt vmcnt(16)\n\ts_barrier" ::: "memory");
    STAMP(2);
    const bool early = (long long)(__builtin_amdgcn_s_memrealtime() - (t_start + DLY_G)) >= 0;
    if (early) { QA4(); }
    if (w < 2) { RD_U0() }
    if (kt < 2) { RD_K1(); }
    if (!early) { wait_until(t_start + DLY_G); QA4(); }
    VMWAIT(4);
    BAR();
    STAMP(3);
    if (kt >= 2) { RD_K1(); }
    for (int spins = 0;; ++spins) {
      VMWAIT(0);
      unsigned bad = 0;
#pragma unroll
      for (int q = 0; q < 4; ++q) bad = umax4(bad, lds128(a_pq + w * 1024 + q * 8192));
      if (all_ok(bad != EMPTY) || spins >= SPIN_CAP) break;
      QA4();
    }
    BAR();
    STAMP(4);
    f4 S0 = {0.f, 0.f, 0.f, 0.f}, S1 = {0.f, 0.f, 0.f, 0.f};
    v4u vf[4][4];
    {
      v4u qf[16];
#pragma unroll
      for (int s = 0; s < 4; ++s) qf[s] = lds128(qa_[s]);
      SCHED();
#pragma unroll
      for (int s = 0; s < 16; ++s) {
        if (s < 12) qf[s + 4] = lds128(qa_[s & 3] + ((s + 4) >> 2) * 256);
        { const v2u hi = ldstr(ta[s & 3] + (s >> 2) * 32768 + 16384); vf[s >> 2][s & 3] = v4u{v0[s >> 2][s & 3].x, v0[s >> 2][s & 3].y, hi.x, hi.y}; }
        if (s < 8 && !(s & 1)) stsc(v4u{EMPTY, EMPTY, EMPTY, EMPTY}, rs, a_c, WS_OP + (s >> 1) * 131072 + ai * 256);
        S0 = mfma(k0[s], qf[s], S0); S1 = mfma(k1[s], qf[s], S1);
        SCHED();
      }
      float m0 = mx3(mx3(mx3(S0[0], S0[1], S0[2]), S0[3], S1[0]), S1[1], S1[2]);
      m0 = max32(max16(fmx(m0, S1[3])));
      if (lane < 16) *lp<float>(a_mw) = m0;
    }
    BAR();
    STAMP(5);
    float M;
    {
      const f4 m4 = *lp<f4>(a_mw & ~15);
      M = fmx(mx3(m4[0], m4[1], m4[2]), m4[3]);
      float p[8];
#pragma unroll
      for (int i = 0; i < 4; ++i) { p[i] = ex2(S0[i] - M); p[4 + i] = ex2(S1[i] - M); }
      *lp<v4u>(a_pq + w * 1024) = v4u{pk(p[0], p[1]), pk(p[2], p[3]), pk(p[4], p[5]), pk(p[6], p[7])};
      const float l = add32(add16(((p[0] + p[1]) + (p[2] + p[3])) + ((p[4] + p[5]) + (p[6] + p[7]))));
      if (lane < 16) *lp<float>(512 + a_mw) = l;
    }
    VMWAIT(0);
    BAR();
    STAMP(6);
    {
      v4u pf_[8];
      pf_[0] = lds128(a_pq);
      const f4 l4 = *lp<f4>(512 + (a_mw & ~15));
#pragma unroll
      for (int i = 1; i < 8; ++i) pf_[i] = lds128(a_pq + ((i & 3) * 2 + (i >> 2)) * 1024);
      SCHED();
#pragma unroll
      for (int hh = 0; hh < 2; ++hh) {
        f4 O[4];
#pragma unroll
        for (int t = 0; t < 4; ++t) O[t] = f4{0.f, 0.f, 0.f, 0.f};
#pragma unroll
        for (int ks = 0; ks < 4; ++ks) {
#pragma unroll
          for (int t = 0; t < 4; ++t) O[t] = mfma(pf_[4 * hh + ks], vf[ks][t], O[t]);
          if (hh == 0 && ks == 0) { SCHED(); if (kt == 0) stsc(v4u{asu(M), tv, asu((l4[0] + l4[1]) + (l4[2] + l4[3])), tv}, rs, a_s, WS_ST + ai * 512); SCHED(); }
        }
        if (hh == 1) { PIN(O[3]); STAMP(13); }
#pragma unroll
        for (int i = 0; i < 4; ++i)
          __builtin_amdgcn_raw_buffer_store_b64(v2u{pkc(O[0][i], O[1][i]), pkc(O[2][i], O[3][i])}, rs, a_o, WS_OP + (16 * hh + i) * 131072 + ai * 256, AUX_SC1);
      }
    }
    STAMP(7);
    const u64 t_pv = __builtin_amdgcn_s_memrealtime();
    if (ai < 112) {
      const rsrc_t rd = mkrs(wd, 1835008);
      v4u dw[2];
#pragma unroll
      for (int i = 0; i < 2; ++i) dw[i] = ldnt(rd, a_d, (64 * fi + 16 * mi + 8 * i) * 1024 + w * 128);
      STAMP(14);
      v4u p; int spins = 0;
      const int gu = (r16 >> 2) & 1, hi = r16 >> 3;
      const int po2 = (r16 & 3) * 1024 + (64 * gu + 8 * hi + 2 * g) * 8;
      do {
        FENCE();
        p = ldsc(rs, po2, WS_FP + fi * 4096 + mi * 128);
      } while (!all_ok((p.y == tv) & (p.w == tv)) && ++spins < SPIN_CAP);
      float v0_ = asf(p.x), v1_ = asf(p.z);
      v0_ += dpp<0xB1>(v0_); v1_ += dpp<0xB1>(v1_);
      v0_ += dpp<0x4E>(v0_); v1_ += dpp<0x4E>(v1_);
      const float o0 = dpp<0x141>(v0_), o1 = dpp<0x141>(v1_);
      float hv[2];
#pragma unroll
      for (int e = 0; e < 2; ++e) {
        const float gt = rbf(gu ? (e ? o1 : o0) : (e ? v1_ : v0_)), up = rbf(gu ? (e ? v1_ : v0_) : (e ? o1 : o0));
        const float z2 = 2.0f * 0.7978845608028654f * 1.4426950408889634f * (gt + 0.044715f * gt * gt * gt);
        hv[e] = rbf(gt * rcp(1.0f + ex2(-z2))) * up;
      }
      const float t0 = dpp<0x128>(hv[0]), t1 = dpp<0x128>(hv[1]);
      unsigned mk2[4];
#pragma unroll
      for (int k = 0; k < 4; ++k) mk2[k] = mk[k] | dppu<0x128>(mk[k]);
      const float ha = hi ? t1 : hv[0], hb = hi ? hv[1] : t0;
      f4 acc = {0.f, 0.f, 0.f, 0.f};
      acc = mfma(xmask(pk(ha, ha), mk2), dw[0], acc);
      acc = mfma(xmask(pk(hb, hb), mk2), dw[1], acc);
      float y[4];
#pragma unroll
      for (int i = 0; i < 4; ++i) {
        const unsigned u = asu(dpp<0x128>(acc[i]));
        auto r = __builtin_amdgcn_permlane32_swap(u, u, false, false);
        y[i] = acc[i] + asf(r[1]);
      }
      const auto ya = __builtin_amdgcn_permlane32_swap(asu(y[0]), asu(y[2]), false, false), yb = __builtin_amdgcn_permlane32_swap(asu(y[1]), asu(y[3]), false, false);
      if (r16 < 8) stsc(v4u{ya[0], tv, yb[0], tv}, rs, (r16 >> 1) * 16384 + ((r16 & 1) * 8 + 4 * (g & 1) + 2 * (g >> 1)) * 8, WS_YP + (4 * w * 128 + ai) * 128);
    }
    STAMP(8);
    if (w >= 6) {
      const int off = (lane >> 1) * 128 + (lane & 1) * 16, so = ((w == 7) ? WS_SP : WS_YP) + h * 16384 + j * 32;
      const unsigned xo = __builtin_amdgcn_raw_buffer_load_b32(rx, (lane & 1) * 4, h * 32 + j * 8, 0);
      const bool skip3 = (w == 6) && (lane >= 32);
      v4u p[4]; int spins = 0; unsigned bad;
      wait_until(t_pv + DLY_F);
      do {
        FENCE();
#pragma unroll
        for (int n = 0; n < 4; ++n) p[n] = ldsc(rs, off, so + n * 4096);
        if (skip3) p[3] = v4u{0u, tv, 0u, tv};
        bad = 0;
#pragma unroll
        for (int n = 0; n < 4; ++n) bad |= (p[n].y ^ tv) | (p[n].w ^ tv);
        PIN(bad);
      } while (!all_ok(bad == 0) && ++spins < SPIN_CAP);
      float a0 = (asf(p[0].x) + asf(p[1].x)) + (asf(p[2].x) + asf(p[3].x));
      float a1 = (asf(p[0].z) + asf(p[1].z)) + (asf(p[2].z) + asf(p[3].z));
      a0 += dpp<0x128>(a0); a1 += dpp<0x128>(a1);
      a0 += dpp<0x124>(a0); a1 += dpp<0x124>(a1);
      a0 += dpp<0x122>(a0); a1 += dpp<0x122>(a1);
      a0 = rbf(add32(add16(a0))); a1 = rbf(add32(add16(a1)));
      if (w == 6) {
        a0 = rbf(a0 * rcp(1.0f + ex2(-1.4426950408889634f * a0)));
        a1 = rbf(a1 * rcp(1.0f + ex2(-1.4426950408889634f * a1)));
        if (lane < 2) *lp<f2>(1024 + lane * 8) = f2{a0, a1};
        STAMP(9);
        BAR();
      } else {
        BAR();
        const f2 gt = *lp<f2>(1024 + (lane & 1) * 8);
        const unsigned o = pk(bflo(xo) + rbf(a0 * gt.x), bfhi(xo) + rbf(a1 * gt.y));
        if (lane < 2) *(GAS unsigned*)(out + 16 * h + 4 * j + 2 * lane) = o;
        if (ai == 0 && lane == 0) *(GAS unsigned*)ws = tag;
        STAMP(10);
      }
    }
  }
  STAMP_OUT();
}

extern "C" void kernel_launch(void* const* d_in, const int* in_sizes, int n_in, void* d_out, int out_size, void* d_ws, size_t ws_size, hipStream_t stream) {
  (void)hipFuncSetAttribute((const void*)mla_decode, hipFuncAttributeMaxDynamicSharedMemorySize, LDS_BYTES);
  mla_decode<<<256, 512, LDS_BYTES, stream>>>((const u16*)d_in[0], (const u16*)d_in[2], (unsigned char*)d_ws + WS_BASE(ws_size), (const u16*)d_in[3], (const u16*)d_in[1], (const u16*)d_in[4], (const u16*)d_in[5],
                                              (const u16*)d_in[7], (const u16*)d_in[8], (const u16*)d_in[9], (u16*)d_out, d_in[0]);
}
```

```cpp
#include <hip/hip_runtime.h>
#include <stdint.h>

typedef unsigned short u16;
typedef unsigned long long u64;
typedef unsigned v4u __attribute__((ext_vector_type(4)));
typedef unsigned v2u __attribute__((ext_vector_type(2)));
typedef unsigned v8u __attribute__((ext_vector_type(8)));
typedef unsigned v16u __attribute__((ext_vector_type(16)));
typedef float f4 __attribute__((ext_vector_type(4)));
typedef float f2 __attribute__((ext_vector_type(2)));
typedef __bf16 bf8 __attribute__((ext_vector_type(8)));
typedef __bf16 bf2 __attribute__((ext_vector_type(2)));
typedef short s4 __attribute__((ext_vector_type(4)));
#define GAS __attribute__((address_space(1)))
#define LAS __attribute__((address_space(3)))
#define CAS __attribute__((address_space(4)))

#define DELAY_A 0
#define DELAY_Q 370
#define DELAY_O 0

#define DELAY_F 0
#define WS_BASE(n) 0
#define DELAY_G 340
#define DELAY_U 30
#define DELAY_H 190
#define ROLE_A_FIRST 1
#define KPRE 3
#define QD 6
#define DELAY_W 0
constexpr int WS_FL = 1024;
constexpr int WS_FL2 = 2048;
constexpr int WS_QP = 4096;
constexpr int WS_QA = WS_QP + 262144;
constexpr int WS_FP = WS_QA + 32768;
constexpr int WS_ST = WS_FP + 131072;
constexpr int WS_SP = WS_ST + 262144;
constexpr int WS_YP = WS_SP + 524288;
constexpr int WS_OP = WS_YP + 524288;
constexpr int WS_END = WS_OP + 4194304;
constexpr int WS_STAMP = 6 << 20;
constexpr int WS_TUNE = 64;
constexpr int LDS_BYTES = 163840;
constexpr int CODE_BYTES = 14336;
constexpr int SPIN_CAP = 1 << 16;
constexpr int AUX_SC1 = 16, AUX_NT = 2;
constexpr int QAI = 131072;
constexpr int XNI = 65536;
constexpr unsigned EMPTY = 0xFFFFFFFFu;

#define BAR() asm volatile("s_waitcnt lgkmcnt(0)\n\ts_barrier" ::: "memory")
#define FENCE() asm volatile("" ::: "memory")
#define SCHED() __builtin_amdgcn_sched_barrier(0)
#define VMWAIT(n) asm volatile("s_waitcnt vmcnt(" #n ")" ::: "memory")
#define PIN(x) asm volatile("" : "+v"(x))
#define PINS(x) asm volatile("" : "+s"(x))

#define STAMP(i) do {} while (0)
#define STAMP_OUT()

typedef __amdgpu_buffer_rsrc_t rsrc_t;
__device__ __forceinline__ float bflo(unsigned u) { return __uint_as_float(u << 16); }
__device__ __forceinline__ float bfhi(unsigned u) { return __uint_as_float(u & 0xFFFF0000u); }
__device__ __forceinline__ unsigned pk(float a, float b) { f2 f = {a, b}; return __builtin_bit_cast(unsigned, __builtin_convertvector(f, bf2)); }
__device__ __forceinline__ unsigned pkc(float a, float b) { return min(pk(a, b), EMPTY - 1u); }
__device__ __forceinline__ float rbf(float a) { return bflo(pk(a, a)); }
__device__ __forceinline__ float asf(unsigned u) { return __uint_as_float(u); }
__device__ __forceinline__ unsigned asu(float f) { return __float_as_uint(f); }
__device__ __forceinline__ float ex2(float x) { return __builtin_amdgcn_exp2f(x); }
__device__ __forceinline__ float rcp(float x) { return __builtin_amdgcn_rcpf(x); }
__device__ __forceinline__ f4 mfma(v4u a, v4u b, f4 c) {
  return __builtin_amdgcn_mfma_f32_16x16x32_bf16(__builtin_bit_cast(bf8, a), __builtin_bit_cast(bf8, b), c, 0, 0, 0);
}
__device__ __forceinline__ void mfma_acc(f4& c, v4u a, v4u b) { asm volatile("v_mfma_f32_16x16x32_bf16 %0, %1, %2, %0" : "+v"(c) : "v"(a), "v"(b)); }
template <int CTRL> __device__ __forceinline__ float dpp(float v) {
  return __builtin_bit_cast(float, __builtin_amdgcn_update_dpp(0, __builtin_bit_cast(int, v), CTRL, 0xf, 0xf, true));
}
template <int CTRL> __device__ __forceinline__ unsigned dppu(unsigned v) { return (unsigned)__builtin_amdgcn_update_dpp(0, (int)v, CTRL, 0xf, 0xf, true); }
__device__ __forceinline__ float add16(float v) { unsigned u = asu(v); auto r = __builtin_amdgcn_permlane16_swap(u, u, false, false); return asf(r[0]) + asf(r[1]); }
__device__ __forceinline__ float add32(float v) { unsigned u = asu(v); auto r = __builtin_amdgcn_permlane32_swap(u, u, false, false); return asf(r[0]) + asf(r[1]); }
__device__ __forceinline__ float fmx(float a, float b) { return __builtin_fmaxf(a, b); }
__device__ __forceinline__ float mx3(float a, float b, float c) { return fmx(fmx(a, b), c); }
__device__ __forceinline__ float max16(float v) { unsigned u = asu(v); auto r = __builtin_amdgcn_permlane16_swap(u, u, false, false); return fmx(asf(r[0]), asf(r[1])); }
__device__ __forceinline__ float max32(float v) { unsigned u = asu(v); auto r = __builtin_amdgcn_permlane32_swap(u, u, false, false); return fmx(asf(r[0]), asf(r[1])); }
__device__ __forceinline__ float wave_sum(float v) {
  v += dpp<0x128>(v); v += dpp<0x124>(v); v += dpp<0x122>(v); v += dpp<0x121>(v);
  return add32(add16(v));
}
__device__ __forceinline__ unsigned umax4(unsigned acc, v4u v) { return max(max(max(acc, v.x), v.y), max(v.z, v.w)); }
__device__ __forceinline__ rsrc_t mkrs(const void* p, int bytes) { return __builtin_amdgcn_make_buffer_rsrc((void*)p, 0, bytes, 0x00020000); }
__device__ __forceinline__ v4u mksrd(const void* p, unsigned bytes) { const u64 a = (u64)p; return v4u{(unsigned)a, (unsigned)(a >> 32) & 0xFFFFu, bytes, 0x00020000u}; }
__device__ __forceinline__ v4u ldnt(rsrc_t r, int voff, int soff) { return __builtin_amdgcn_raw_buffer_load_b128(r, voff, soff, AUX_NT); }
__device__ __forceinline__ v4u ldsc(rsrc_t r, int voff, int soff) { return __builtin_amdgcn_raw_buffer_load_b128(r, voff, soff, AUX_SC1); }
__device__ __forceinline__ void stsc(v4u v, rsrc_t r, int voff, int soff) { __builtin_amdgcn_raw_buffer_store_b128(v, r, voff, soff, AUX_SC1); }
__device__ __forceinline__ void dma16(v4u srd, int voff, int soff, int lds_dst) {
  asm volatile("s_mov_b32 m0, %3\n\ts_nop 0\n\tbuffer_load_dwordx4 %0, %1, %2 offen nt lds" :: "v"(voff), "s"(srd), "s"(soff), "s"(lds_dst) : "memory");
}
__device__ __forceinline__ void dma16p(v4u srd, int voff, int soff, int lds_dst) {
  asm volatile("s_mov_b32 m0, %3\n\ts_nop 0\n\tbuffer_load_dwordx4 %0, %1, %2 offen lds" :: "v"(voff), "s"(srd), "s"(soff), "s"(lds_dst) : "memory");
}
__device__ __forceinline__ void dma16sc(v4u srd, int voff, int soff, int lds_dst) {
  asm volatile("s_mov_b32 m0, %3\n\ts_nop 0\n\tbuffer_load_dwordx4 %0, %1, %2 offen sc1 lds" :: "v"(voff), "s"(srd), "s"(soff), "s"(lds_dst) : "memory");
}
template <typename T> __device__ __forceinline__ LAS T* lp(int off) { return (LAS T*)(uintptr_t)(unsigned)off; }
__device__ __forceinline__ v4u lds128(int off) { return *lp<v4u>(off); }
__device__ __forceinline__ v2u ldstr(int off) { return __builtin_bit_cast(v2u, __builtin_amdgcn_ds_read_tr16_b64_v4i16(lp<s4>(off))); }
__device__ __forceinline__ int swz(int r) { return ((r & 7) << 1) | ((r >> 3) & 1); }
__device__ __forceinline__ v4u xmask(unsigned xx, const unsigned (&mk)[4]) { return v4u{xx & mk[0], xx & mk[1], xx & mk[2], xx & mk[3]}; }
__device__ __forceinline__ bool all_ok(bool ok) { return __builtin_amdgcn_ballot_w64(!ok) == 0ull; }
__device__ __forceinline__ void wait_until(u64 t) { while ((long long)(__builtin_amdgcn_s_memrealtime() - t) < 0) __builtin_amdgcn_s_sleep(1); }

__device__ __forceinline__ void ln_stats(v4u xa, float& mb, float& ri) {
  float v[8] = {bflo(xa.x), bfhi(xa.x), bflo(xa.y), bfhi(xa.y), bflo(xa.z), bfhi(xa.z), bflo(xa.w), bfhi(xa.w)};
  const float mean = wave_sum(((v[0] + v[1]) + (v[2] + v[3])) + ((v[4] + v[5]) + (v[6] + v[7]))) * (1.0f / 512.0f);
  float q0 = 0.f, q1 = 0.f;
#pragma unroll
  for (int i = 0; i < 4; ++i) { const float d = v[i] - mean, e = v[i + 4] - mean; q0 += d * d; q1 += e * e; }
  const float var = wave_sum(q0 + q1) * (1.0f / 512.0f);
  mb = rbf(mean);
  ri = rcp(rbf(__builtin_amdgcn_sqrtf(rbf(rbf(var) + 1e-5f))));
}

__device__ __forceinline__ v4u layernorm8(v4u xa) {
  float mb, ri;
  ln_stats(xa, mb, ri);
  v4u o;
#pragma unroll
  for (int i = 0; i < 4; ++i) { const unsigned d = pk(bflo(xa[i]) - mb, bfhi(xa[i]) - mb); o[i] = pk(bflo(d) * ri, bfhi(d) * ri); }
  return o;
}

typedef u64 v8q __attribute__((ext_vector_type(8)));
typedef u64 v4q __attribute__((ext_vector_type(4)));
extern "C" __global__ void __launch_bounds__(512, 2) mla_decode(const void*, const void*, void*, const void*, const void*, const void*, const void*, const void*,
                                                      const void*, const void*, void*, const void*) {
  v8q ka0; v4q ka1; u64 t_start;
  asm volatile("s_load_dwordx16 %0, %3, 0x0\n\ts_load_dwordx8 %1, %3, 0x40\n\ts_memrealtime %2" : "=&s"(ka0), "=&s"(ka1), "=&s"(t_start) : "s"(__builtin_amdgcn_kernarg_segment_ptr()));
  const int b = blockIdx.x, tid = threadIdx.x, lane = tid & 63, w = __builtin_amdgcn_readfirstlane(tid >> 6);
  const int xcd = b & 7, rr = (b >> 3) & 15;
  const bool isA = b < 128;
  const int r16 = lane & 15, g = lane >> 4;
  const int h = xcd * 4 + (rr & 3), j = rr >> 2;
  const int ai = rr * 8 + xcd;
  {
    u64 pc; asm volatile("s_getpc_b64 %0" : "=s"(pc));
    asm volatile("s_mov_b32 m0, %2\n\ts_nop 0\n\tglobal_load_lds_dword %0, %1" :: "v"(min(((b >> 3) * 8 + w) * 128, CODE_BYTES - 128) + r16 * 8), "s"(pc & ~255ull), "s"(114688) : "memory");
  }
#define COMMON_CONSTS \
  const int dchunk = (r16 ^ swz(4 * (w & 3) + g)) * 16;     \
  const int trrow = 4 * g + (r16 >> 2); \
  const int trbase = trrow * 256 + ((((r16 >> 1) & 1) ^ swz(trrow)) * 16) + 8 * (r16 & 1);     \
  const int rbase = r16 * 256 + ((4 * g) ^ swz(r16)) * 16;                                      \
  const int trbaseK = trbase + trrow * 768, rbaseK = rbase + r16 * 768;                         \
  const int vrow0 = g * 256 + (r16 ^ (2 * w)) * 16, vrow1 = vrow0 ^ 16;

  const int cons = h * 4 + j;
  const int jj = 2 * j + (b >> 7);
  v4u wqv[4], xa; v2u xb;
  int oW = (64 * jj + 16 * (w & 3) + 4 * g) * 8192 + h * 256 + r16 * 16, oW1 = oW + 8192, oW2 = oW + 16384, oW3 = oW + 24576,
      ox = (w == 2) ? lane * 16 : (w == 3) ? WS_QA + cons * 256 + lane * 4 : (64 * jj + 16 * (w & 3) + 4 * g) * 2;
  unsigned em = EMPTY;
  PIN(oW); PIN(oW1); PIN(oW2); PIN(oW3); PIN(ox); PIN(em);
  SCHED();
  asm volatile("s_waitcnt lgkmcnt(0)" : "+s"(ka0), "+s"(ka1), "+s"(t_start));
  const u16 *x = (const u16*)ka0[0], *wq = (const u16*)ka0[1], *wuk = (const u16*)ka0[3], *ckv = (const u16*)ka0[4], *wuv = (const u16*)ka0[5], *wo = (const u16*)ka0[6],
            *wg = (const u16*)ka0[7], *wu = (const u16*)ka1[0], *wd = (const u16*)ka1[1];
  unsigned char* ws = (unsigned char*)ka0[2];
  u16* out = (u16*)ka1[2];
  if (w >= 4) {
    asm volatile("global_load_dwordx2 %4, %5, %6\n\tglobal_load_dwordx4 %0, %7, %11 nt\n\tglobal_load_dwordx4 %1, %8, %11 nt\n\tglobal_load_dwordx4 %2, %9, %11 nt\n\tglobal_load_dwordx4 %3, %10, %11 nt"
                 : "=&v"(wqv[0]), "=&v"(wqv[1]), "=&v"(wqv[2]), "=&v"(wqv[3]), "=&v"(xb) : "v"(ox), "s"(x), "v"(oW), "v"(oW1), "v"(oW2), "v"(oW3), "s"(wq) : "memory");
  }
  if (w == 2) asm volatile("global_load_dwordx4 %0, %1, %2" : "=&v"(xa) : "v"(ox), "s"(x) : "memory");
  if (w == 3 && !isA) asm volatile("global_store_dword %0, %1, %2 sc1" :: "v"(ox), "v"(em), "s"(ws) : "memory");
  unsigned tag;
  asm volatile("s_load_dword %0, %1, 0x0\n\ts_barrier" : "=&s"(tag) : "s"(ws) : "memory");
  int po = lane * 16;
  if (w < 2) {
    if (isA) {
      const v4u skv = mksrd(ckv, 16777216);
      const int so = (128 * ai + w) * 1024, vb = po ^ (w * 32);
#pragma unroll 1
      for (int i = 0; i < 8; ++i) {
        const int blk = ((2 * i) & 7) + (i >> 2);
        if (i == 1) asm volatile("s_barrier\n\ts_barrier" ::: "memory");
        if (i == 6) asm volatile("s_waitcnt vmcnt(16)\n\ts_barrier" ::: "memory");
#pragma unroll
        for (int r = 0; r < 8; ++r) dma16(skv, vb ^ ((r & 3) * 64 + (r >> 2) * 16), so + blk * 16384 + r * 2048, w * 1024 + blk * 16384 + r * 2048);
      }
      STAMP(15);
    } else {
      const v4u sk = mksrd(wuk, 4194304);
      const int vk = g * 8192 + (r16 ^ (2 * g)) * 16;
#pragma unroll 1
      for (int i = 0; i < 4; ++i) {
        if (i == KPRE) asm volatile("s_barrier\n\ts_barrier" ::: "memory");
#pragma unroll
        for (int n = 0; n < 4; ++n) dma16(sk, vk ^ ((n & 1) * 128 + (n >> 1) * 16), (128 * j + 64 * w + 16 * i + 4 * n) * 8192 + h * 256, QAI + (16 * w + 4 * i + n) * 1024);
      }
    }
  }
  SCHED();
  const rsrc_t rs = mkrs(ws, WS_END);
  unsigned mk[4];
#pragma unroll
  for (int k = 0; k < 4; ++k) { mk[k] = ((r16 >> 1) == k) ? ((r16 & 1) ? 0xFFFF0000u : 0x0000FFFFu) : 0u; PIN(mk[k]); }
  if (w >= 4) {
    unsigned mk2[4];
    v4u xm[4];
#pragma unroll
    for (int k = 0; k < 4; ++k) { mk2[k] = mk[k] | dppu<0x128>(mk[k]); PIN(mk2[k]); }
    int a_w0 = 116736 + (w - 4) * 1024 + (g >> 1) * 512 + r16 * 32 + (lane & 16);
    PIN(a_w0);
    asm volatile("s_waitcnt vmcnt(4)" : "+v"(xb) :: "memory");
#pragma unroll
    for (int k = 0; k < 2; ++k) {
      const unsigned xp = (r16 >= 8) ? 0x3F803F80u : xb[k];
      xm[2 * k] = xmask(__builtin_amdgcn_perm(xp, xp, 0x01000100u), mk2); xm[2 * k + 1] = xmask(__builtin_amdgcn_perm(xp, xp, 0x03020302u), mk2);
      PIN(xm[2 * k]); PIN(xm[2 * k + 1]);
    }
    SCHED();
    f4 acc = {0.f, 0.f, 0.f, 0.f};
    asm volatile("s_waitcnt vmcnt(0)" : "+v"(wqv[0]), "+v"(wqv[1]), "+v"(wqv[2]), "+v"(wqv[3]) :: "memory");
#pragma unroll
    for (int i = 0; i < 4; ++i) acc = mfma(xm[i], wqv[i], acc);
    STAMP(12);
    *lp<f4>(a_w0) = acc;
    BAR();
    asm volatile("s_barrier" ::: "memory");
  }
  float mb = 0.f, ri = 0.f;
  if (w == 2) {
    __builtin_amdgcn_s_setprio(3);
    asm volatile("s_waitcnt vmcnt(0)" : "+v"(xa) :: "memory");
    ln_stats(xa, mb, ri);
    PIN(mb); PIN(ri);
  }
  asm volatile("s_waitcnt lgkmcnt(0)" : "+s"(tag));
  tag += 1u;
  unsigned tv = tag;
  PIN(tv);
  if (w == 2) {
    int a_qp = ((lane & 31) * 2 + (lane >> 5)) * 16, a_r0 = 116736 + po;
    PIN(a_qp); PIN(a_r0);
    BAR();
    f4 p[4];
#pragma unroll
    for (int i = 0; i < 4; ++i) p[i] = *lp<f4>(a_r0 + i * 1024);
    const f4 s = (p[0] + p[1]) + (p[2] + p[3]);
    const auto r0 = __builtin_amdgcn_permlane32_swap(asu(s[0]), asu(s[2]), false, false), r1 = __builtin_amdgcn_permlane32_swap(asu(s[1]), asu(s[3]), false, false);
    stsc(v4u{asu((asf(r0[0]) - mb * asf(r0[1])) * ri), tv, asu((asf(r1[0]) - mb * asf(r1[1])) * ri), tv}, rs, a_qp, WS_QP + (h * 8 + jj) * 1024);
    asm volatile("s_barrier" ::: "memory");
    STAMP(1);
    __builtin_amdgcn_s_setprio(0);
    if (!isA) {
      v4u xn;
#pragma unroll
      for (int i = 0; i < 4; ++i) { const unsigned d = pk(bflo(xa[i]) - mb, bfhi(xa[i]) - mb); xn[i] = pk(bflo(d) * ri, bfhi(d) * ri); }
      *lp<v4u>(XNI + po) = xn;
    }
  }
  if (w == 3) {
    VMWAIT(0);
    if (!isA && lane == 0) __builtin_amdgcn_raw_buffer_store_b64(v2u{tv, tv}, rs, 0, WS_FL + cons * 8, AUX_SC1);
    asm volatile("s_barrier\n\ts_barrier" ::: "memory");
  }
#define DLY_G DELAY_G
#define DLY_W DELAY_W
#define DLY_H DELAY_H
#define DLY_U DELAY_U
#define DLY_A DELAY_A
#define DLY_Q DELAY_Q
#define DLY_O DELAY_O
#define DLY_F DELAY_F
  if (!isA) {
    const int xrow0 = 128 * j + 16 * w;
    COMMON_CONSTS
    int a_wk = QAI + w * 4096 + rbase, a_qw = 73728 + lane * 4, a_qr = 73728 + g * 64, a_qa = 74240 + w * 32 + g * 8;
    int a_wk4[4];
#pragma unroll
    for (int s = 0; s < 4; ++s) { a_wk4[s] = a_wk ^ (s * 16); PIN(a_wk4[s]); }
    PIN(a_qw); PIN(a_qr); PIN(a_qa);
    if (w == 4) {
      v4u p[8]; int spins = 0; unsigned bad;
      wait_until(t_start + DLY_H);
      for (;;) {
        FENCE();
#pragma unroll
        for (int i = 0; i < 8; ++i) p[i] = ldsc(rs, po + (i & 3) * 1024, WS_QP + h * 8192 + (i >> 2) * 4096);
        bad = 0;
#pragma unroll
        for (int i = 0; i < 8; ++i) bad |= (p[i].y ^ tv) | (p[i].w ^ tv);
        PIN(bad);
        if (all_ok(bad == 0) || ++spins >= SPIN_CAP) break;
        __builtin_amdgcn_s_sleep(1);
      }
      const float q0 = ((asf(p[0].x) + asf(p[1].x)) + (asf(p[2].x) + asf(p[3].x))) + ((asf(p[4].x) + asf(p[5].x)) + (asf(p[6].x) + asf(p[7].x)));
      const float q1 = ((asf(p[0].z) + asf(p[1].z)) + (asf(p[2].z) + asf(p[3].z))) + ((asf(p[4].z) + asf(p[5].z)) + (asf(p[6].z) + asf(p[7].z)));
      *lp<unsigned>(a_qw) = pk(q0, q1);
    }
    if (w < 2) { VMWAIT(0); STAMP(13); }
    BAR();
    STAMP(2);
    v4u xm[4];
    {
      v4u qf[4], wkv[4];
#pragma unroll
      for (int s = 0; s < 4; ++s) { wkv[s] = lds128(a_wk4[s]); qf[s] = lds128(a_qr + s * 16); }
      SCHED();
      f4 acc = {0.f, 0.f, 0.f, 0.f};
#pragma unroll
      for (int s = 0; s < 4; ++s) acc = mfma(wkv[s], qf[s], acc);
      const float sc = 0.08838834764831845f * 1.4426950408889634f;
      if (r16 == 0) *lp<v2u>(a_qa) = v2u{pkc(acc[0] * sc, acc[1] * sc), pkc(acc[2] * sc, acc[3] * sc)};
    }
    BAR();
    if (w == 0 && lane < 16) stsc(lds128(74240 + po), rs, po, WS_QA + cons * 256);
    STAMP(3);
    {
      const v2u x4 = *lp<v2u>(XNI + (xrow0 + 4 * g) * 2);
      xm[0] = xmask(__builtin_amdgcn_perm(x4.x, x4.x, 0x01000100u), mk); xm[1] = xmask(__builtin_amdgcn_perm(x4.x, x4.x, 0x03020302u), mk);
      xm[2] = xmask(__builtin_amdgcn_perm(x4.y, x4.y, 0x01000100u), mk); xm[3] = xmask(__builtin_amdgcn_perm(x4.y, x4.y, 0x03020302u), mk);
#pragma unroll
      for (int i = 0; i < 4; ++i) PIN(xm[i]);
    }
    BAR();
    wait_until(t_start + DLY_Q);
    v4u buv[4], bo[4][4], st;
    const v4u sws = mksrd(ws, WS_END);
    int a_st = (4 * g + (r16 & 3)) * 512, a_od = g * 1024 + r16 * 16, a_w1 = (g >> 1) * 12288 + w * 272 + r16 * 16 + (g & 1) * 8, a_r1 = (r16 & 7) * 272 + g * 16,
        a_w2 = 8192 + ((16 * w + r16 - 32 * j) & 127) * 2, a_r2 = 8192 + g * 16, a_o = g * 16384 + r16 * 8, a_sw = 4096 + w * 8, a_sr = 4096 + (g & 1) * 32;
    PIN(a_st); PIN(a_od); PIN(a_w1); PIN(a_r1); PIN(a_w2); PIN(a_r2); PIN(a_o); PIN(a_sw); PIN(a_sr);
    {
      const v4u suv = mksrd(wuv, 4194304), so = mksrd(wo, 4194304);
      const int so2 = (h * 128 + 8 * (w >> 2) + (w & 3)) * 1024;
      int tq[4];
#pragma unroll
      for (int t = 0; t < 4; ++t) tq[t] = (w >> 1) * 256 + (trbaseK ^ (32 * (4 * (w & 1) + t)));
#define WO4(n) _Pragma("unroll") for (int i = 0; i < 4; ++i) dma16p(so, (i & 1) ? vrow1 : vrow0, so2 + ((((n) + j) & 3) * 32 + 16 * (i & 1) + 4 * (i >> 1)) * 1024, (32 * (n) + w + 8 * i) * 1024)
#define RD_BO(n) _Pragma("unroll") for (int t = 0; t < 4; ++t) { const v2u lo = ldstr(tq[t] + (n) * 32768), hi = ldstr(tq[t] + (n) * 32768 + 16384); bo[t][n] = v4u{lo.x, lo.y, hi.x, hi.y}; }
      v4u fw[4];
      const GAS char* pf = (const GAS char*)((r16 < 8) ? wg : wu) + (h < 28 ? (xrow0 + 4 * g + 1) * 3584 + h * 128 : 3584) + (r16 & 7) * 16;
      const int vo1 = g * 16 * 8192 + dchunk, so1 = (128 * j + 2 * (w & 3) + (w >> 2)) * 8192 + h * 256;
      WO4(0);
      asm volatile("s_barrier\n\tglobal_load_dwordx4 %0, %4, off offset:-3584 nt\n\tglobal_load_dwordx4 %1, %4, off nt\n\tglobal_load_dwordx4 %2, %4, off offset:3584 nt\n\tglobal_load_dwordx4 %3, %5, off nt\n\ts_barrier"
                   : "=&v"(fw[0]), "=&v"(fw[1]), "=&v"(fw[2]), "=&v"(fw[3]) : "v"(pf), "v"(pf + 7168) : "memory");
#define UV4() _Pragma("unroll 1") for (int n = 0; n < 4; ++n) dma16(suv, vo1, so1 + (64 * (n >> 1) + 8 * (n & 1)) * 8192, QAI + (32 * n + 4 * w) * 256)
      UV4();
      asm volatile("s_waitcnt vmcnt(4)" : "+v"(fw[0]), "+v"(fw[1]), "+v"(fw[2]), "+v"(fw[3]) :: "memory");
      f4 acc = {0.f, 0.f, 0.f, 0.f};
#pragma unroll
      for (int i = 0; i < 4; ++i) acc = mfma(xm[i], fw[i], acc);
      if (lane < 32) *lp<f4>(102400 + w * 512 + r16 * 32 + (lane & 16)) = acc;
      BAR();
      if (w == 0) {
        f2 p[8];
#pragma unroll
        for (int i = 0; i < 8; ++i) p[i] = *lp<f2>(102400 + lane * 8 + i * 512);
        const f2 s = ((p[0] + p[1]) + (p[2] + p[3])) + ((p[4] + p[5]) + (p[6] + p[7]));
        stsc(v4u{asu(s.x), tv, asu(s.y), tv}, rs, po, WS_FP + cons * 1024);
        STAMP(15);
      }
      asm volatile("s_barrier" ::: "memory");
      WO4(1);
      asm volatile("s_barrier" ::: "memory");
      WO4(2);
      RD_BO(0)
      asm volatile("s_barrier" ::: "memory");
      WO4(3);
      asm volatile("s_waitcnt vmcnt(8) lgkmcnt(0)\n\ts_barrier" ::: "memory");
      RD_BO(1)
#pragma unroll
      for (int ks = 0; ks < 4; ++ks) {
        const int a = QAI + (trbase ^ (32 * w)) + ks * 8192;
        const v2u lo = ldstr(a), hi = ldstr(a + 4096);
        buv[ks] = v4u{lo.x, lo.y, hi.x, hi.y};
      }
      asm volatile("s_waitcnt vmcnt(4)\n\ts_barrier\n\tbuffer_load_dwordx4 %0, %1, %2, %3 offen sc1" : "=&v"(st) : "v"(a_st), "s"(sws), "s"(WS_ST + w * 8192 + h * 16) : "memory");
      RD_BO(2)
      asm volatile("s_waitcnt vmcnt(1)\n\ts_barrier" ::: "memory");
      RD_BO(3)
      asm volatile("s_waitcnt vmcnt(0)" : "+v"(st) :: "memory");
      STAMP(14);
    }
    v4u od[4];
    {
      for (int spins = 0; !all_ok((st.y == tv) & (st.w == tv)) && spins < SPIN_CAP; ++spins)
        asm volatile("buffer_load_dwordx4 %0, %1, %2, %3 offen sc1\n\ts_waitcnt vmcnt(0)" : "=&v"(st) : "v"(a_st), "s"(sws), "s"(WS_ST + w * 8192 + h * 16) : "memory");
      STAMP(6);
#pragma unroll
      for (int i = 0; i < 4; ++i) od[i] = ldsc(rs, a_od + i * 256, WS_OP + (cons * 128 + 16 * w) * 256);
      SCHED();
      const float m = asf(st.x);
      float mw = fmx(m, dpp<0xB1>(m)); mw = fmx(mw, dpp<0x4E>(mw)); mw = max32(max16(mw));
      const float e = ex2(m - mw);
      const unsigned wb = pk(e, e);
      xm[0] = xmask(dppu<0x00>(wb), mk); xm[1] = xmask(dppu<0x55>(wb), mk); xm[2] = xmask(dppu<0xAA>(wb), mk); xm[3] = xmask(dppu<0xFF>(wb), mk);
      float lw = bflo(wb) * asf(st.z);
      lw += dpp<0xB1>(lw); lw += dpp<0x4E>(lw); lw = add32(add16(lw));
      *lp<f2>(a_sw) = f2{mw, lw};
#pragma unroll
      for (int i = 0; i < 4; ++i) PIN(xm[i]);
      SCHED();
    }
    {
      int spins = 0; f4 acc;
      for (;;) {
        unsigned bad = 0;
        acc = f4{0.f, 0.f, 0.f, 0.f};
#pragma unroll
        for (int i = 0; i < 4; ++i) { acc = mfma(xm[i], od[i], acc); bad = umax4(bad, od[i]); }
        if (all_ok(bad != EMPTY) || ++spins >= SPIN_CAP) break;
        FENCE();
#pragma unroll
        for (int i = 0; i < 4; ++i) od[i] = ldsc(rs, a_od + i * 256, WS_OP + (cons * 128 + 16 * w) * 256);
      }
      *lp<v2u>(a_w1) = v2u{pk(acc[0], acc[1]), pk(acc[2], acc[3])};
    }
    STAMP(7);
    BAR();
    {
      v4u af[4];
      const f4 s0 = *lp<f4>(a_sr), s1 = *lp<f4>(a_sr + 16);
#pragma unroll
      for (int ks = 0; ks < 4; ++ks) af[ks] = lds128(a_r1 + ks * 64);
      SCHED();
      f4 acc = {0.f, 0.f, 0.f, 0.f};
#pragma unroll
      for (int ks = 0; ks < 4; ++ks) acc = mfma(af[ks], buv[ks], acc);
      const float M = max16(fmx(mx3(s0[0], s0[2], s1[0]), s1[2]));
      const float f0 = ex2(s0[0] - M), f1 = ex2(s0[2] - M), f2_ = ex2(s1[0] - M), f3 = ex2(s1[2] - M);
      const float ri = rcp(add16((f0 * s0[1] + f1 * s0[3]) + (f2_ * s1[1] + f3 * s1[3])));
      const float c = add16((f0 * acc[0] + f1 * acc[1]) + (f2_ * acc[2] + f3 * acc[3])) * ri;
      *lp<u16>(a_w2) = (u16)pk(c, c);
    }
    BAR();
    {
      v4u cf[4];
#pragma unroll
      for (int ks = 0; ks < 4; ++ks) cf[ks] = lds128(a_r2 + ks * 64);
      SCHED();
      f4 acc[4];
#pragma unroll
      for (int t = 0; t < 4; ++t) acc[t] = f4{0.f, 0.f, 0.f, 0.f};
#pragma unroll
      for (int ks = 0; ks < 4; ++ks)
#pragma unroll
        for (int t = 0; t < 4; ++t) acc[t] = mfma(cf[ks], bo[t][ks], acc[t]);
      const float v01 = (g & 1) ? acc[1][0] : acc[0][0], v23 = (g & 1) ? acc[3][0] : acc[2][0];
      const float v = (g & 2) ? v23 : v01;
      __builtin_amdgcn_raw_buffer_store_b64(v2u{asu(v), tv}, rs, a_o, WS_SP + (4 * w * 128 + cons) * 128, AUX_SC1);
    }
    STAMP(8);
    STAMP_OUT();
    __builtin_amdgcn_endpgm();
  }
  {
    const v4u sws = mksrd(ws, WS_END);
    const int ht = w & 1, kt = w >> 1;
    v4u k0[16], k1[16];
    v2u v0[4][4];
    const rsrc_t rx = mkrs(x, 1024);
    COMMON_CONSTS
    const int mi = ai & 3, fi = (ai < 112) ? (ai >> 2) : 0;
    int ka[4], ta[4], qa_[4], vo0 = vrow0, vo1 = vrow1;
#pragma unroll
    for (int s = 0; s < 4; ++s) {
      ka[s] = kt * 32768 + (rbaseK ^ (s * 16)); ta[s] = (w >> 1) * 256 + (trbaseK ^ (32 * (4 * (w & 1) + s))); qa_[s] = ka[s] + (QAI + ht * 16384 - kt * 32768);
      PIN(ka[s]); PIN(ta[s]); PIN(qa_[s]);
    }
    int a_d = (2 * g + (r16 >> 3)) * 1024 + (r16 & 7) * 16, a_mw = (16 * ht + r16) * 16 + kt * 4,
        a_s = (lane < 16) ? (16 * ht + r16) * 16 : WS_END, a_o = (16 * g + (w >> 1)) * 32768 + (w & 1) * 128 + r16 * 8, a_pq = QAI + po,
        a_c = (16 * w + g) * 32768 + r16 * 16;
    int a_mr = a_mw & ~15, a_lw = 2048 + g * 512 + a_mw, a_pw = a_pq + w * 1024;
    PIN(a_mr); PIN(a_lw); PIN(a_pw);
    PIN(a_mw); PIN(a_s); PIN(a_o); PIN(a_pq); PIN(a_c); PIN(vo0); PIN(vo1);
#define RD_U0() _Pragma("unroll") for (int i = 0; i < 4; ++i) _Pragma("unroll") for (int s = 0; s < 4; ++s) { k0[4 * i + s] = lds128(ka[s] + i * 256); v0[i][s] = ldstr(ta[s] + i * 32768); }
#define RD_K1() _Pragma("unroll") for (int s = 0; s < 16; ++s) k1[s] = lds128(ka[s & 3] + (s >> 2) * 256 + 16384)
#define FLAGS_OK() all_ok(((fl.x ^ tv) | (fl.y ^ tv) | (fl.z ^ tv) | (fl.w ^ tv)) == 0)
#define QA4() _Pragma("unroll") for (int q = 0; q < 4; ++q) dma16sc(sws, (q & 1) ? vo1 : vo0, WS_QA + (w + 8 * q) * 1024, QAI + (w + 8 * q) * 1024)
    if (w >= 2) {
      v4u fl;
      if (w == 3) {
        __builtin_amdgcn_s_setprio(3);
        wait_until(t_start + 210);
        asm volatile("s_waitcnt vmcnt(0)\n\tbuffer_load_dwordx4 %0, %1, %2, %3 offen sc1" : "=&v"(fl) : "v"(po), "s"(sws), "s"(WS_FL) : "memory");
      }
      asm volatile("s_barrier" ::: "memory");
      RD_U0()
      if (w == 3) {
        asm volatile("s_waitcnt vmcnt(0)" : "+v"(fl) :: "memory");
        int spins = 0;
        while (!FLAGS_OK() && ++spins < SPIN_CAP)
          asm volatile("buffer_load_dwordx4 %0, %1, %2, %3 offen sc1\n\ts_waitcnt vmcnt(0)" : "=&v"(fl) : "v"(po), "s"(sws), "s"(WS_FL) : "memory");
        __builtin_amdgcn_s_setprio(0);
      }
    }
    asm volatile("s_waitcnt vmcnt(16)\n\ts_barrier" ::: "memory");
    STAMP(2);
    const bool early = (long long)(__builtin_amdgcn_s_memrealtime() - (t_start + DLY_G)) >= 0;
    if (early) { QA4(); }
    if (w < 2) { RD_U0() }
    if (kt < 2) { RD_K1(); }
    v4u vf[4][4];
#pragma unroll
    for (int i = 0; i < 8; ++i) { const v2u hi = ldstr(ta[i & 3] + (i >> 2) * 32768 + 16384); vf[i >> 2][i & 3] = v4u{v0[i >> 2][i & 3].x, v0[i >> 2][i & 3].y, hi.x, hi.y}; }
    if (!early) { wait_until(t_start + DLY_G); QA4(); }
    VMWAIT(4);
    BAR();
    STAMP(3);
    if (kt >= 2) { RD_K1(); }
#define RD_V1(i) { const v2u hi = ldstr(ta[(i) & 3] + ((i) >> 2) * 32768 + 16384); vf[(i) >> 2][(i) & 3] = v4u{v0[(i) >> 2][(i) & 3].x, v0[(i) >> 2][(i) & 3].y, hi.x, hi.y}; }
    f4 S0 = {0.f, 0.f, 0.f, 0.f}, S1 = {0.f, 0.f, 0.f, 0.f};
    PIN(S0); PIN(S1);
    for (int spins = 0;; ++spins) {
      VMWAIT(0);
      unsigned bad = 0;
#pragma unroll
      for (int q = 0; q < 4; ++q) bad = umax4(bad, lds128(a_pw + q * 8192));
      if (all_ok(bad != EMPTY) || spins >= SPIN_CAP) break;
      QA4();
    }
    BAR();
    STAMP(4);
    {
      v4u qf[16];
#pragma unroll
      for (int s = 0; s < QD; ++s) qf[s] = lds128(qa_[s & 3] + (s >> 2) * 256);
      SCHED();
#pragma unroll
      for (int s = 0; s < 16; ++s) {
        mfma_acc(S0, k0[s], qf[s]); mfma_acc(S1, k1[s], qf[s]);
        if (s + QD < 16) qf[s + QD] = lds128(qa_[(s + QD) & 3] + ((s + QD) >> 2) * 256);
        if (s >= 8) RD_V1(s)
        if (s < 8 && !(s & 1)) stsc(v4u{EMPTY, EMPTY, EMPTY, EMPTY}, rs, a_c, WS_OP + (s >> 1) * 131072 + ai * 256);
        SCHED();
      }
      float m0, mt;
      asm volatile("s_nop 7\n\tv_max3_f32 %0, %2, %3, %4\n\tv_max3_f32 %0, %0, %5, %6\n\tv_max3_f32 %1, %7, %8, %9\n\tv_max_f32 %0, %0, %1\n\t"
                   "v_mov_b32 %1, %0\n\ts_nop 1\n\tv_permlane16_swap_b32 %0, %1\n\tv_max_f32 %0, %0, %1\n\t"
                   "v_mov_b32 %1, %0\n\ts_nop 1\n\tv_permlane32_swap_b32 %0, %1\n\tv_max_f32 %0, %0, %1"
                   : "=&v"(m0), "=&v"(mt) : "v"(S0[0]), "v"(S0[1]), "v"(S0[2]), "v"(S0[3]), "v"(S1[0]), "v"(S1[1]), "v"(S1[2]), "v"(S1[3]));
      *lp<float>(a_mw) = m0;
    }
    BAR();
    STAMP(5);
    float M;
    int a_f = (r16 & 3) * 1024 + (64 * ((r16 >> 2) & 1) + 8 * (r16 >> 3) + 2 * g) * 8;
    {
      const f4 m4 = *lp<f4>(a_mr);
      PIN(a_f); PIN(a_d);
      asm("v_max3_f32 %0, %1, %2, %3\n\tv_max_f32 %0, %0, %4" : "=&v"(M) : "v"(m4[0]), "v"(m4[1]), "v"(m4[2]), "v"(m4[3]));
      float p[8];
#pragma unroll
      for (int i = 0; i < 4; ++i) { p[i] = ex2(S0[i] - M); p[4 + i] = ex2(S1[i] - M); }
      *lp<v4u>(a_pw) = v4u{pk(p[0], p[1]), pk(p[2], p[3]), pk(p[4], p[5]), pk(p[6], p[7])};
      *lp<float>(a_lw) = ((p[0] + p[1]) + (p[2] + p[3])) + ((p[4] + p[5]) + (p[6] + p[7]));
    }
    VMWAIT(0);
    v4u dw[2], fp;
    asm volatile("buffer_load_dwordx4 %0, %1, %2, %3 offen sc1" : "=&v"(fp) : "v"(a_f), "s"(sws), "s"(WS_FP + fi * 4096 + mi * 128) : "memory");
    BAR();
    STAMP(6);
    {
      v4u pf_[8];
      pf_[0] = lds128(a_pq);
      const f4 l4 = *lp<f4>(a_lw & ~15);
#pragma unroll
      for (int i = 1; i < 8; ++i) pf_[i] = lds128(a_pq + ((i & 3) * 2 + (i >> 2)) * 1024);
      SCHED();
#pragma unroll
      for (int hh = 0; hh < 2; ++hh) {
        f4 O[4];
#pragma unroll
        for (int t = 0; t < 4; ++t) O[t] = f4{0.f, 0.f, 0.f, 0.f};
#pragma unroll
        for (int ks = 0; ks < 4; ++ks) {
#pragma unroll
          for (int t = 0; t < 4; ++t) O[t] = mfma(pf_[4 * hh + ks], vf[ks][t], O[t]);
          if (hh == 0 && ks == 0) { SCHED(); if (kt == 0) stsc(v4u{asu(M), tv, asu(add32(add16((l4[0] + l4[1]) + (l4[2] + l4[3])))), tv}, rs, a_s, WS_ST + ai * 512); SCHED(); }
        }
        if (hh == 1) { PIN(O[3]); STAMP(13); }
#pragma unroll
        for (int i = 0; i < 4; ++i)
          __builtin_amdgcn_raw_buffer_store_b64(v2u{pkc(O[0][i], O[1][i]), pkc(O[2][i], O[3][i])}, rs, a_o, WS_OP + (16 * hh + i) * 131072 + ai * 256, AUX_SC1);
      }
    }
    STAMP(7);
    const u64 t_pv = __builtin_amdgcn_s_memrealtime();
    asm volatile("s_waitcnt vmcnt(8)" : "+v"(fp) :: "memory");
    if (ai < 112) {
      wait_until(t_pv + DLY_U);
      const rsrc_t rd = mkrs(wd, 1835008);
#pragma unroll
      for (int i = 0; i < 2; ++i) dw[i] = ldnt(rd, a_d, (64 * fi + 16 * mi + 8 * i) * 1024 + w * 128);
      SCHED();
      const int gu = (r16 >> 2) & 1, hi = r16 >> 3;
      v4u p = fp;
      int spins = 0;
      for (; !all_ok((p.y == tv) & (p.w == tv)) && spins < SPIN_CAP; ++spins)
        asm volatile("buffer_load_dwordx4 %0, %1, %2, %3 offen sc1\n\ts_waitcnt vmcnt(0)" : "=&v"(p) : "v"(a_f), "s"(sws), "s"(WS_FP + fi * 4096 + mi * 128) : "memory");
      STAMP(14);
      float v0_ = asf(p.x), v1_ = asf(p.z);
      v0_ += dpp<0xB1>(v0_); v1_ += dpp<0xB1>(v1_);
      v0_ += dpp<0x4E>(v0_); v1_ += dpp<0x4E>(v1_);
      const float o0 = dpp<0x141>(v0_), o1 = dpp<0x141>(v1_);
      float hv[2];
#pragma unroll
      for (int e = 0; e < 2; ++e) {
        const float gt = rbf(gu ? (e ? o1 : o0) : (e ? v1_ : v0_)), up = rbf(gu ? (e ? v1_ : v0_) : (e ? o1 : o0));
        const float z2 = 2.0f * 0.7978845608028654f * 1.4426950408889634f * (gt + 0.044715f * gt * gt * gt);
        hv[e] = rbf(gt * rcp(1.0f + ex2(-z2))) * up;
      }
      const float t0 = dpp<0x128>(hv[0]), t1 = dpp<0x128>(hv[1]);
      unsigned mk2[4];
#pragma unroll
      for (int k = 0; k < 4; ++k) mk2[k] = mk[k] | dppu<0x128>(mk[k]);
      const float ha = hi ? t1 : hv[0], hb = hi ? hv[1] : t0;
      f4 acc = {0.f, 0.f, 0.f, 0.f};
      acc = mfma(xmask(pk(ha, ha), mk2), dw[0], acc);
      acc = mfma(xmask(pk(hb, hb), mk2), dw[1], acc);
      float y[4];
#pragma unroll
      for (int i = 0; i < 4; ++i) {
        const unsigned u = asu(dpp<0x128>(acc[i]));
        auto r = __builtin_amdgcn_permlane32_swap(u, u, false, false);
        y[i] = acc[i] + asf(r[1]);
      }
      const auto ya = __builtin_amdgcn_permlane32_swap(asu(y[0]), asu(y[2]), false, false), yb = __builtin_amdgcn_permlane32_swap(asu(y[1]), asu(y[3]), false, false);
      if (r16 < 8) stsc(v4u{ya[0], tv, yb[0], tv}, rs, (r16 >> 1) * 16384 + ((r16 & 1) * 8 + 4 * (g & 1) + 2 * (g >> 1)) * 8, WS_YP + (4 * w * 128 + ai) * 128);
    }
    STAMP(8);
    if (w >= 6) {
      const int off = (lane >> 1) * 128 + (lane & 1) * 16, so = ((w == 7) ? WS_SP : WS_YP) + h * 16384 + j * 32;
      const unsigned xo = __builtin_amdgcn_raw_buffer_load_b32(rx, (lane & 1) * 4, h * 32 + j * 8, 0);
      const bool skip3 = (w == 6) && (lane >= 32);
      v4u p[4]; int spins = 0; unsigned bad;
      {
        const int go = so + ((ai * 5 + 3) % 112) * 128;
        wait_until(t_pv + 80);
        for (int sp = 0; sp < SPIN_CAP; ++sp) {
          FENCE();
          const v2u t = __builtin_amdgcn_raw_buffer_load_b64(rs, 0, go, AUX_SC1);
          if (__builtin_amdgcn_readfirstlane(t.y) == tag) break;
          __builtin_amdgcn_s_sleep(2);
        }
        wait_until(__builtin_amdgcn_s_memrealtime() + ((w == 7) ? DLY_F : DLY_W));
      }
      do {
        FENCE();
#pragma unroll
        for (int n = 0; n < 4; ++n) p[n] = ldsc(rs, off, so + n * 4096);
        if (skip3) p[3] = v4u{0u, tv, 0u, tv};
        bad = 0;
#pragma unroll
        for (int n = 0; n < 4; ++n) bad |= (p[n].y ^ tv) | (p[n].w ^ tv);
        PIN(bad);
      } while (!all_ok(bad == 0) && ++spins < SPIN_CAP);
      float a0 = (asf(p[0].x) + asf(p[1].x)) + (asf(p[2].x) + asf(p[3].x));
      float a1 = (asf(p[0].z) + asf(p[1].z)) + (asf(p[2].z) + asf(p[3].z));
      a0 += dpp<0x128>(a0); a1 += dpp<0x128>(a1);
      a0 += dpp<0x124>(a0); a1 += dpp<0x124>(a1);
      a0 += dpp<0x122>(a0); a1 += dpp<0x122>(a1);
      a0 = rbf(add32(add16(a0))); a1 = rbf(add32(add16(a1)));
      if (w == 6) {
        a0 = rbf(a0 * rcp(1.0f + ex2(-1.4426950408889634f * a0)));
        a1 = rbf(a1 * rcp(1.0f + ex2(-1.4426950408889634f * a1)));
        if (lane < 2) *lp<f2>(1024 + lane * 8) = f2{a0, a1};
        STAMP(9);
        BAR();
      } else {
        BAR();
        const f2 gt = *lp<f2>(1024 + (lane & 1) * 8);
        const unsigned o = pk(bflo(xo) + rbf(a0 * gt.x), bfhi(xo) + rbf(a1 * gt.y));
        if (lane < 2) *(GAS unsigned*)(out + 16 * h + 4 * j + 2 * lane) = o;
        if (ai == 0 && lane == 0) *(GAS unsigned*)ws = tag;
        STAMP(10);
      }
    }
  }
  STAMP_OUT();
}

extern "C" void kernel_launch(void* const* d_in, const int* in_sizes, int n_in, void* d_out, int out_size, void* d_ws, size_t ws_size, hipStream_t stream) {
  (void)hipFuncSetAttribute((const void*)mla_decode, hipFuncAttributeMaxDynamicSharedMemorySize, LDS_BYTES);
  mla_decode<<<256, 512, LDS_BYTES, stream>>>((const u16*)d_in[0], (const u16*)d_in[2], (unsigned char*)d_ws + WS_BASE(ws_size), (const u16*)d_in[3], (const u16*)d_in[1], (const u16*)d_in[4], (const u16*)d_in[5],
                                              (const u16*)d_in[7], (const u16*)d_in[8], (const u16*)d_in[9], (u16*)d_out, d_in[0]);
}
```

```cpp
#include <hip/hip_runtime.h>
#include <stdint.h>

typedef unsigned short u16;
typedef unsigned long long u64;
typedef unsigned v4u __attribute__((ext_vector_type(4)));
typedef unsigned v2u __attribute__((ext_vector_type(2)));
typedef unsigned v8u __attribute__((ext_vector_type(8)));
typedef unsigned v16u __attribute__((ext_vector_type(16)));
typedef float f4 __attribute__((ext_vector_type(4)));
typedef float f2 __attribute__((ext_vector_type(2)));
typedef __bf16 bf8 __attribute__((ext_vector_type(8)));
typedef __bf16 bf2 __attribute__((ext_vector_type(2)));
typedef short s4 __attribute__((ext_vector_type(4)));
#define GAS __attribute__((address_space(1)))
#define LAS __attribute__((address_space(3)))
#define CAS __attribute__((address_space(4)))

#define DELAY_A 0
#define DELAY_Q 195
#define DELAY_O 0

#define DELAY_F 0
#define WS_BASE(n) 0
#define DELAY_G 220
#define DELAY_U 30
#define DELAY_H 45
#define ROLE_A_FIRST 1
#define ROLE_PARITY 1
#define WO_NT 9
#define KPRE 3
#define QD 6
#define DELAY_W 0
constexpr int WS_FL = 1024;
constexpr int WS_FL2 = 2048;
constexpr int WS_QP = 4096;
constexpr int WS_QA = WS_QP + 262144;
constexpr int WS_FP = WS_QA + 32768;
constexpr int WS_ST = WS_FP + 131072;
constexpr int WS_SP = WS_ST + 262144;
constexpr int WS_YP = WS_SP + 524288;
constexpr int WS_OP = WS_YP + 524288;
constexpr int WS_END = WS_OP + 4194304;
constexpr int WS_STAMP = 6 << 20;
constexpr int WS_TUNE = 64;
constexpr int LDS_BYTES = 163840;
constexpr int CODE_BYTES = 14336;
constexpr int SPIN_CAP = 1 << 16;
constexpr int AUX_SC1 = 16, AUX_NT = 2;
constexpr int QAI = 131072;
constexpr int XNI = 65536;
constexpr unsigned EMPTY = 0xFFFFFFFFu;

#define BAR() asm volatile("s_waitcnt lgkmcnt(0)\n\ts_barrier" ::: "memory")
#define FENCE() asm volatile("" ::: "memory")
#define SCHED() __builtin_amdgcn_sched_barrier(0)
#define VMWAIT(n) asm volatile("s_waitcnt vmcnt(" #n ")" ::: "memory")
#define PIN(x) asm volatile("" : "+v"(x))
#define PINS(x) asm volatile("" : "+s"(x))

#define STAMP(i) do {} while (0)
#define STAMP_OUT()

typedef __amdgpu_buffer_rsrc_t rsrc_t;
__device__ __forceinline__ float bflo(unsigned u) { return __uint_as_float(u << 16); }
__device__ __forceinline__ float bfhi(unsigned u) { return __uint_as_float(u & 0xFFFF0000u); }
__device__ __forceinline__ unsigned pk(float a, float b) { f2 f = {a, b}; return __builtin_bit_cast(unsigned, __builtin_convertvector(f, bf2)); }
__device__ __forceinline__ unsigned pkc(float a, float b) { return min(pk(a, b), EMPTY - 1u); }
__device__ __forceinline__ float rbf(float a) { return bflo(pk(a, a)); }
__device__ __forceinline__ float asf(unsigned u) { return __uint_as_float(u); }
__device__ __forceinline__ unsigned asu(float f) { return __float_as_uint(f); }
__device__ __forceinline__ float ex2(float x) { return __builtin_amdgcn_exp2f(x); }
__device__ __forceinline__ float rcp(float x) { return __builtin_amdgcn_rcpf(x); }
__device__ __forceinline__ f4 mfma(v4u a, v4u b, f4 c) {
  return __builtin_amdgcn_mfma_f32_16x16x32_bf16(__builtin_bit_cast(bf8, a), __builtin_bit_cast(bf8, b), c, 0, 0, 0);
}
__device__ __forceinline__ void mfma_acc(f4& c, v4u a, v4u b) { asm volatile("v_mfma_f32_16x16x32_bf16 %0, %1, %2, %0" : "+v"(c) : "v"(a), "v"(b)); }
template <int CTRL> __device__ __forceinline__ float dpp(float v) {
  return __builtin_bit_cast(float, __builtin_amdgcn_update_dpp(0, __builtin_bit_cast(int, v), CTRL, 0xf, 0xf, true));
}
template <int CTRL> __device__ __forceinline__ unsigned dppu(unsigned v) { return (unsigned)__builtin_amdgcn_update_dpp(0, (int)v, CTRL, 0xf, 0xf, true); }
__device__ __forceinline__ float add16(float v) { unsigned u = asu(v); auto r = __builtin_amdgcn_permlane16_swap(u, u, false, false); return asf(r[0]) + asf(r[1]); }
__device__ __forceinline__ float add32(float v) { unsigned u = asu(v); auto r = __builtin_amdgcn_permlane32_swap(u, u, false, false); return asf(r[0]) + asf(r[1]); }
__device__ __forceinline__ float fmx(float a, float b) { return __builtin_fmaxf(a, b); }
__device__ __forceinline__ float mx3(float a, float b, float c) { return fmx(fmx(a, b), c); }
__device__ __forceinline__ float max16(float v) { unsigned u = asu(v); auto r = __builtin_amdgcn_permlane16_swap(u, u, false, false); return fmx(asf(r[0]), asf(r[1])); }
__device__ __forceinline__ float max32(float v) { unsigned u = asu(v); auto r = __builtin_amdgcn_permlane32_swap(u, u, false, false); return fmx(asf(r[0]), asf(r[1])); }
__device__ __forceinline__ float wave_sum(float v) {
  v += dpp<0x128>(v); v += dpp<0x124>(v); v += dpp<0x122>(v); v += dpp<0x121>(v);
  return add32(add16(v));
}
__device__ __forceinline__ unsigned umax4(unsigned acc, v4u v) { return max(max(max(acc, v.x), v.y), max(v.z, v.w)); }
__device__ __forceinline__ rsrc_t mkrs(const void* p, int bytes) { return __builtin_amdgcn_make_buffer_rsrc((void*)p, 0, bytes, 0x00020000); }
__device__ __forceinline__ v4u mksrd(const void* p, unsigned bytes) { const u64 a = (u64)p; return v4u{(unsigned)a, (unsigned)(a >> 32) & 0xFFFFu, bytes, 0x00020000u}; }
__device__ __forceinline__ v4u ldnt(rsrc_t r, int voff, int soff) { return __builtin_amdgcn_raw_buffer_load_b128(r, voff, soff, AUX_NT); }
__device__ __forceinline__ v4u ldsc(rsrc_t r, int voff, int soff) { return __builtin_amdgcn_raw_buffer_load_b128(r, voff, soff, AUX_SC1); }
__device__ __forceinline__ void stsc(v4u v, rsrc_t r, int voff, int soff) { __builtin_amdgcn_raw_buffer_store_b128(v, r, voff, soff, AUX_SC1); }
__device__ __forceinline__ void dma16(v4u srd, int voff, int soff, int lds_dst) {
  asm volatile("s_mov_b32 m0, %3\n\ts_nop 0\n\tbuffer_load_dwordx4 %0, %1, %2 offen nt lds" :: "v"(voff), "s"(srd), "s"(soff), "s"(lds_dst) : "memory");
}
__device__ __forceinline__ void dma16p(v4u srd, int voff, int soff, int lds_dst) {
  asm volatile("s_mov_b32 m0, %3\n\ts_nop 0\n\tbuffer_load_dwordx4 %0, %1, %2 offen lds" :: "v"(voff), "s"(srd), "s"(soff), "s"(lds_dst) : "memory");
}
template <int NT> __device__ __forceinline__ void dmaw(v4u srd, int voff, int soff, int lds_dst) { if (NT) dma16(srd, voff, soff, lds_dst); else dma16p(srd, voff, soff, lds_dst); }
__device__ __forceinline__ void dma16sc(v4u srd, int voff, int soff, int lds_dst) {
  asm volatile("s_mov_b32 m0, %3\n\ts_nop 0\n\tbuffer_load_dwordx4 %0, %1, %2 offen sc1 lds" :: "v"(voff), "s"(srd), "s"(soff), "s"(lds_dst) : "memory");
}
template <typename T> __device__ __forceinline__ LAS T* lp(int off) { return (LAS T*)(uintptr_t)(unsigned)off; }
__device__ __forceinline__ v4u lds128(int off) { return *lp<v4u>(off); }
__device__ __forceinline__ v2u ldstr(int off) { return __builtin_bit_cast(v2u, __builtin_amdgcn_ds_read_tr16_b64_v4i16(lp<s4>(off))); }
__device__ __forceinline__ int swz(int r) { return ((r & 7) << 1) | ((r >> 3) & 1); }
__device__ __forceinline__ v4u xmask(unsigned xx, const unsigned (&mk)[4]) { return v4u{xx & mk[0], xx & mk[1], xx & mk[2], xx & mk[3]}; }
__device__ __forceinline__ bool all_ok(bool ok) { return __builtin_amdgcn_ballot_w64(!ok) == 0ull; }
__device__ __forceinline__ void wait_until(u64 t) { while ((long long)(__builtin_amdgcn_s_memrealtime() - t) < 0) __builtin_amdgcn_s_sleep(1); }

__device__ __forceinline__ void ln_stats(v4u xa, float& mb, float& ri) {
  float v[8] = {bflo(xa.x), bfhi(xa.x), bflo(xa.y), bfhi(xa.y), bflo(xa.z), bfhi(xa.z), bflo(xa.w), bfhi(xa.w)};
  const float mean = wave_sum(((v[0] + v[1]) + (v[2] + v[3])) + ((v[4] + v[5]) + (v[6] + v[7]))) * (1.0f / 512.0f);
  float q0 = 0.f, q1 = 0.f;
#pragma unroll
  for (int i = 0; i < 4; ++i) { const float d = v[i] - mean, e = v[i + 4] - mean; q0 += d * d; q1 += e * e; }
  const float var = wave_sum(q0 + q1) * (1.0f / 512.0f);
  mb = rbf(mean);
  ri = rcp(rbf(__builtin_amdgcn_sqrtf(rbf(rbf(var) + 1e-5f))));
}

__device__ __forceinline__ v4u layernorm8(v4u xa) {
  float mb, ri;
  ln_stats(xa, mb, ri);
  v4u o;
#pragma unroll
  for (int i = 0; i < 4; ++i) { const unsigned d = pk(bflo(xa[i]) - mb, bfhi(xa[i]) - mb); o[i] = pk(bflo(d) * ri, bfhi(d) * ri); }
  return o;
}

typedef u64 v8q __attribute__((ext_vector_type(8)));
typedef u64 v4q __attribute__((ext_vector_type(4)));
extern "C" __global__ void __launch_bounds__(512, 2) mla_decode(const void*, const void*, void*, const void*, const void*, const void*, const void*, const void*,
                                                      const void*, const void*, void*, const void*) {
  v8q ka0; v4q ka1; u64 t_start;
  asm volatile("s_load_dwordx16 %0, %3, 0x0\n\ts_load_dwordx8 %1, %3, 0x40\n\ts_memrealtime %2" : "=&s"(ka0), "=&s"(ka1), "=&s"(t_start) : "s"(__builtin_amdgcn_kernarg_segment_ptr()));
  const int b = blockIdx.x, tid = threadIdx.x, lane = tid & 63, w = __builtin_amdgcn_readfirstlane(tid >> 6);
  const int xcd = b & 7, rr = (b >> 3) & 15;
  const bool isA = (b & 1) == 0;
  const int r16 = lane & 15, g = lane >> 4;
  const int h = (xcd >> 1) * 8 + ((b >> 3) & 7), j = b >> 6;
  const int ai = b >> 1;
  {
    u64 pc; asm volatile("s_getpc_b64 %0" : "=s"(pc));
    asm volatile("s_mov_b32 m0, %2\n\ts_nop 0\n\tglobal_load_lds_dword %0, %1" :: "v"(min(((b >> 3) * 8 + w) * 128, CODE_BYTES - 128) + r16 * 8), "s"(pc & ~255ull), "s"(114688) : "memory");
  }
#define COMMON_CONSTS \
  const int dchunk = (r16 ^ swz(4 * (w & 3) + g)) * 16;     \
  const int trrow = 4 * g + (r16 >> 2); \
  const int trbase = trrow * 256 + ((((r16 >> 1) & 1) ^ swz(trrow)) * 16) + 8 * (r16 & 1);     \
  const int rbase = r16 * 256 + ((4 * g) ^ swz(r16)) * 16;                                      \
  const int trbaseK = trbase + trrow * 768, rbaseK = rbase + r16 * 768;                         \
  const int vrow0 = g * 256 + (r16 ^ (2 * w)) * 16, vrow1 = vrow0 ^ 16;

  const int cons = h * 4 + j;
  const int jj = 2 * j + (b & 1);
  v4u wqv[4], xa; v2u xb;
  int oW = (64 * jj + 16 * (w & 3) + 4 * g) * 8192 + h * 256 + r16 * 16, oW1 = oW + 8192, oW2 = oW + 16384, oW3 = oW + 24576,
      ox = (w == 2) ? lane * 16 : (w == 3) ? WS_QA + cons * 256 + lane * 4 : (64 * jj + 16 * (w & 3) + 4 * g) * 2;
  unsigned em = EMPTY;
  PIN(oW); PIN(oW1); PIN(oW2); PIN(oW3); PIN(ox); PIN(em);
  SCHED();
  asm volatile("s_waitcnt lgkmcnt(0)" : "+s"(ka0), "+s"(ka1), "+s"(t_start));
  const u16 *x = (const u16*)ka0[0], *wq = (const u16*)ka0[1], *wuk = (const u16*)ka0[3], *ckv = (const u16*)ka0[4], *wuv = (const u16*)ka0[5], *wo = (const u16*)ka0[6],
            *wg = (const u16*)ka0[7], *wu = (const u16*)ka1[0], *wd = (const u16*)ka1[1];
  unsigned char* ws = (unsigned char*)ka0[2];
  u16* out = (u16*)ka1[2];
  if (w >= 4) {
    asm volatile("global_load_dwordx2 %4, %5, %6\n\tglobal_load_dwordx4 %0, %7, %11 nt\n\tglobal_load_dwordx4 %1, %8, %11 nt\n\tglobal_load_dwordx4 %2, %9, %11 nt\n\tglobal_load_dwordx4 %3, %10, %11 nt"
                 : "=&v"(wqv[0]), "=&v"(wqv[1]), "=&v"(wqv[2]), "=&v"(wqv[3]), "=&v"(xb) : "v"(ox), "s"(x), "v"(oW), "v"(oW1), "v"(oW2), "v"(oW3), "s"(wq) : "memory");
  }
  if (w == 2) asm volatile("global_load_dwordx4 %0, %1, %2" : "=&v"(xa) : "v"(ox), "s"(x) : "memory");
  if (w == 3 && !isA) asm volatile("global_store_dword %0, %1, %2 sc1" :: "v"(ox), "v"(em), "s"(ws) : "memory");
  unsigned tag;
  asm volatile("s_load_dword %0, %1, 0x0\n\ts_barrier" : "=&s"(tag) : "s"(ws) : "memory");
  int po = lane * 16;
  u64 t_pub = 0;
  if (w < 2) {
    if (isA) {
      const v4u skv = mksrd(ckv, 16777216);
      const int so = (128 * ai + w) * 1024, vb = po ^ (w * 32);
#pragma unroll 1
      for (int i = 0; i < 8; ++i) {
        const int blk = ((2 * i) & 7) + (i >> 2);
        if (i == 1) { asm volatile("s_barrier\n\ts_barrier" ::: "memory"); t_pub = __builtin_amdgcn_s_memrealtime(); }
        if (i == 6) asm volatile("s_waitcnt vmcnt(16)\n\ts_barrier" ::: "memory");
#pragma unroll
        for (int r = 0; r < 8; ++r) dma16(skv, vb ^ ((r & 3) * 64 + (r >> 2) * 16), so + blk * 16384 + r * 2048, w * 1024 + blk * 16384 + r * 2048);
      }
      STAMP(15);
    } else {
      const v4u sk = mksrd(wuk, 4194304);
      const int vk = g * 8192 + (r16 ^ (2 * g)) * 16;
#pragma unroll 1
      for (int i = 0; i < 4; ++i) {
        if (i == KPRE) { asm volatile("s_barrier\n\ts_barrier" ::: "memory"); t_pub = __builtin_amdgcn_s_memrealtime(); }
#pragma unroll
        for (int n = 0; n < 4; ++n) dma16(sk, vk ^ ((n & 1) * 128 + (n >> 1) * 16), (128 * j + 64 * w + 16 * i + 4 * n) * 8192 + h * 256, QAI + (16 * w + 4 * i + n) * 1024);
      }
    }
  }
  SCHED();
  const rsrc_t rs = mkrs(ws, WS_END);
  unsigned mk[4];
#pragma unroll
  for (int k = 0; k < 4; ++k) { mk[k] = ((r16 >> 1) == k) ? ((r16 & 1) ? 0xFFFF0000u : 0x0000FFFFu) : 0u; PIN(mk[k]); }
  if (w >= 4) {
    unsigned mk2[4];
    v4u xm[4];
#pragma unroll
    for (int k = 0; k < 4; ++k) { mk2[k] = mk[k] | dppu<0x128>(mk[k]); PIN(mk2[k]); }
    int a_w0 = 116736 + (w - 4) * 1024 + (g >> 1) * 512 + r16 * 32 + (lane & 16);
    PIN(a_w0);
    asm volatile("s_waitcnt vmcnt(4)" : "+v"(xb) :: "memory");
#pragma unroll
    for (int k = 0; k < 2; ++k) {
      const unsigned xp = (r16 >= 8) ? 0x3F803F80u : xb[k];
      xm[2 * k] = xmask(__builtin_amdgcn_perm(xp, xp, 0x01000100u), mk2); xm[2 * k + 1] = xmask(__builtin_amdgcn_perm(xp, xp, 0x03020302u), mk2);
      PIN(xm[2 * k]); PIN(xm[2 * k + 1]);
    }
    SCHED();
    f4 acc = {0.f, 0.f, 0.f, 0.f};
    asm volatile("s_waitcnt vmcnt(0)" : "+v"(wqv[0]), "+v"(wqv[1]), "+v"(wqv[2]), "+v"(wqv[3]) :: "memory");
#pragma unroll
    for (int i = 0; i < 4; ++i) acc = mfma(xm[i], wqv[i], acc);
    STAMP(12);
    *lp<f4>(a_w0) = acc;
    BAR();
    asm volatile("s_barrier" ::: "memory");
  }
  float mb = 0.f, ri = 0.f;
  if (w == 2) {
    __builtin_amdgcn_s_setprio(3);
    asm volatile("s_waitcnt vmcnt(0)" : "+v"(xa) :: "memory");
    ln_stats(xa, mb, ri);
    PIN(mb); PIN(ri);
  }
  asm volatile("s_waitcnt lgkmcnt(0)" : "+s"(tag));
  tag += 1u;
  unsigned tv = tag;
  PIN(tv);
  if (w == 2) {
    int a_qp = ((lane & 31) * 2 + (lane >> 5)) * 16, a_r0 = 116736 + po;
    PIN(a_qp); PIN(a_r0);
    BAR();
    f4 p[4];
#pragma unroll
    for (int i = 0; i < 4; ++i) p[i] = *lp<f4>(a_r0 + i * 1024);
    const f4 s = (p[0] + p[1]) + (p[2] + p[3]);
    const auto r0 = __builtin_amdgcn_permlane32_swap(asu(s[0]), asu(s[2]), false, false), r1 = __builtin_amdgcn_permlane32_swap(asu(s[1]), asu(s[3]), false, false);
    stsc(v4u{asu((asf(r0[0]) - mb * asf(r0[1])) * ri), tv, asu((asf(r1[0]) - mb * asf(r1[1])) * ri), tv}, rs, a_qp, WS_QP + (h * 8 + jj) * 1024);
    asm volatile("s_barrier" ::: "memory");
    STAMP(1);
    __builtin_amdgcn_s_setprio(0);
    if (!isA) {
      v4u xn;
#pragma unroll
      for (int i = 0; i < 4; ++i) { const unsigned d = pk(bflo(xa[i]) - mb, bfhi(xa[i]) - mb); xn[i] = pk(bflo(d) * ri, bfhi(d) * ri); }
      *lp<v4u>(XNI + po) = xn;
    }
  }
  if (w == 3) {
    VMWAIT(0);
    if (!isA && lane == 0) __builtin_amdgcn_raw_buffer_store_b64(v2u{tv, tv}, rs, 0, WS_FL + cons * 8, AUX_SC1);
    asm volatile("s_barrier\n\ts_barrier" ::: "memory");
  }
  if (w >= 2) t_pub = __builtin_amdgcn_s_memrealtime();
#define DLY_G DELAY_G
#define DLY_W DELAY_W
#define DLY_H DELAY_H
#define DLY_U DELAY_U
#define DLY_A DELAY_A
#define DLY_Q DELAY_Q
#define DLY_O DELAY_O
#define DLY_F DELAY_F
  if (!isA) {
    const int xrow0 = 128 * j + 16 * w;
    COMMON_CONSTS
    int a_wk = QAI + w * 4096 + rbase, a_qw = 73728 + lane * 4, a_qr = 73728 + g * 64, a_qa = 74240 + w * 32 + g * 8;
    int a_wk4[4];
#pragma unroll
    for (int s = 0; s < 4; ++s) { a_wk4[s] = a_wk ^ (s * 16); PIN(a_wk4[s]); }
    PIN(a_qw); PIN(a_qr); PIN(a_qa);
    if (w == 4) {
      v4u p[8]; int spins = 0; unsigned bad;
      wait_until(t_pub + DLY_H);
      for (;;) {
        FENCE();
#pragma unroll
        for (int i = 0; i < 8; ++i) p[i] = ldsc(rs, po + (i & 3) * 1024, WS_QP + h * 8192 + (i >> 2) * 4096);
        bad = 0;
#pragma unroll
        for (int i = 0; i < 8; ++i) bad |= (p[i].y ^ tv) | (p[i].w ^ tv);
        PIN(bad);
        if (all_ok(bad == 0) || ++spins >= SPIN_CAP) break;
        __builtin_amdgcn_s_sleep(1);
      }
      const float q0 = ((asf(p[0].x) + asf(p[1].x)) + (asf(p[2].x) + asf(p[3].x))) + ((asf(p[4].x) + asf(p[5].x)) + (asf(p[6].x) + asf(p[7].x)));
      const float q1 = ((asf(p[0].z) + asf(p[1].z)) + (asf(p[2].z) + asf(p[3].z))) + ((asf(p[4].z) + asf(p[5].z)) + (asf(p[6].z) + asf(p[7].z)));
      *lp<unsigned>(a_qw) = pk(q0, q1);
    }
    if (w < 2) { VMWAIT(0); STAMP(13); }
    BAR();
    STAMP(2);
    v4u xm[4];
    {
      v4u qf[4], wkv[4];
#pragma unroll
      for (int s = 0; s < 4; ++s) { wkv[s] = lds128(a_wk4[s]); qf[s] = lds128(a_qr + s * 16); }
      SCHED();
      f4 acc = {0.f, 0.f, 0.f, 0.f};
#pragma unroll
      for (int s = 0; s < 4; ++s) acc = mfma(wkv[s], qf[s], acc);
      const float sc = 0.08838834764831845f * 1.4426950408889634f;
      if (r16 == 0) *lp<v2u>(a_qa) = v2u{pkc(acc[0] * sc, acc[1] * sc), pkc(acc[2] * sc, acc[3] * sc)};
    }
    BAR();
    if (w == 0 && lane < 16) stsc(lds128(74240 + po), rs, po, WS_QA + cons * 256);
    STAMP(3);
    {
      const v2u x4 = *lp<v2u>(XNI + (xrow0 + 4 * g) * 2);
      xm[0] = xmask(__builtin_amdgcn_perm(x4.x, x4.x, 0x01000100u), mk); xm[1] = xmask(__builtin_amdgcn_perm(x4.x, x4.x, 0x03020302u), mk);
      xm[2] = xmask(__builtin_amdgcn_perm(x4.y, x4.y, 0x01000100u), mk); xm[3] = xmask(__builtin_amdgcn_perm(x4.y, x4.y, 0x03020302u), mk);
#pragma unroll
      for (int i = 0; i < 4; ++i) PIN(xm[i]);
    }
    BAR();
    wait_until(t_pub + DLY_Q);
    v4u buv[4], bo[4][4], st;
    int tq[4];
#pragma unroll
    for (int t = 0; t < 4; ++t) tq[t] = (w >> 1) * 256 + (trbaseK ^ (32 * (4 * (w & 1) + t)));
    const v4u sws = mksrd(ws, WS_END);
    int a_st = (4 * g + (r16 & 3)) * 512, a_od = g * 1024 + r16 * 16, a_w1 = (g >> 1) * 12288 + w * 272 + r16 * 16 + (g & 1) * 8, a_r1 = (r16 & 7) * 272 + g * 16,
        a_w2 = 8192 + ((16 * w + r16 - 32 * j) & 127) * 2, a_r2 = 8192 + g * 16, a_o = g * 16384 + r16 * 8, a_sw = 4096 + w * 8, a_sr = 4096 + (g & 1) * 32;
    PIN(a_st); PIN(a_od); PIN(a_w1); PIN(a_r1); PIN(a_w2); PIN(a_r2); PIN(a_o); PIN(a_sw); PIN(a_sr);
    {
      const v4u suv = mksrd(wuv, 4194304), so = mksrd(wo, 4194304);
      const int so2 = (h * 128 + 8 * (w >> 2) + (w & 3)) * 1024;
#define WO4(n) _Pragma("unroll") for (int i = 0; i < 4; ++i) dmaw<(WO_NT >> (n)) & 1>(so, (i & 1) ? vrow1 : vrow0, so2 + ((((n) + j) & 3) * 32 + 16 * (i & 1) + 4 * (i >> 1)) * 1024, (32 * (n) + w + 8 * i) * 1024)
#define RD_UV() _Pragma("unroll") for (int ks = 0; ks < 4; ++ks) { const int a = QAI + (trbase ^ (32 * w)) + ks * 8192; const v2u lo = ldstr(a), hi = ldstr(a + 4096); buv[ks] = v4u{lo.x, lo.y, hi.x, hi.y}; }
#define RD_BO(n) _Pragma("unroll") for (int t = 0; t < 4; ++t) { const v2u lo = ldstr(tq[t] + (n) * 32768), hi = ldstr(tq[t] + (n) * 32768 + 16384); bo[t][n] = v4u{lo.x, lo.y, hi.x, hi.y}; }
      v4u fw[4];
      const GAS char* pf = (const GAS char*)((r16 < 8) ? wg : wu) + (h < 28 ? (xrow0 + 4 * g + 1) * 3584 + h * 128 : 3584) + (r16 & 7) * 16;
      const int vo1 = g * 16 * 8192 + dchunk, so1 = (128 * j + 2 * (w & 3) + (w >> 2)) * 8192 + h * 256;
#define SB() asm volatile("s_barrier" ::: "memory")
#define WB(n) asm volatile("s_waitcnt vmcnt(" #n ") lgkmcnt(0)\n\ts_barrier" ::: "memory")
#define GU4() asm volatile("global_load_dwordx4 %0, %4, off offset:-3584 nt\n\tglobal_load_dwordx4 %1, %4, off nt\n\tglobal_load_dwordx4 %2, %4, off offset:3584 nt\n\tglobal_load_dwordx4 %3, %5, off nt" \
                   : "=&v"(fw[0]), "=&v"(fw[1]), "=&v"(fw[2]), "=&v"(fw[3]) : "v"(pf), "v"(pf + 7168) : "memory")
#define UV4() _Pragma("unroll 1") for (int n = 0; n < 4; ++n) dma16(suv, vo1, so1 + (64 * (n >> 1) + 8 * (n & 1)) * 8192, QAI + (32 * n + 4 * w) * 256)
#define S1(scr) { \
      asm volatile("s_waitcnt vmcnt(4)" : "+v"(fw[0]), "+v"(fw[1]), "+v"(fw[2]), "+v"(fw[3]) :: "memory"); \
      f4 acc = {0.f, 0.f, 0.f, 0.f}; \
      _Pragma("unroll") for (int i = 0; i < 4; ++i) acc = mfma(xm[i], fw[i], acc); \
      if (lane < 32) *lp<f4>((scr) + w * 512 + r16 * 32 + (lane & 16)) = acc; \
      BAR(); \
      if (w == 0) { \
        f2 p[8]; \
        _Pragma("unroll") for (int i = 0; i < 8; ++i) p[i] = *lp<f2>((scr) + lane * 8 + i * 512); \
        const f2 s = ((p[0] + p[1]) + (p[2] + p[3])) + ((p[4] + p[5]) + (p[6] + p[7])); \
        stsc(v4u{asu(s.x), tv, asu(s.y), tv}, rs, po, WS_FP + cons * 1024); \
        STAMP(15); \
      } \
      SB(); }
#define ST1() asm volatile("buffer_load_dwordx4 %0, %1, %2, %3 offen sc1" : "=&v"(st) : "v"(a_st), "s"(sws), "s"(WS_ST + w * 8192 + h * 16) : "memory")
      GU4(); SB(); WO4(0); S1(102400)
      WO4(1); SB(); WO4(2); WB(8); UV4(); RD_BO(0) WB(8); WO4(3); RD_BO(1) WB(8); ST1(); RD_BO(2) WB(5); RD_UV()
      asm volatile("s_waitcnt vmcnt(0)" : "+v"(st) :: "memory");
      STAMP(14);
    }
    v4u od[4];
    {
      for (int spins = 0; !all_ok((st.y == tv) & (st.w == tv)) && spins < SPIN_CAP; ++spins)
        asm volatile("buffer_load_dwordx4 %0, %1, %2, %3 offen sc1\n\ts_waitcnt vmcnt(0)" : "=&v"(st) : "v"(a_st), "s"(sws), "s"(WS_ST + w * 8192 + h * 16) : "memory");
      STAMP(6);
#pragma unroll
      for (int i = 0; i < 4; ++i) od[i] = ldsc(rs, a_od + i * 256, WS_OP + (cons * 128 + 16 * w) * 256);
      SCHED();
      const float m = asf(st.x);
      float mw = fmx(m, dpp<0xB1>(m)); mw = fmx(mw, dpp<0x4E>(mw)); mw = max32(max16(mw));
      const float e = ex2(m - mw);
      const unsigned wb = pk(e, e);
      xm[0] = xmask(dppu<0x00>(wb), mk); xm[1] = xmask(dppu<0x55>(wb), mk); xm[2] = xmask(dppu<0xAA>(wb), mk); xm[3] = xmask(dppu<0xFF>(wb), mk);
      float lw = bflo(wb) * asf(st.z);
      lw += dpp<0xB1>(lw); lw += dpp<0x4E>(lw); lw = add32(add16(lw));
      *lp<f2>(a_sw) = f2{mw, lw};
#pragma unroll
      for (int i = 0; i < 4; ++i) PIN(xm[i]);
      SCHED();
    }
    {
      int spins = 0; f4 acc;
      for (;;) {
        unsigned bad = 0;
        acc = f4{0.f, 0.f, 0.f, 0.f};
#pragma unroll
        for (int i = 0; i < 4; ++i) { acc = mfma(xm[i], od[i], acc); bad = umax4(bad, od[i]); }
        if (all_ok(bad != EMPTY) || ++spins >= SPIN_CAP) break;
        FENCE();
#pragma unroll
        for (int i = 0; i < 4; ++i) od[i] = ldsc(rs, a_od + i * 256, WS_OP + (cons * 128 + 16 * w) * 256);
      }
      *lp<v2u>(a_w1) = v2u{pk(acc[0], acc[1]), pk(acc[2], acc[3])};
    }
    STAMP(7);
    BAR();
    STAMP(4);
    float ri;
    {
      v4u af[4];
      const f4 s0 = *lp<f4>(a_sr), s1 = *lp<f4>(a_sr + 16);
#pragma unroll
      for (int ks = 0; ks < 4; ++ks) af[ks] = lds128(a_r1 + ks * 64);
      RD_BO(3)
      SCHED();
      const float M = max16(fmx(mx3(s0[0], s0[2], s1[0]), s1[2]));
      f4 acc = {0.f, 0.f, 0.f, 0.f};
#pragma unroll
      for (int ks = 0; ks < 4; ++ks) acc = mfma(af[ks], buv[ks], acc);
      const float f0 = ex2(s0[0] - M), f1 = ex2(s0[2] - M), f2_ = ex2(s1[0] - M), f3 = ex2(s1[2] - M);
      const float c = add16((f0 * acc[0] + f1 * acc[1]) + (f2_ * acc[2] + f3 * acc[3]));
      *lp<u16>(a_w2) = (u16)pk(c, c);
      SCHED();
      ri = rcp(add16((f0 * s0[1] + f1 * s0[3]) + (f2_ * s1[1] + f3 * s1[3])));
      PIN(ri);
    }
    STAMP(9);
    BAR();
    STAMP(10);
    {
      v4u cf[4];
#pragma unroll
      for (int ks = 0; ks < 4; ++ks) cf[ks] = lds128(a_r2 + ks * 64);
      SCHED();
      f4 acc[4];
#pragma unroll
      for (int t = 0; t < 4; ++t) acc[t] = f4{0.f, 0.f, 0.f, 0.f};
#pragma unroll
      for (int ks = 0; ks < 4; ++ks)
#pragma unroll
        for (int t = 0; t < 4; ++t) acc[t] = mfma(cf[ks], bo[t][ks], acc[t]);
      const float v01 = (g & 1) ? acc[1][0] : acc[0][0], v23 = (g & 1) ? acc[3][0] : acc[2][0];
      float v = ((g & 2) ? v23 : v01) * ri;
      PIN(v); STAMP(11);
      __builtin_amdgcn_raw_buffer_store_b64(v2u{asu(v), tv}, rs, a_o, WS_SP + (4 * w * 128 + cons) * 128, AUX_SC1);
    }
    STAMP(8);
    STAMP_OUT();
    __builtin_amdgcn_endpgm();
  }
  {
    const v4u sws = mksrd(ws, WS_END);
    const int ht = w & 1, kt = w >> 1;
    v4u k0[16], k1[16];
    v2u v0[4][4];
    const rsrc_t rx = mkrs(x, 1024);
    COMMON_CONSTS
    const int mi = ai & 3, fi = (ai < 112) ? (ai >> 2) : 0;
    int ka[4], ta[4], qa_[4], vo0 = vrow0, vo1 = vrow1;
#pragma unroll
    for (int s = 0; s < 4; ++s) {
      ka[s] = kt * 32768 + (rbaseK ^ (s * 16)); ta[s] = (w >> 1) * 256 + (trbaseK ^ (32 * (4 * (w & 1) + s))); qa_[s] = ka[s] + (QAI + ht * 16384 - kt * 32768);
      PIN(ka[s]); PIN(ta[s]); PIN(qa_[s]);
    }
    int a_d = (2 * g + (r16 >> 3)) * 1024 + (r16 & 7) * 16, a_mw = (16 * ht + r16) * 16 + kt * 4,
        a_s = (lane < 16) ? (16 * ht + r16) * 16 : WS_END, a_o = (16 * g + (w >> 1)) * 32768 + (w & 1) * 128 + r16 * 8, a_pq = QAI + po,
        a_c = (16 * w + g) * 32768 + r16 * 16;
    int a_mr = a_mw & ~15, a_lw = 2048 + g * 512 + a_mw, a_pw = a_pq + w * 1024;
    PIN(a_mr); PIN(a_lw); PIN(a_pw);
    PIN(a_mw); PIN(a_s); PIN(a_o); PIN(a_pq); PIN(a_c); PIN(vo0); PIN(vo1);
#define RD_U0() _Pragma("unroll") for (int i = 0; i < 4; ++i) _Pragma("unroll") for (int s = 0; s < 4; ++s) { k0[4 * i + s] = lds128(ka[s] + i * 256); v0[i][s] = ldstr(ta[s] + i * 32768); }
#define RD_K1() _Pragma("unroll") for (int s = 0; s < 16; ++s) k1[s] = lds128(ka[s & 3] + (s >> 2) * 256 + 16384)
#define FLAGS_OK() all_ok(((fl.x ^ tv) | (fl.y ^ tv) | (fl.z ^ tv) | (fl.w ^ tv)) == 0)
#define QA4() _Pragma("unroll") for (int q = 0; q < 4; ++q) dma16sc(sws, (q & 1) ? vo1 : vo0, WS_QA + (w + 8 * q) * 1024, QAI + (w + 8 * q) * 1024)
    if (w >= 2) {
      v4u fl;
      if (w == 3) {
        __builtin_amdgcn_s_setprio(3);
      }
      asm volatile("s_barrier" ::: "memory");
      if (w == 3) asm volatile("buffer_load_dwordx4 %0, %1, %2, %3 offen sc1" : "=&v"(fl) : "v"(po), "s"(sws), "s"(WS_FL) : "memory");
      RD_U0()
      if (w == 3) {
        asm volatile("s_waitcnt vmcnt(0)" : "+v"(fl) :: "memory");
        int spins = 0;
        while (!FLAGS_OK() && ++spins < SPIN_CAP)
          asm volatile("buffer_load_dwordx4 %0, %1, %2, %3 offen sc1\n\ts_waitcnt vmcnt(0)" : "=&v"(fl) : "v"(po), "s"(sws), "s"(WS_FL) : "memory");
        __builtin_amdgcn_s_setprio(0);
      }
    }
    asm volatile("s_waitcnt vmcnt(16)\n\ts_barrier" ::: "memory");
    STAMP(2);
    const bool early = (long long)(__builtin_amdgcn_s_memrealtime() - (t_pub + DLY_G)) >= 0;
    if (early) { QA4(); }
    if (w < 2) { RD_U0() }
    if (kt < 2) { RD_K1(); }
    v4u vf[4][4];
#pragma unroll
    for (int i = 0; i < 8; ++i) { const v2u hi = ldstr(ta[i & 3] + (i >> 2) * 32768 + 16384); vf[i >> 2][i & 3] = v4u{v0[i >> 2][i & 3].x, v0[i >> 2][i & 3].y, hi.x, hi.y}; }
    if (!early) { wait_until(t_pub + DLY_G); QA4(); }
    VMWAIT(4);
    BAR();
    STAMP(3);
    if (kt >= 2) { RD_K1(); }
#define RD_V1(i) { const v2u hi = ldstr(ta[(i) & 3] + ((i) >> 2) * 32768 + 16384); vf[(i) >> 2][(i) & 3] = v4u{v0[(i) >> 2][(i) & 3].x, v0[(i) >> 2][(i) & 3].y, hi.x, hi.y}; }
    f4 S0 = {0.f, 0.f, 0.f, 0.f}, S1 = {0.f, 0.f, 0.f, 0.f};
    PIN(S0); PIN(S1);
    for (int spins = 0;; ++spins) {
      VMWAIT(0);
      unsigned bad = 0;
#pragma unroll
      for (int q = 0; q < 4; ++q) bad = umax4(bad, lds128(a_pw + q * 8192));
      if (all_ok(bad != EMPTY) || spins >= SPIN_CAP) break;
      QA4();
    }
    BAR();
    STAMP(4);
    {
      v4u qf[16];
#pragma unroll
      for (int s = 0; s < QD; ++s) qf[s] = lds128(qa_[s & 3] + (s >> 2) * 256);
      SCHED();
#pragma unroll
      for (int s = 0; s < 16; ++s) {
        mfma_acc(S0, k0[s], qf[s]); mfma_acc(S1, k1[s], qf[s]);
        if (s + QD < 16) qf[s + QD] = lds128(qa_[(s + QD) & 3] + ((s + QD) >> 2) * 256);
        if (s >= 8) RD_V1(s)
        if (s < 8 && !(s & 1)) stsc(v4u{EMPTY, EMPTY, EMPTY, EMPTY}, rs, a_c, WS_OP + (s >> 1) * 131072 + ai * 256);
        SCHED();
      }
      float m0, mt;
      asm volatile("s_nop 7\n\tv_max3_f32 %0, %2, %3, %4\n\tv_max3_f32 %0, %0, %5, %6\n\tv_max3_f32 %1, %7, %8, %9\n\tv_max_f32 %0, %0, %1\n\t"
                   "v_mov_b32 %1, %0\n\ts_nop 1\n\tv_permlane16_swap_b32 %0, %1\n\tv_max_f32 %0, %0, %1\n\t"
                   "v_mov_b32 %1, %0\n\ts_nop 1\n\tv_permlane32_swap_b32 %0, %1\n\tv_max_f32 %0, %0, %1"
                   : "=&v"(m0), "=&v"(mt) : "v"(S0[0]), "v"(S0[1]), "v"(S0[2]), "v"(S0[3]), "v"(S1[0]), "v"(S1[1]), "v"(S1[2]), "v"(S1[3]));
      *lp<float>(a_mw) = m0;
    }
    BAR();
    STAMP(5);
    float M;
    int a_f = (r16 & 3) * 1024 + (64 * ((r16 >> 2) & 1) + 8 * (r16 >> 3) + 2 * g) * 8;
    {
      const f4 m4 = *lp<f4>(a_mr);
      PIN(a_f); PIN(a_d);
      asm("v_max3_f32 %0, %1, %2, %3\n\tv_max_f32 %0, %0, %4" : "=&v"(M) : "v"(m4[0]), "v"(m4[1]), "v"(m4[2]), "v"(m4[3]));
      float p[8];
#pragma unroll
      for (int i = 0; i < 4; ++i) { p[i] = ex2(S0[i] - M); p[4 + i] = ex2(S1[i] - M); }
      *lp<v4u>(a_pw) = v4u{pk(p[0], p[1]), pk(p[2], p[3]), pk(p[4], p[5]), pk(p[6], p[7])};
      *lp<float>(a_lw) = ((p[0] + p[1]) + (p[2] + p[3])) + ((p[4] + p[5]) + (p[6] + p[7]));
    }
    VMWAIT(0);
    v4u dw[2], fp;
    asm volatile("buffer_load_dwordx4 %0, %1, %2, %3 offen sc1" : "=&v"(fp) : "v"(a_f), "s"(sws), "s"(WS_FP + fi * 4096 + mi * 128) : "memory");
    BAR();
    STAMP(6);
    {
      v4u pf_[8];
      pf_[0] = lds128(a_pq);
      const f4 l4 = *lp<f4>(a_lw & ~15);
#pragma unroll
      for (int i = 1; i < 8; ++i) pf_[i] = lds128(a_pq + ((i & 3) * 2 + (i >> 2)) * 1024);
      SCHED();
#pragma unroll
      for (int hh = 0; hh < 2; ++hh) {
        f4 O[4];
#pragma unroll
        for (int t = 0; t < 4; ++t) O[t] = f4{0.f, 0.f, 0.f, 0.f};
#pragma unroll
        for (int ks = 0; ks < 4; ++ks) {
#pragma unroll
          for (int t = 0; t < 4; ++t) O[t] = mfma(pf_[4 * hh + ks], vf[ks][t], O[t]);
          if (hh == 0 && ks == 0) { SCHED(); if (kt == 0) stsc(v4u{asu(M), tv, asu(add32(add16((l4[0] + l4[1]) + (l4[2] + l4[3])))), tv}, rs, a_s, WS_ST + ai * 512); SCHED(); }
        }
        if (hh == 1) { PIN(O[3]); STAMP(13); }
#pragma unroll
        for (int i = 0; i < 4; ++i)
          __builtin_amdgcn_raw_buffer_store_b64(v2u{pkc(O[0][i], O[1][i]), pkc(O[2][i], O[3][i])}, rs, a_o, WS_OP + (16 * hh + i) * 131072 + ai * 256, AUX_SC1);
      }
    }
    STAMP(7);
    const u64 t_pv = __builtin_amdgcn_s_memrealtime();
    asm volatile("s_waitcnt vmcnt(8)" : "+v"(fp) :: "memory");
    if (ai < 112) {
      wait_until(t_pv + DLY_U);
      const rsrc_t rd = mkrs(wd, 1835008);
#pragma unroll
      for (int i = 0; i < 2; ++i) dw[i] = ldnt(rd, a_d, (64 * fi + 16 * mi + 8 * i) * 1024 + w * 128);
      SCHED();
      const int gu = (r16 >> 2) & 1, hi = r16 >> 3;
      v4u p = fp;
      int spins = 0;
      for (; !all_ok((p.y == tv) & (p.w == tv)) && spins < SPIN_CAP; ++spins)
        asm volatile("buffer_load_dwordx4 %0, %1, %2, %3 offen sc1\n\ts_waitcnt vmcnt(0)" : "=&v"(p) : "v"(a_f), "s"(sws), "s"(WS_FP + fi * 4096 + mi * 128) : "memory");
      STAMP(14);
      float v0_ = asf(p.x), v1_ = asf(p.z);
      v0_ += dpp<0xB1>(v0_); v1_ += dpp<0xB1>(v1_);
      v0_ += dpp<0x4E>(v0_); v1_ += dpp<0x4E>(v1_);
      const float o0 = dpp<0x141>(v0_), o1 = dpp<0x141>(v1_);
      float hv[2];
#pragma unroll
      for (int e = 0; e < 2; ++e) {
        const float gt = rbf(gu ? (e ? o1 : o0) : (e ? v1_ : v0_)), up = rbf(gu ? (e ? v1_ : v0_) : (e ? o1 : o0));
        const float z2 = 2.0f * 0.7978845608028654f * 1.4426950408889634f * (gt + 0.044715f * gt * gt * gt);
        hv[e] = rbf(gt * rcp(1.0f + ex2(-z2))) * up;
      }
      const float t0 = dpp<0x128>(hv[0]), t1 = dpp<0x128>(hv[1]);
      unsigned mk2[4];
#pragma unroll
      for (int k = 0; k < 4; ++k) mk2[k] = mk[k] | dppu<0x128>(mk[k]);
      const float ha = hi ? t1 : hv[0], hb = hi ? hv[1] : t0;
      f4 acc = {0.f, 0.f, 0.f, 0.f};
      acc = mfma(xmask(pk(ha, ha), mk2), dw[0], acc);
      acc = mfma(xmask(pk(hb, hb), mk2), dw[1], acc);
      float y[4];
#pragma unroll
      for (int i = 0; i < 4; ++i) {
        const unsigned u = asu(dpp<0x128>(acc[i]));
        auto r = __builtin_amdgcn_permlane32_swap(u, u, false, false);
        y[i] = acc[i] + asf(r[1]);
      }
      const auto ya = __builtin_amdgcn_permlane32_swap(asu(y[0]), asu(y[2]), false, false), yb = __builtin_amdgcn_permlane32_swap(asu(y[1]), asu(y[3]), false, false);
      if (r16 < 8) stsc(v4u{ya[0], tv, yb[0], tv}, rs, (r16 >> 1) * 16384 + ((r16 & 1) * 8 + 4 * (g & 1) + 2 * (g >> 1)) * 8, WS_YP + (4 * w * 128 + ai) * 128);
    }
    STAMP(8);
    if (w >= 6) {
      const int off = (lane >> 1) * 128 + (lane & 1) * 16, so = ((w == 7) ? WS_SP : WS_YP) + h * 16384 + j * 32;
      const unsigned xo = __builtin_amdgcn_raw_buffer_load_b32(rx, (lane & 1) * 4, h * 32 + j * 8, 0);
      const bool skip3 = (w == 6) && (lane >= 32);
      v4u p[4]; int spins = 0; unsigned bad;
      {
        const int go = so + ((ai * 5 + 3) % 112) * 128;
        wait_until(t_pv + 80);
        for (int sp = 0; sp < SPIN_CAP; ++sp) {
          FENCE();
          const v2u t = __builtin_amdgcn_raw_buffer_load_b64(rs, 0, go, AUX_SC1);
          if (__builtin_amdgcn_readfirstlane(t.y) == tag) break;
          __builtin_amdgcn_s_sleep(2);
        }
        wait_until(__builtin_amdgcn_s_memrealtime() + ((w == 7) ? DLY_F : DLY_W));
      }
      do {
        FENCE();
#pragma unroll
        for (int n = 0; n < 4; ++n) p[n] = ldsc(rs, off, so + n * 4096);
        if (skip3) p[3] = v4u{0u, tv, 0u, tv};
        bad = 0;
#pragma unroll
        for (int n = 0; n < 4; ++n) bad |= (p[n].y ^ tv) | (p[n].w ^ tv);
        PIN(bad);
      } while (!all_ok(bad == 0) && ++spins < SPIN_CAP);
      float a0 = (asf(p[0].x) + asf(p[1].x)) + (asf(p[2].x) + asf(p[3].x));
      float a1 = (asf(p[0].z) + asf(p[1].z)) + (asf(p[2].z) + asf(p[3].z));
      a0 += dpp<0x128>(a0); a1 += dpp<0x128>(a1);
      a0 += dpp<0x124>(a0); a1 += dpp<0x124>(a1);
      a0 += dpp<0x122>(a0); a1 += dpp<0x122>(a1);
      a0 = rbf(add32(add16(a0))); a1 = rbf(add32(add16(a1)));
      if (w == 6) {
        a0 = rbf(a0 * rcp(1.0f + ex2(-1.4426950408889634f * a0)));
        a1 = rbf(a1 * rcp(1.0f + ex2(-1.4426950408889634f * a1)));
        if (lane < 2) *lp<f2>(1024 + lane * 8) = f2{a0, a1};
        STAMP(9);
        BAR();
      } else {
        BAR();
        const f2 gt = *lp<f2>(1024 + (lane & 1) * 8);
        const unsigned o = pk(bflo(xo) + rbf(a0 * gt.x), bfhi(xo) + rbf(a1 * gt.y));
        if (lane < 2) *(GAS unsigned*)(out + 16 * h + 4 * j + 2 * lane) = o;
        if (ai == 0 && lane == 0) *(GAS unsigned*)ws = tag;
        STAMP(10);
      }
    }
  }
  STAMP_OUT();
}

extern "C" void kernel_launch(void* const* d_in, const int* in_sizes, int n_in, void* d_out, int out_size, void* d_ws, size_t ws_size, hipStream_t stream) {
  (void)hipFuncSetAttribute((const void*)mla_decode, hipFuncAttributeMaxDynamicSharedMemorySize, LDS_BYTES);
  mla_decode<<<256, 512, LDS_BYTES, stream>>>((const u16*)d_in[0], (const u16*)d_in[2], (unsigned char*)d_ws + WS_BASE(ws_size), (const u16*)d_in[3], (const u16*)d_in[1], (const u16*)d_in[4], (const u16*)d_in[5],
                                              (const u16*)d_in[7], (const u16*)d_in[8], (const u16*)d_in[9], (u16*)d_out, d_in[0]);
}
```

```cpp
#include <hip/hip_runtime.h>
#include <stdint.h>

typedef unsigned short u16;
typedef unsigned long long u64;
typedef unsigned v4u __attribute__((ext_vector_type(4)));
typedef unsigned v2u __attribute__((ext_vector_type(2)));
typedef unsigned v8u __attribute__((ext_vector_type(8)));
typedef unsigned v16u __attribute__((ext_vector_type(16)));
typedef float f4 __attribute__((ext_vector_type(4)));
typedef float f2 __attribute__((ext_vector_type(2)));
typedef __bf16 bf8 __attribute__((ext_vector_type(8)));
typedef __bf16 bf2 __attribute__((ext_vector_type(2)));
typedef short s4 __attribute__((ext_vector_type(4)));
#define GAS __attribute__((address_space(1)))
#define LAS __attribute__((address_space(3)))
#define CAS __attribute__((address_space(4)))

#define DELAY_A 0
#define DELAY_Q 195
#define DELAY_O 0

#define DELAY_F 0
#define WS_BASE(n) 0
#define DELAY_G 220
#define DELAY_U 30
#define DELAY_H 45
#define ROLE_A_FIRST 1
#define ROLE_PARITY 1
#define KV_NT 1
#define WK_NT 1
#define UV_NT 1
#define WQ_NT " nt"
#define GU_NT " nt"
#define WO_NT 9
#define KPRE 3
#define QD 6
#define DELAY_W 0
constexpr int WS_FL = 1024;
constexpr int WS_FL2 = 2048;
constexpr int WS_QP = 4096;
constexpr int WS_QA = WS_QP + 262144;
constexpr int WS_FP = WS_QA + 32768;
constexpr int WS_ST = WS_FP + 131072;
constexpr int WS_SP = WS_ST + 262144;
constexpr int WS_YP = WS_SP + 524288;
constexpr int WS_OP = WS_YP + 524288;
constexpr int WS_END = WS_OP + 4194304;
constexpr int WS_STAMP = 6 << 20;
constexpr int WS_TUNE = 64;
constexpr int LDS_BYTES = 163840;
constexpr int CODE_BYTES = 14336;
constexpr int SPIN_CAP = 1 << 16;
constexpr int AUX_SC1 = 16, AUX_NT = 2;
constexpr int QAI = 131072;
constexpr int XNI = 65536;
constexpr unsigned EMPTY = 0xFFFFFFFFu;

#define BAR() asm volatile("s_waitcnt lgkmcnt(0)\n\ts_barrier" ::: "memory")
#define FENCE() asm volatile("" ::: "memory")
#define SCHED() __builtin_amdgcn_sched_barrier(0)
#define VMWAIT(n) asm volatile("s_waitcnt vmcnt(" #n ")" ::: "memory")
#define PIN(x) asm volatile("" : "+v"(x))
#define PINS(x) asm volatile("" : "+s"(x))

#define STAMP(i) do {} while (0)
#define STAMP_OUT()

typedef __amdgpu_buffer_rsrc_t rsrc_t;
__device__ __forceinline__ float bflo(unsigned u) { return __uint_as_float(u << 16); }
__device__ __forceinline__ float bfhi(unsigned u) { return __uint_as_float(u & 0xFFFF0000u); }
__device__ __forceinline__ unsigned pk(float a, float b) { f2 f = {a, b}; return __builtin_bit_cast(unsigned, __builtin_convertvector(f, bf2)); }
__device__ __forceinline__ unsigned pkc(float a, float b) { return min(pk(a, b), EMPTY - 1u); }
__device__ __forceinline__ float rbf(float a) { return bflo(pk(a, a)); }
__device__ __forceinline__ float asf(unsigned u) { return __uint_as_float(u); }
__device__ __forceinline__ unsigned asu(float f) { return __float_as_uint(f); }
__device__ __forceinline__ float ex2(float x) { return __builtin_amdgcn_exp2f(x); }
__device__ __forceinline__ float rcp(float x) { return __builtin_amdgcn_rcpf(x); }
__device__ __forceinline__ f4 mfma(v4u a, v4u b, f4 c) {
  return __builtin_amdgcn_mfma_f32_16x16x32_bf16(__builtin_bit_cast(bf8, a), __builtin_bit_cast(bf8, b), c, 0, 0, 0);
}
__device__ __forceinline__ void mfma_acc(f4& c, v4u a, v4u b) { asm volatile("v_mfma_f32_16x16x32_bf16 %0, %1, %2, %0" : "+v"(c) : "v"(a), "v"(b)); }
template <int CTRL> __device__ __forceinline__ float dpp(float v) {
  return __builtin_bit_cast(float, __builtin_amdgcn_update_dpp(0, __builtin_bit_cast(int, v), CTRL, 0xf, 0xf, true));
}
template <int CTRL> __device__ __forceinline__ unsigned dppu(unsigned v) { return (unsigned)__builtin_amdgcn_update_dpp(0, (int)v, CTRL, 0xf, 0xf, true); }
__device__ __forceinline__ float add16(float v) { unsigned u = asu(v); auto r = __builtin_amdgcn_permlane16_swap(u, u, false, false); return asf(r[0]) + asf(r[1]); }
__device__ __forceinline__ float add32(float v) { unsigned u = asu(v); auto r = __builtin_amdgcn_permlane32_swap(u, u, false, false); return asf(r[0]) + asf(r[1]); }
__device__ __forceinline__ float fmx(float a, float b) { return __builtin_fmaxf(a, b); }
__device__ __forceinline__ float mx3(float a, float b, float c) { return fmx(fmx(a, b), c); }
__device__ __forceinline__ float max16(float v) { unsigned u = asu(v); auto r = __builtin_amdgcn_permlane16_swap(u, u, false, false); return fmx(asf(r[0]), asf(r[1])); }
__device__ __forceinline__ float max32(float v) { unsigned u = asu(v); auto r = __builtin_amdgcn_permlane32_swap(u, u, false, false); return fmx(asf(r[0]), asf(r[1])); }
__device__ __forceinline__ float wave_sum(float v) {
  v += dpp<0x128>(v); v += dpp<0x124>(v); v += dpp<0x122>(v); v += dpp<0x121>(v);
  return add32(add16(v));
}
__device__ __forceinline__ unsigned umax4(unsigned acc, v4u v) { return max(max(max(acc, v.x), v.y), max(v.z, v.w)); }
__device__ __forceinline__ rsrc_t mkrs(const void* p, int bytes) { return __builtin_amdgcn_make_buffer_rsrc((void*)p, 0, bytes, 0x00020000); }
__device__ __forceinline__ v4u mksrd(const void* p, unsigned bytes) { const u64 a = (u64)p; return v4u{(unsigned)a, (unsigned)(a >> 32) & 0xFFFFu, bytes, 0x00020000u}; }
__device__ __forceinline__ v4u ldnt(rsrc_t r, int voff, int soff) { return __builtin_amdgcn_raw_buffer_load_b128(r, voff, soff, AUX_NT); }
__device__ __forceinline__ v4u ldsc(rsrc_t r, int voff, int soff) { return __builtin_amdgcn_raw_buffer_load_b128(r, voff, soff, AUX_SC1); }
__device__ __forceinline__ void stsc(v4u v, rsrc_t r, int voff, int soff) { __builtin_amdgcn_raw_buffer_store_b128(v, r, voff, soff, AUX_SC1); }
__device__ __forceinline__ void dma16(v4u srd, int voff, int soff, int lds_dst) {
  asm volatile("s_mov_b32 m0, %3\n\ts_nop 0\n\tbuffer_load_dwordx4 %0, %1, %2 offen nt lds" :: "v"(voff), "s"(srd), "s"(soff), "s"(lds_dst) : "memory");
}
__device__ __forceinline__ void dma16p(v4u srd, int voff, int soff, int lds_dst) {
  asm volatile("s_mov_b32 m0, %3\n\ts_nop 0\n\tbuffer_load_dwordx4 %0, %1, %2 offen lds" :: "v"(voff), "s"(srd), "s"(soff), "s"(lds_dst) : "memory");
}
template <int NT> __device__ __forceinline__ void dmaw(v4u srd, int voff, int soff, int lds_dst) { if (NT) dma16(srd, voff, soff, lds_dst); else dma16p(srd, voff, soff, lds_dst); }
__device__ __forceinline__ void dma16sc(v4u srd, int voff, int soff, int lds_dst) {
  asm volatile("s_mov_b32 m0, %3\n\ts_nop 0\n\tbuffer_load_dwordx4 %0, %1, %2 offen sc1 lds" :: "v"(voff), "s"(srd), "s"(soff), "s"(lds_dst) : "memory");
}
template <typename T> __device__ __forceinline__ LAS T* lp(int off) { return (LAS T*)(uintptr_t)(unsigned)off; }
__device__ __forceinline__ v4u lds128(int off) { return *lp<v4u>(off); }
__device__ __forceinline__ v2u ldstr(int off) { return __builtin_bit_cast(v2u, __builtin_amdgcn_ds_read_tr16_b64_v4i16(lp<s4>(off))); }
__device__ __forceinline__ int swz(int r) { return ((r & 7) << 1) | ((r >> 3) & 1); }
__device__ __forceinline__ v4u xmask(unsigned xx, const unsigned (&mk)[4]) { return v4u{xx & mk[0], xx & mk[1], xx & mk[2], xx & mk[3]}; }
__device__ __forceinline__ bool all_ok(bool ok) { return __builtin_amdgcn_ballot_w64(!ok) == 0ull; }
__device__ __forceinline__ void wait_until(u64 t) { while ((long long)(__builtin_amdgcn_s_memrealtime() - t) < 0) __builtin_amdgcn_s_sleep(1); }

__device__ __forceinline__ void ln_stats(v4u xa, float& mb, float& ri) {
  float v[8] = {bflo(xa.x), bfhi(xa.x), bflo(xa.y), bfhi(xa.y), bflo(xa.z), bfhi(xa.z), bflo(xa.w), bfhi(xa.w)};
  const float mean = wave_sum(((v[0] + v[1]) + (v[2] + v[3])) + ((v[4] + v[5]) + (v[6] + v[7]))) * (1.0f / 512.0f);
  float q0 = 0.f, q1 = 0.f;
#pragma unroll
  for (int i = 0; i < 4; ++i) { const float d = v[i] - mean, e = v[i + 4] - mean; q0 += d * d; q1 += e * e; }
  const float var = wave_sum(q0 + q1) * (1.0f / 512.0f);
  mb = rbf(mean);
  ri = rcp(rbf(__builtin_amdgcn_sqrtf(rbf(rbf(var) + 1e-5f))));
}

__device__ __forceinline__ v4u layernorm8(v4u xa) {
  float mb, ri;
  ln_stats(xa, mb, ri);
  v4u o;
#pragma unroll
  for (int i = 0; i < 4; ++i) { const unsigned d = pk(bflo(xa[i]) - mb, bfhi(xa[i]) - mb); o[i] = pk(bflo(d) * ri, bfhi(d) * ri); }
  return o;
}

typedef u64 v8q __attribute__((ext_vector_type(8)));
typedef u64 v4q __attribute__((ext_vector_type(4)));
extern "C" __global__ void __launch_bounds__(512, 2) mla_decode(const void*, const void*, void*, const void*, const void*, const void*, const void*, const void*,
                                                      const void*, const void*, void*, const void*) {
  v8q ka0; v4q ka1; u64 t_start;
  asm volatile("s_load_dwordx16 %0, %3, 0x0\n\ts_load_dwordx8 %1, %3, 0x40\n\ts_memrealtime %2" : "=&s"(ka0), "=&s"(ka1), "=&s"(t_start) : "s"(__builtin_amdgcn_kernarg_segment_ptr()));
  const int b = blockIdx.x, tid = threadIdx.x, lane = tid & 63, w = __builtin_amdgcn_readfirstlane(tid >> 6);
  const int xcd = b & 7, rr = (b >> 3) & 15;
  const bool isA = (b & 1) == 0;
  const int r16 = lane & 15, g = lane >> 4;
  const int h = (xcd >> 1) * 8 + ((b >> 3) & 7), j = b >> 6;
  const int ai = b >> 1;
  {
    u64 pc; asm volatile("s_getpc_b64 %0" : "=s"(pc));
    asm volatile("s_mov_b32 m0, %2\n\ts_nop 0\n\tglobal_load_lds_dword %0, %1" :: "v"(min(((b >> 3) * 8 + w) * 128, CODE_BYTES - 128) + r16 * 8), "s"(pc & ~255ull), "s"(114688) : "memory");
  }
#define COMMON_CONSTS \
  const int dchunk = (r16 ^ swz(4 * (w & 3) + g)) * 16;     \
  const int trrow = 4 * g + (r16 >> 2); \
  const int trbase = trrow * 256 + ((((r16 >> 1) & 1) ^ swz(trrow)) * 16) + 8 * (r16 & 1);     \
  const int rbase = r16 * 256 + ((4 * g) ^ swz(r16)) * 16;                                      \
  const int trbaseK = trbase + trrow * 768, rbaseK = rbase + r16 * 768;                         \
  const int vrow0 = g * 256 + (r16 ^ (2 * w)) * 16, vrow1 = vrow0 ^ 16;

  const int cons = h * 4 + j;
  const int jj = 2 * j + (b & 1);
  v4u wqv[4], xa; v2u xb;
  int oW = (64 * jj + 16 * (w & 3) + 4 * g) * 8192 + h * 256 + r16 * 16, oW1 = oW + 8192, oW2 = oW + 16384, oW3 = oW + 24576,
      ox = (w == 2) ? lane * 16 : (w == 3) ? WS_QA + cons * 256 + lane * 4 : (64 * jj + 16 * (w & 3) + 4 * g) * 2;
  unsigned em = EMPTY;
  PIN(oW); PIN(oW1); PIN(oW2); PIN(oW3); PIN(ox); PIN(em);
  SCHED();
  asm volatile("s_waitcnt lgkmcnt(0)" : "+s"(ka0), "+s"(ka1), "+s"(t_start));
  const u16 *x = (const u16*)ka0[0], *wq = (const u16*)ka0[1], *wuk = (const u16*)ka0[3], *ckv = (const u16*)ka0[4], *wuv = (const u16*)ka0[5], *wo = (const u16*)ka0[6],
            *wg = (const u16*)ka0[7], *wu = (const u16*)ka1[0], *wd = (const u16*)ka1[1];
  unsigned char* ws = (unsigned char*)ka0[2];
  u16* out = (u16*)ka1[2];
  if (w >= 4) {
    asm volatile("global_load_dwordx2 %4, %5, %6\n\tglobal_load_dwordx4 %0, %7, %11" WQ_NT "\n\tglobal_load_dwordx4 %1, %8, %11" WQ_NT "\n\tglobal_load_dwordx4 %2, %9, %11" WQ_NT "\n\tglobal_load_dwordx4 %3, %10, %11" WQ_NT
                 : "=&v"(wqv[0]), "=&v"(wqv[1]), "=&v"(wqv[2]), "=&v"(wqv[3]), "=&v"(xb) : "v"(ox), "s"(x), "v"(oW), "v"(oW1), "v"(oW2), "v"(oW3), "s"(wq) : "memory");
  }
  if (w == 2) asm volatile("global_load_dwordx4 %0, %1, %2" : "=&v"(xa) : "v"(ox), "s"(x) : "memory");
  if (w == 3 && !isA) asm volatile("global_store_dword %0, %1, %2 sc1" :: "v"(ox), "v"(em), "s"(ws) : "memory");
  unsigned tag;
  asm volatile("s_load_dword %0, %1, 0x0\n\ts_barrier" : "=&s"(tag) : "s"(ws) : "memory");
  int po = lane * 16;
  u64 t_pub = 0;
  if (w < 2) {
    if (isA) {
      const v4u skv = mksrd(ckv, 16777216);
      const int so = (128 * ai + w) * 1024, vb = po ^ (w * 32);
#pragma unroll 1
      for (int i = 0; i < 8; ++i) {
        const int blk = ((2 * i) & 7) + (i >> 2);
        if (i == 1) { asm volatile("s_barrier\n\ts_barrier" ::: "memory"); t_pub = __builtin_amdgcn_s_memrealtime(); }
        if (i == 6) asm volatile("s_waitcnt vmcnt(16)\n\ts_barrier" ::: "memory");
#pragma unroll
        for (int r = 0; r < 8; ++r) dmaw<KV_NT>(skv, vb ^ ((r & 3) * 64 + (r >> 2) * 16), so + blk * 16384 + r * 2048, w * 1024 + blk * 16384 + r * 2048);
      }
      STAMP(15);
    } else {
      const v4u sk = mksrd(wuk, 4194304);
      const int vk = g * 8192 + (r16 ^ (2 * g)) * 16;
#pragma unroll 1
      for (int i = 0; i < 4; ++i) {
        if (i == KPRE) { asm volatile("s_barrier\n\ts_barrier" ::: "memory"); t_pub = __builtin_amdgcn_s_memrealtime(); }
#pragma unroll
        for (int n = 0; n < 4; ++n) dmaw<WK_NT>(sk, vk ^ ((n & 1) * 128 + (n >> 1) * 16), (128 * j + 64 * w + 16 * i + 4 * n) * 8192 + h * 256, QAI + (16 * w + 4 * i + n) * 1024);
      }
    }
  }
  SCHED();
  const rsrc_t rs = mkrs(ws, WS_END);
  unsigned mk[4];
#pragma unroll
  for (int k = 0; k < 4; ++k) { mk[k] = ((r16 >> 1) == k) ? ((r16 & 1) ? 0xFFFF0000u : 0x0000FFFFu) : 0u; PIN(mk[k]); }
  if (w >= 4) {
    unsigned mk2[4];
    v4u xm[4];
#pragma unroll
    for (int k = 0; k < 4; ++k) { mk2[k] = mk[k] | dppu<0x128>(mk[k]); PIN(mk2[k]); }
    int a_w0 = 116736 + (w - 4) * 1024 + (g >> 1) * 512 + r16 * 32 + (lane & 16);
    PIN(a_w0);
    asm volatile("s_waitcnt vmcnt(4)" : "+v"(xb) :: "memory");
#pragma unroll
    for (int k = 0; k < 2; ++k) {
      const unsigned xp = (r16 >= 8) ? 0x3F803F80u : xb[k];
      xm[2 * k] = xmask(__builtin_amdgcn_perm(xp, xp, 0x01000100u), mk2); xm[2 * k + 1] = xmask(__builtin_amdgcn_perm(xp, xp, 0x03020302u), mk2);
      PIN(xm[2 * k]); PIN(xm[2 * k + 1]);
    }
    SCHED();
    f4 acc = {0.f, 0.f, 0.f, 0.f};
    asm volatile("s_waitcnt vmcnt(0)" : "+v"(wqv[0]), "+v"(wqv[1]), "+v"(wqv[2]), "+v"(wqv[3]) :: "memory");
#pragma unroll
    for (int i = 0; i < 4; ++i) acc = mfma(xm[i], wqv[i], acc);
    STAMP(12);
    *lp<f4>(a_w0) = acc;
    BAR();
    asm volatile("s_barrier" ::: "memory");
  }
  float mb = 0.f, ri = 0.f;
  if (w == 2) {
    __builtin_amdgcn_s_setprio(3);
    asm volatile("s_waitcnt vmcnt(0)" : "+v"(xa) :: "memory");
    ln_stats(xa, mb, ri);
    PIN(mb); PIN(ri);
  }
  asm volatile("s_waitcnt lgkmcnt(0)" : "+s"(tag));
  tag += 1u;
  unsigned tv = tag;
  PIN(tv);
  if (w == 2) {
    int a_qp = ((lane & 31) * 2 + (lane >> 5)) * 16, a_r0 = 116736 + po;
    PIN(a_qp); PIN(a_r0);
    BAR();
    f4 p[4];
#pragma unroll
    for (int i = 0; i < 4; ++i) p[i] = *lp<f4>(a_r0 + i * 1024);
    const f4 s = (p[0] + p[1]) + (p[2] + p[3]);
    const auto r0 = __builtin_amdgcn_permlane32_swap(asu(s[0]), asu(s[2]), false, false), r1 = __builtin_amdgcn_permlane32_swap(asu(s[1]), asu(s[3]), false, false);
    stsc(v4u{asu((asf(r0[0]) - mb * asf(r0[1])) * ri), tv, asu((asf(r1[0]) - mb * asf(r1[1])) * ri), tv}, rs, a_qp, WS_QP + (h * 8 + jj) * 1024);
    asm volatile("s_barrier" ::: "memory");
    STAMP(1);
    __builtin_amdgcn_s_setprio(0);
    if (!isA) {
      v4u xn;
#pragma unroll
      for (int i = 0; i < 4; ++i) { const unsigned d = pk(bflo(xa[i]) - mb, bfhi(xa[i]) - mb); xn[i] = pk(bflo(d) * ri, bfhi(d) * ri); }
      *lp<v4u>(XNI + po) = xn;
    }
  }
  if (w == 3) {
    VMWAIT(0);
    if (!isA && lane == 0) __builtin_amdgcn_raw_buffer_store_b64(v2u{tv, tv}, rs, 0, WS_FL + cons * 8, AUX_SC1);
    asm volatile("s_barrier\n\ts_barrier" ::: "memory");
  }
  if (w >= 2) t_pub = __builtin_amdgcn_s_memrealtime();
#define DLY_G DELAY_G
#define DLY_W DELAY_W
#define DLY_H DELAY_H
#define DLY_U DELAY_U
#define DLY_A DELAY_A
#define DLY_Q DELAY_Q
#define DLY_O DELAY_O
#define DLY_F DELAY_F
  if (!isA) {
    const int xrow0 = 128 * j + 16 * w;
    COMMON_CONSTS
    int a_wk = QAI + w * 4096 + rbase, a_qw = 73728 + lane * 4, a_qr = 73728 + g * 64, a_qa = 74240 + w * 32 + g * 8;
    int a_wk4[4];
#pragma unroll
    for (int s = 0; s < 4; ++s) { a_wk4[s] = a_wk ^ (s * 16); PIN(a_wk4[s]); }
    PIN(a_qw); PIN(a_qr); PIN(a_qa);
    if (w == 4) {
      v4u p[8]; int spins = 0; unsigned bad;
      wait_until(t_pub + DLY_H);
      for (;;) {
        FENCE();
#pragma unroll
        for (int i = 0; i < 8; ++i) p[i] = ldsc(rs, po + (i & 3) * 1024, WS_QP + h * 8192 + (i >> 2) * 4096);
        bad = 0;
#pragma unroll
        for (int i = 0; i < 8; ++i) bad |= (p[i].y ^ tv) | (p[i].w ^ tv);
        PIN(bad);
        if (all_ok(bad == 0) || ++spins >= SPIN_CAP) break;
        __builtin_amdgcn_s_sleep(1);
      }
      const float q0 = ((asf(p[0].x) + asf(p[1].x)) + (asf(p[2].x) + asf(p[3].x))) + ((asf(p[4].x) + asf(p[5].x)) + (asf(p[6].x) + asf(p[7].x)));
      const float q1 = ((asf(p[0].z) + asf(p[1].z)) + (asf(p[2].z) + asf(p[3].z))) + ((asf(p[4].z) + asf(p[5].z)) + (asf(p[6].z) + asf(p[7].z)));
      *lp<unsigned>(a_qw) = pk(q0, q1);
    }
    if (w < 2) { VMWAIT(0); STAMP(13); }
    BAR();
    STAMP(2);
    v4u xm[4];
    {
      v4u qf[4], wkv[4];
#pragma unroll
      for (int s = 0; s < 4; ++s) { wkv[s] = lds128(a_wk4[s]); qf[s] = lds128(a_qr + s * 16); }
      SCHED();
      f4 acc = {0.f, 0.f, 0.f, 0.f};
#pragma unroll
      for (int s = 0; s < 4; ++s) acc = mfma(wkv[s], qf[s], acc);
      const float sc = 0.08838834764831845f * 1.4426950408889634f;
      if (r16 == 0) *lp<v2u>(a_qa) = v2u{pkc(acc[0] * sc, acc[1] * sc), pkc(acc[2] * sc, acc[3] * sc)};
    }
    BAR();
    if (w == 0 && lane < 16) stsc(lds128(74240 + po), rs, po, WS_QA + cons * 256);
    STAMP(3);
    {
      const v2u x4 = *lp<v2u>(XNI + (xrow0 + 4 * g) * 2);
      xm[0] = xmask(__builtin_amdgcn_perm(x4.x, x4.x, 0x01000100u), mk); xm[1] = xmask(__builtin_amdgcn_perm(x4.x, x4.x, 0x03020302u), mk);
      xm[2] = xmask(__builtin_amdgcn_perm(x4.y, x4.y, 0x01000100u), mk); xm[3] = xmask(__builtin_amdgcn_perm(x4.y, x4.y, 0x03020302u), mk);
#pragma unroll
      for (int i = 0; i < 4; ++i) PIN(xm[i]);
    }
    BAR();
    wait_until(t_pub + DLY_Q);
    v4u buv[4], bo[4][4], st;
    int tq[4];
#pragma unroll
    for (int t = 0; t < 4; ++t) tq[t] = (w >> 1) * 256 + (trbaseK ^ (32 * (4 * (w & 1) + t)));
    const v4u sws = mksrd(ws, WS_END);
    int a_st = (4 * g + (r16 & 3)) * 512, a_od = g * 1024 + r16 * 16, a_w1 = (g >> 1) * 12288 + w * 272 + r16 * 16 + (g & 1) * 8, a_r1 = (r16 & 7) * 272 + g * 16,
        a_w2 = 8192 + ((16 * w + r16 - 32 * j) & 127) * 2, a_r2 = 8192 + g * 16, a_o = g * 16384 + r16 * 8, a_sw = 4096 + w * 8, a_sr = 4096 + (g & 1) * 32;
    PIN(a_st); PIN(a_od); PIN(a_w1); PIN(a_r1); PIN(a_w2); PIN(a_r2); PIN(a_o); PIN(a_sw); PIN(a_sr);
    {
      const v4u suv = mksrd(wuv, 4194304), so = mksrd(wo, 4194304);
      const int so2 = (h * 128 + 8 * (w >> 2) + (w & 3)) * 1024;
#define WO4(n) _Pragma("unroll") for (int i = 0; i < 4; ++i) dmaw<(WO_NT >> (n)) & 1>(so, (i & 1) ? vrow1 : vrow0, so2 + ((((n) + j) & 3) * 32 + 16 * (i & 1) + 4 * (i >> 1)) * 1024, (32 * (n) + w + 8 * i) * 1024)
#define RD_UV() _Pragma("unroll") for (int ks = 0; ks < 4; ++ks) { const int a = QAI + (trbase ^ (32 * w)) + ks * 8192; const v2u lo = ldstr(a), hi = ldstr(a + 4096); buv[ks] = v4u{lo.x, lo.y, hi.x, hi.y}; }
#define RD_BO(n) _Pragma("unroll") for (int t = 0; t < 4; ++t) { const v2u lo = ldstr(tq[t] + (n) * 32768), hi = ldstr(tq[t] + (n) * 32768 + 16384); bo[t][n] = v4u{lo.x, lo.y, hi.x, hi.y}; }
      v4u fw[4];
      const GAS char* pf = (const GAS char*)((r16 < 8) ? wg : wu) + (h < 28 ? (xrow0 + 4 * g + 1) * 3584 + h * 128 : 3584) + (r16 & 7) * 16;
      const int vo1 = g * 16 * 8192 + dchunk, so1 = (128 * j + 2 * (w & 3) + (w >> 2)) * 8192 + h * 256;
#define SB() asm volatile("s_barrier" ::: "memory")
#define WB(n) asm volatile("s_waitcnt vmcnt(" #n ") lgkmcnt(0)\n\ts_barrier" ::: "memory")
#define GU4() asm volatile("global_load_dwordx4 %0, %4, off offset:-3584" GU_NT "\n\tglobal_load_dwordx4 %1, %4, off" GU_NT "\n\tglobal_load_dwordx4 %2, %4, off offset:3584" GU_NT "\n\tglobal_load_dwordx4 %3, %5, off" GU_NT \
                   : "=&v"(fw[0]), "=&v"(fw[1]), "=&v"(fw[2]), "=&v"(fw[3]) : "v"(pf), "v"(pf + 7168) : "memory")
#define UV4() _Pragma("unroll 1") for (int n = 0; n < 4; ++n) dmaw<UV_NT>(suv, vo1, so1 + (64 * (n >> 1) + 8 * (n & 1)) * 8192, QAI + (32 * n + 4 * w) * 256)
#define S1(scr) { \
      asm volatile("s_waitcnt vmcnt(4)" : "+v"(fw[0]), "+v"(fw[1]), "+v"(fw[2]), "+v"(fw[3]) :: "memory"); \
      f4 acc = {0.f, 0.f, 0.f, 0.f}; \
      _Pragma("unroll") for (int i = 0; i < 4; ++i) acc = mfma(xm[i], fw[i], acc); \
      if (lane < 32) *lp<f4>((scr) + w * 512 + r16 * 32 + (lane & 16)) = acc; \
      BAR(); \
      if (w == 0) { \
        f2 p[8]; \
        _Pragma("unroll") for (int i = 0; i < 8; ++i) p[i] = *lp<f2>((scr) + lane * 8 + i * 512); \
        const f2 s = ((p[0] + p[1]) + (p[2] + p[3])) + ((p[4] + p[5]) + (p[6] + p[7])); \
        stsc(v4u{asu(s.x), tv, asu(s.y), tv}, rs, po, WS_FP + cons * 1024); \
        STAMP(15); \
      } \
      SB(); }
#define ST1() asm volatile("buffer_load_dwordx4 %0, %1, %2, %3 offen sc1" : "=&v"(st) : "v"(a_st), "s"(sws), "s"(WS_ST + w * 8192 + h * 16) : "memory")
      GU4(); SB(); WO4(0); S1(102400)
      WO4(1); SB(); WO4(2); WB(8); UV4(); RD_BO(0) WB(8); WO4(3); RD_BO(1) WB(8); ST1(); RD_BO(2) WB(5); RD_UV()
      asm volatile("s_waitcnt vmcnt(0)" : "+v"(st) :: "memory");
      STAMP(14);
    }
    v4u od[4];
    {
      for (int spins = 0; !all_ok((st.y == tv) & (st.w == tv)) && spins < SPIN_CAP; ++spins)
        asm volatile("buffer_load_dwordx4 %0, %1, %2, %3 offen sc1\n\ts_waitcnt vmcnt(0)" : "=&v"(st) : "v"(a_st), "s"(sws), "s"(WS_ST + w * 8192 + h * 16) : "memory");
      STAMP(6);
#pragma unroll
      for (int i = 0; i < 4; ++i) od[i] = ldsc(rs, a_od + i * 256, WS_OP + (cons * 128 + 16 * w) * 256);
      SCHED();
      const float m = asf(st.x);
      float mw = fmx(m, dpp<0xB1>(m)); mw = fmx(mw, dpp<0x4E>(mw)); mw = max32(max16(mw));
      const float e = ex2(m - mw);
      const unsigned wb = pk(e, e);
      xm[0] = xmask(dppu<0x00>(wb), mk); xm[1] = xmask(dppu<0x55>(wb), mk); xm[2] = xmask(dppu<0xAA>(wb), mk); xm[3] = xmask(dppu<0xFF>(wb), mk);
      float lw = bflo(wb) * asf(st.z);
      lw += dpp<0xB1>(lw); lw += dpp<0x4E>(lw); lw = add32(add16(lw));
      *lp<f2>(a_sw) = f2{mw, lw};
#pragma unroll
      for (int i = 0; i < 4; ++i) PIN(xm[i]);
      SCHED();
    }
    {
      int spins = 0; f4 acc;
      for (;;) {
        unsigned bad = 0;
        acc = f4{0.f, 0.f, 0.f, 0.f};
#pragma unroll
        for (int i = 0; i < 4; ++i) { acc = mfma(xm[i], od[i], acc); bad = umax4(bad, od[i]); }
        if (all_ok(bad != EMPTY) || ++spins >= SPIN_CAP) break;
        FENCE();
#pragma unroll
        for (int i = 0; i < 4; ++i) od[i] = ldsc(rs, a_od + i * 256, WS_OP + (cons * 128 + 16 * w) * 256);
      }
      *lp<v2u>(a_w1) = v2u{pk(acc[0], acc[1]), pk(acc[2], acc[3])};
    }
    STAMP(7);
    BAR();
    STAMP(4);
    float ri;
    {
      v4u af[4];
      const f4 s0 = *lp<f4>(a_sr), s1 = *lp<f4>(a_sr + 16);
#pragma unroll
      for (int ks = 0; ks < 4; ++ks) af[ks] = lds128(a_r1 + ks * 64);
      RD_BO(3)
      SCHED();
      const float M = max16(fmx(mx3(s0[0], s0[2], s1[0]), s1[2]));
      f4 acc = {0.f, 0.f, 0.f, 0.f};
#pragma unroll
      for (int ks = 0; ks < 4; ++ks) acc = mfma(af[ks], buv[ks], acc);
      const float f0 = ex2(s0[0] - M), f1 = ex2(s0[2] - M), f2_ = ex2(s1[0] - M), f3 = ex2(s1[2] - M);
      const float c = add16((f0 * acc[0] + f1 * acc[1]) + (f2_ * acc[2] + f3 * acc[3]));
      *lp<u16>(a_w2) = (u16)pk(c, c);
      SCHED();
      ri = rcp(add16((f0 * s0[1] + f1 * s0[3]) + (f2_ * s1[1] + f3 * s1[3])));
      PIN(ri);
    }
    STAMP(9);
    BAR();
    STAMP(10);
    {
      v4u cf[4];
#pragma unroll
      for (int ks = 0; ks < 4; ++ks) cf[ks] = lds128(a_r2 + ks * 64);
      SCHED();
      f4 acc[4];
#pragma unroll
      for (int t = 0; t < 4; ++t) acc[t] = f4{0.f, 0.f, 0.f, 0.f};
#pragma unroll
      for (int ks = 0; ks < 4; ++ks)
#pragma unroll
        for (int t = 0; t < 4; ++t) acc[t] = mfma(cf[ks], bo[t][ks], acc[t]);
      const float v01 = (g & 1) ? acc[1][0] : acc[0][0], v23 = (g & 1) ? acc[3][0] : acc[2][0];
      float v = ((g & 2) ? v23 : v01) * ri;
      PIN(v); STAMP(11);
      __builtin_amdgcn_raw_buffer_store_b64(v2u{asu(v), tv}, rs, a_o, WS_SP + (4 * w * 128 + cons) * 128, AUX_SC1);
    }
    STAMP(8);
    STAMP_OUT();
    __builtin_amdgcn_endpgm();
  }
  {
    const v4u sws = mksrd(ws, WS_END);
    const int ht = w & 1, kt = w >> 1;
    v4u k0[16], k1[16];
    v2u v0[4][4];
    const rsrc_t rx = mkrs(x, 1024);
    COMMON_CONSTS
    const int mi = ai & 3, fi = (ai < 112) ? (ai >> 2) : 0;
    int ka[4], ta[4], qa_[4], vo0 = vrow0, vo1 = vrow1;
#pragma unroll
    for (int s = 0; s < 4; ++s) {
      ka[s] = kt * 32768 + (rbaseK ^ (s * 16)); ta[s] = (w >> 1) * 256 + (trbaseK ^ (32 * (4 * (w & 1) + s))); qa_[s] = ka[s] + (QAI + ht * 16384 - kt * 32768);
      PIN(ka[s]); PIN(ta[s]); PIN(qa_[s]);
    }
    int a_d = (2 * g + (r16 >> 3)) * 1024 + (r16 & 7) * 16, a_mw = (16 * ht + r16) * 16 + kt * 4,
        a_s = (lane < 16) ? (16 * ht + r16) * 16 : WS_END, a_o = (16 * g + (w >> 1)) * 32768 + (w & 1) * 128 + r16 * 8, a_pq = QAI + po,
        a_c = (16 * w + g) * 32768 + r16 * 16;
    int a_mr = a_mw & ~15, a_lw = 2048 + g * 512 + a_mw, a_pw = a_pq + w * 1024;
    PIN(a_mr); PIN(a_lw); PIN(a_pw);
    PIN(a_mw); PIN(a_s); PIN(a_o); PIN(a_pq); PIN(a_c); PIN(vo0); PIN(vo1);
#define RD_U0() _Pragma("unroll") for (int i = 0; i < 4; ++i) _Pragma("unroll") for (int s = 0; s < 4; ++s) { k0[4 * i + s] = lds128(ka[s] + i * 256); v0[i][s] = ldstr(ta[s] + i * 32768); }
#define RD_K1() _Pragma("unroll") for (int s = 0; s < 16; ++s) k1[s] = lds128(ka[s & 3] + (s >> 2) * 256 + 16384)
#define FLAGS_OK() all_ok(((fl.x ^ tv) | (fl.y ^ tv) | (fl.z ^ tv) | (fl.w ^ tv)) == 0)
#define QA4() _Pragma("unroll") for (int q = 0; q < 4; ++q) dma16sc(sws, (q & 1) ? vo1 : vo0, WS_QA + (w + 8 * q) * 1024, QAI + (w + 8 * q) * 1024)
    if (w >= 2) {
      v4u fl;
      if (w == 3) {
        __builtin_amdgcn_s_setprio(3);
      }
      asm volatile("s_barrier" ::: "memory");
      if (w == 3) asm volatile("buffer_load_dwordx4 %0, %1, %2, %3 offen sc1" : "=&v"(fl) : "v"(po), "s"(sws), "s"(WS_FL) : "memory");
      RD_U0()
      if (w == 3) {
        asm volatile("s_waitcnt vmcnt(0)" : "+v"(fl) :: "memory");
        int spins = 0;
        while (!FLAGS_OK() && ++spins < SPIN_CAP)
          asm volatile("buffer_load_dwordx4 %0, %1, %2, %3 offen sc1\n\ts_waitcnt vmcnt(0)" : "=&v"(fl) : "v"(po), "s"(sws), "s"(WS_FL) : "memory");
        __builtin_amdgcn_s_setprio(0);
      }
    }
    asm volatile("s_waitcnt vmcnt(16)\n\ts_barrier" ::: "memory");
    STAMP(2);
    const bool early = (long long)(__builtin_amdgcn_s_memrealtime() - (t_pub + DLY_G)) >= 0;
    if (early) { QA4(); }
    if (w < 2) { RD_U0() }
    if (kt < 2) { RD_K1(); }
    v4u vf[4][4];
#pragma unroll
    for (int i = 0; i < 8; ++i) { const v2u hi = ldstr(ta[i & 3] + (i >> 2) * 32768 + 16384); vf[i >> 2][i & 3] = v4u{v0[i >> 2][i & 3].x, v0[i >> 2][i & 3].y, hi.x, hi.y}; }
    if (!early) { wait_until(t_pub + DLY_G); QA4(); }
    VMWAIT(4);
    BAR();
    STAMP(3);
    if (kt >= 2) { RD_K1(); }
#define RD_V1(i) { const v2u hi = ldstr(ta[(i) & 3] + ((i) >> 2) * 32768 + 16384); vf[(i) >> 2][(i) & 3] = v4u{v0[(i) >> 2][(i) & 3].x, v0[(i) >> 2][(i) & 3].y, hi.x, hi.y}; }
    f4 S0 = {0.f, 0.f, 0.f, 0.f}, S1 = {0.f, 0.f, 0.f, 0.f};
    PIN(S0); PIN(S1);
    for (int spins = 0;; ++spins) {
      VMWAIT(0);
      unsigned bad = 0;
#pragma unroll
      for (int q = 0; q < 4; ++q) bad = umax4(bad, lds128(a_pw + q * 8192));
      if (all_ok(bad != EMPTY) || spins >= SPIN_CAP) break;
      QA4();
    }
    BAR();
    STAMP(4);
    {
      v4u qf[16];
#pragma unroll
      for (int s = 0; s < QD; ++s) qf[s] = lds128(qa_[s & 3] + (s >> 2) * 256);
      SCHED();
#pragma unroll
      for (int s = 0; s < 16; ++s) {
        mfma_acc(S0, k0[s], qf[s]); mfma_acc(S1, k1[s], qf[s]);
        if (s + QD < 16) qf[s + QD] = lds128(qa_[(s + QD) & 3] + ((s + QD) >> 2) * 256);
        if (s >= 8) RD_V1(s)
        if (s < 8 && !(s & 1)) stsc(v4u{EMPTY, EMPTY, EMPTY, EMPTY}, rs, a_c, WS_OP + (s >> 1) * 131072 + ai * 256);
        SCHED();
      }
      float m0, mt;
      asm volatile("s_nop 7\n\tv_max3_f32 %0, %2, %3, %4\n\tv_max3_f32 %0, %0, %5, %6\n\tv_max3_f32 %1, %7, %8, %9\n\tv_max_f32 %0, %0, %1\n\t"
                   "v_mov_b32 %1, %0\n\ts_nop 1\n\tv_permlane16_swap_b32 %0, %1\n\tv_max_f32 %0, %0, %1\n\t"
                   "v_mov_b32 %1, %0\n\ts_nop 1\n\tv_permlane32_swap_b32 %0, %1\n\tv_max_f32 %0, %0, %1"
                   : "=&v"(m0), "=&v"(mt) : "v"(S0[0]), "v"(S0[1]), "v"(S0[2]), "v"(S0[3]), "v"(S1[0]), "v"(S1[1]), "v"(S1[2]), "v"(S1[3]));
      *lp<float>(a_mw) = m0;
    }
    BAR();
    STAMP(5);
    float M;
    int a_f = (r16 & 3) * 1024 + (64 * ((r16 >> 2) & 1) + 8 * (r16 >> 3) + 2 * g) * 8;
    {
      const f4 m4 = *lp<f4>(a_mr);
      PIN(a_f); PIN(a_d);
      asm("v_max3_f32 %0, %1, %2, %3\n\tv_max_f32 %0, %0, %4" : "=&v"(M) : "v"(m4[0]), "v"(m4[1]), "v"(m4[2]), "v"(m4[3]));
      float p[8];
#pragma unroll
      for (int i = 0; i < 4; ++i) { p[i] = ex2(S0[i] - M); p[4 + i] = ex2(S1[i] - M); }
      *lp<v4u>(a_pw) = v4u{pk(p[0], p[1]), pk(p[2], p[3]), pk(p[4], p[5]), pk(p[6], p[7])};
      *lp<float>(a_lw) = ((p[0] + p[1]) + (p[2] + p[3])) + ((p[4] + p[5]) + (p[6] + p[7]));
    }
    VMWAIT(0);
    v4u dw[2], fp;
    asm volatile("buffer_load_dwordx4 %0, %1, %2, %3 offen sc1" : "=&v"(fp) : "v"(a_f), "s"(sws), "s"(WS_FP + fi * 4096 + mi * 128) : "memory");
    BAR();
    STAMP(6);
    {
      v4u pf_[8];
      pf_[0] = lds128(a_pq);
      const f4 l4 = *lp<f4>(a_lw & ~15);
#pragma unroll
      for (int i = 1; i < 8; ++i) pf_[i] = lds128(a_pq + ((i & 3) * 2 + (i >> 2)) * 1024);
      SCHED();
#pragma unroll
      for (int hh = 0; hh < 2; ++hh) {
        f4 O[4];
#pragma unroll
        for (int t = 0; t < 4; ++t) O[t] = f4{0.f, 0.f, 0.f, 0.f};
#pragma unroll
        for (int ks = 0; ks < 4; ++ks) {
#pragma unroll
          for (int t = 0; t < 4; ++t) O[t] = mfma(pf_[4 * hh + ks], vf[ks][t], O[t]);
          if (hh == 0 && ks == 0) { SCHED(); if (kt == 0) stsc(v4u{asu(M), tv, asu(add32(add16((l4[0] + l4[1]) + (l4[2] + l4[3])))), tv}, rs, a_s, WS_ST + ai * 512); SCHED(); }
        }
        if (hh == 1) { PIN(O[3]); STAMP(13); }
#pragma unroll
        for (int i = 0; i < 4; ++i)
          __builtin_amdgcn_raw_buffer_store_b64(v2u{pkc(O[0][i], O[1][i]), pkc(O[2][i], O[3][i])}, rs, a_o, WS_OP + (16 * hh + i) * 131072 + ai * 256, AUX_SC1);
      }
    }
    STAMP(7);
    const u64 t_pv = __builtin_amdgcn_s_memrealtime();
    asm volatile("s_waitcnt vmcnt(8)" : "+v"(fp) :: "memory");
    if (ai < 112) {
      wait_until(t_pv + DLY_U);
      const rsrc_t rd = mkrs(wd, 1835008);
#pragma unroll
      for (int i = 0; i < 2; ++i) dw[i] = ldnt(rd, a_d, (64 * fi + 16 * mi + 8 * i) * 1024 + w * 128);
      SCHED();
      const int gu = (r16 >> 2) & 1, hi = r16 >> 3;
      v4u p = fp;
      int spins = 0;
      for (; !all_ok((p.y == tv) & (p.w == tv)) && spins < SPIN_CAP; ++spins)
        asm volatile("buffer_load_dwordx4 %0, %1, %2, %3 offen sc1\n\ts_waitcnt vmcnt(0)" : "=&v"(p) : "v"(a_f), "s"(sws), "s"(WS_FP + fi * 4096 + mi * 128) : "memory");
      STAMP(14);
      float v0_ = asf(p.x), v1_ = asf(p.z);
      v0_ += dpp<0xB1>(v0_); v1_ += dpp<0xB1>(v1_);
      v0_ += dpp<0x4E>(v0_); v1_ += dpp<0x4E>(v1_);
      const float o0 = dpp<0x141>(v0_), o1 = dpp<0x141>(v1_);
      float hv[2];
#pragma unroll
      for (int e = 0; e < 2; ++e) {
        const float gt = rbf(gu ? (e ? o1 : o0) : (e ? v1_ : v0_)), up = rbf(gu ? (e ? v1_ : v0_) : (e ? o1 : o0));
        const float z2 = 2.0f * 0.7978845608028654f * 1.4426950408889634f * (gt + 0.044715f * gt * gt * gt);
        hv[e] = rbf(gt * rcp(1.0f + ex2(-z2))) * up;
      }
      const float t0 = dpp<0x128>(hv[0]), t1 = dpp<0x128>(hv[1]);
      unsigned mk2[4];
#pragma unroll
      for (int k = 0; k < 4; ++k) mk2[k] = mk[k] | dppu<0x128>(mk[k]);
      const float ha = hi ? t1 : hv[0], hb = hi ? hv[1] : t0;
      f4 acc = {0.f, 0.f, 0.f, 0.f};
      acc = mfma(xmask(pk(ha, ha), mk2), dw[0], acc);
      acc = mfma(xmask(pk(hb, hb), mk2), dw[1], acc);
      float y[4];
#pragma unroll
      for (int i = 0; i < 4; ++i) {
        const unsigned u = asu(dpp<0x128>(acc[i]));
        auto r = __builtin_amdgcn_permlane32_swap(u, u, false, false);
        y[i] = acc[i] + asf(r[1]);
      }
      const auto ya = __builtin_amdgcn_permlane32_swap(asu(y[0]), asu(y[2]), false, false), yb = __builtin_amdgcn_permlane32_swap(asu(y[1]), asu(y[3]), false, false);
      if (r16 < 8) stsc(v4u{ya[0], tv, yb[0], tv}, rs, (r16 >> 1) * 16384 + ((r16 & 1) * 8 + 4 * (g & 1) + 2 * (g >> 1)) * 8, WS_YP + (4 * w * 128 + ai) * 128);
    }
    STAMP(8);
    if (w >= 6) {
      const int off = (lane >> 1) * 128 + (lane & 1) * 16, so = ((w == 7) ? WS_SP : WS_YP) + h * 16384 + j * 32;
      const unsigned xo = __builtin_amdgcn_raw_buffer_load_b32(rx, (lane & 1) * 4, h * 32 + j * 8, 0);
      const bool skip3 = (w == 6) && (lane >= 32);
      v4u p[4]; int spins = 0; unsigned bad;
      {
        const int go = so + ((ai * 5 + 3) % 112) * 128;
        wait_until(t_pv + 80);
        for (int sp = 0; sp < SPIN_CAP; ++sp) {
          FENCE();
          const v2u t = __builtin_amdgcn_raw_buffer_load_b64(rs, 0, go, AUX_SC1);
          if (__builtin_amdgcn_readfirstlane(t.y) == tag) break;
          __builtin_amdgcn_s_sleep(2);
        }
        wait_until(__builtin_amdgcn_s_memrealtime() + ((w == 7) ? DLY_F : DLY_W));
      }
      do {
        FENCE();
#pragma unroll
        for (int n = 0; n < 4; ++n) p[n] = ldsc(rs, off, so + n * 4096);
        if (skip3) p[3] = v4u{0u, tv, 0u, tv};
        bad = 0;
#pragma unroll
        for (int n = 0; n < 4; ++n) bad |= (p[n].y ^ tv) | (p[n].w ^ tv);
        PIN(bad);
      } while (!all_ok(bad == 0) && ++spins < SPIN_CAP);
      float a0 = (asf(p[0].x) + asf(p[1].x)) + (asf(p[2].x) + asf(p[3].x));
      float a1 = (asf(p[0].z) + asf(p[1].z)) + (asf(p[2].z) + asf(p[3].z));
      a0 += dpp<0x128>(a0); a1 += dpp<0x128>(a1);
      a0 += dpp<0x124>(a0); a1 += dpp<0x124>(a1);
      a0 += dpp<0x122>(a0); a1 += dpp<0x122>(a1);
      a0 = rbf(add32(add16(a0))); a1 = rbf(add32(add16(a1)));
      if (w == 6) {
        a0 = rbf(a0 * rcp(1.0f + ex2(-1.4426950408889634f * a0)));
        a1 = rbf(a1 * rcp(1.0f + ex2(-1.4426950408889634f * a1)));
        if (lane < 2) *lp<f2>(1024 + lane * 8) = f2{a0, a1};
        STAMP(9);
        BAR();
      } else {
        BAR();
        const f2 gt = *lp<f2>(1024 + (lane & 1) * 8);
        const unsigned o = pk(bflo(xo) + rbf(a0 * gt.x), bfhi(xo) + rbf(a1 * gt.y));
        if (lane < 2) *(GAS unsigned*)(out + 16 * h + 4 * j + 2 * lane) = o;
        if (ai == 0 && lane == 0) *(GAS unsigned*)ws = tag;
        STAMP(10);
      }
    }
  }
  STAMP_OUT();
}

extern "C" void kernel_launch(void* const* d_in, const int* in_sizes, int n_in, void* d_out, int out_size, void* d_ws, size_t ws_size, hipStream_t stream) {
  (void)hipFuncSetAttribute((const void*)mla_decode, hipFuncAttributeMaxDynamicSharedMemorySize, LDS_BYTES);
  mla_decode<<<256, 512, LDS_BYTES, stream>>>((const u16*)d_in[0], (const u16*)d_in[2], (unsigned char*)d_ws + WS_BASE(ws_size), (const u16*)d_in[3], (const u16*)d_in[1], (const u16*)d_in[4], (const u16*)d_in[5],
                                              (const u16*)d_in[7], (const u16*)d_in[8], (const u16*)d_in[9], (u16*)d_out, d_in[0]);
}
```

```cpp
#include <hip/hip_runtime.h>
#include <stdint.h>

typedef unsigned short u16;
typedef unsigned long long u64;
typedef unsigned v4u __attribute__((ext_vector_type(4)));
typedef unsigned v2u __attribute__((ext_vector_type(2)));
typedef unsigned v8u __attribute__((ext_vector_type(8)));
typedef unsigned v16u __attribute__((ext_vector_type(16)));
typedef float f4 __attribute__((ext_vector_type(4)));
typedef float f2 __attribute__((ext_vector_type(2)));
typedef __bf16 bf8 __attribute__((ext_vector_type(8)));
typedef __bf16 bf2 __attribute__((ext_vector_type(2)));
typedef short s4 __attribute__((ext_vector_type(4)));
#define GAS __attribute__((address_space(1)))
#define LAS __attribute__((address_space(3)))
#define CAS __attribute__((address_space(4)))

#define DELAY_A 0
#define DELAY_Q 285
#define DELAY_O 55

#define DELAY_F 0
#define WS_BASE(n) 0
#define DELAY_G 220
#define DELAY_U 30
#define DELAY_H 45
#define ROLE_A_FIRST 1
#define ROLE_PARITY 1
#define GUW 0xEC
#define APRE 8
#define KV_NT 1
#define WK_NT 1
#define UV_NT 1
#define WQ_NT " nt"
#define GU_NT " nt"
#define WO_NT 0
#define KPRE 3
#define QD 6
#define DELAY_W 0
constexpr int WS_FL = 1024;
constexpr int WS_FL2 = 2048;
constexpr int WS_QP = 4096;
constexpr int WS_QA = WS_QP + 262144;
constexpr int WS_FP = WS_QA + 32768;
constexpr int WS_ST = WS_FP + 131072;
constexpr int WS_SP = WS_ST + 262144;
constexpr int WS_YP = WS_SP + 524288;
constexpr int WS_OP = WS_YP + 524288;
constexpr int WS_END = WS_OP + 4194304;
constexpr int WS_STAMP = 6 << 20;
constexpr int WS_TUNE = 64;
constexpr int LDS_BYTES = 163840;
constexpr int CODE_BYTES = 14336;
constexpr int SPIN_CAP = 1 << 16;
constexpr int AUX_SC1 = 16, AUX_NT = 2;
constexpr int QAI = 131072;
constexpr int XNI = 65536;
constexpr unsigned EMPTY = 0xFFFFFFFFu;

#define BAR() asm volatile("s_waitcnt lgkmcnt(0)\n\ts_barrier" ::: "memory")
#define FENCE() asm volatile("" ::: "memory")
#define SCHED() __builtin_amdgcn_sched_barrier(0)
#define VMWAIT(n) asm volatile("s_waitcnt vmcnt(" #n ")" ::: "memory")
#define PIN(x) asm volatile("" : "+v"(x))
#define PINS(x) asm volatile("" : "+s"(x))

#define STAMP(i) do {} while (0)
#define STAMP_OUT()

typedef __amdgpu_buffer_rsrc_t rsrc_t;
__device__ __forceinline__ float bflo(unsigned u) { return __uint_as_float(u << 16); }
__device__ __forceinline__ float bfhi(unsigned u) { return __uint_as_float(u & 0xFFFF0000u); }
__device__ __forceinline__ unsigned pk(float a, float b) { f2 f = {a, b}; return __builtin_bit_cast(unsigned, __builtin_convertvector(f, bf2)); }
__device__ __forceinline__ unsigned pkc(float a, float b) { return min(pk(a, b), EMPTY - 1u); }
__device__ __forceinline__ float rbf(float a) { return bflo(pk(a, a)); }
__device__ __forceinline__ float asf(unsigned u) { return __uint_as_float(u); }
__device__ __forceinline__ unsigned asu(float f) { return __float_as_uint(f); }
__device__ __forceinline__ float ex2(float x) { return __builtin_amdgcn_exp2f(x); }
__device__ __forceinline__ float rcp(float x) { return __builtin_amdgcn_rcpf(x); }
__device__ __forceinline__ f4 mfma(v4u a, v4u b, f4 c) {
  return __builtin_amdgcn_mfma_f32_16x16x32_bf16(__builtin_bit_cast(bf8, a), __builtin_bit_cast(bf8, b), c, 0, 0, 0);
}
__device__ __forceinline__ void mfma_acc(f4& c, v4u a, v4u b) { asm volatile("v_mfma_f32_16x16x32_bf16 %0, %1, %2, %0" : "+v"(c) : "v"(a), "v"(b)); }
template <int CTRL> __device__ __forceinline__ float dpp(float v) {
  return __builtin_bit_cast(float, __builtin_amdgcn_update_dpp(0, __builtin_bit_cast(int, v), CTRL, 0xf, 0xf, true));
}
template <int CTRL> __device__ __forceinline__ unsigned dppu(unsigned v) { return (unsigned)__builtin_amdgcn_update_dpp(0, (int)v, CTRL, 0xf, 0xf, true); }
__device__ __forceinline__ float add16(float v) { unsigned u = asu(v); auto r = __builtin_amdgcn_permlane16_swap(u, u, false, false); return asf(r[0]) + asf(r[1]); }
__device__ __forceinline__ float add32(float v) { unsigned u = asu(v); auto r = __builtin_amdgcn_permlane32_swap(u, u, false, false); return asf(r[0]) + asf(r[1]); }
__device__ __forceinline__ float fmx(float a, float b) { return __builtin_fmaxf(a, b); }
__device__ __forceinline__ float mx3(float a, float b, float c) { return fmx(fmx(a, b), c); }
__device__ __forceinline__ float max16(float v) { unsigned u = asu(v); auto r = __builtin_amdgcn_permlane16_swap(u, u, false, false); return fmx(asf(r[0]), asf(r[1])); }
__device__ __forceinline__ float max32(float v) { unsigned u = asu(v); auto r = __builtin_amdgcn_permlane32_swap(u, u, false, false); return fmx(asf(r[0]), asf(r[1])); }
__device__ __forceinline__ float wave_sum(float v) {
  v += dpp<0x128>(v); v += dpp<0x124>(v); v += dpp<0x122>(v); v += dpp<0x121>(v);
  return add32(add16(v));
}
__device__ __forceinline__ unsigned umax4(unsigned acc, v4u v) { return max(max(max(acc, v.x), v.y), max(v.z, v.w)); }
__device__ __forceinline__ rsrc_t mkrs(const void* p, int bytes) { return __builtin_amdgcn_make_buffer_rsrc((void*)p, 0, bytes, 0x00020000); }
__device__ __forceinline__ v4u mksrd(const void* p, unsigned bytes) { const u64 a = (u64)p; return v4u{(unsigned)a, (unsigned)(a >> 32) & 0xFFFFu, bytes, 0x00020000u}; }
__device__ __forceinline__ v4u ldnt(rsrc_t r, int voff, int soff) { return __builtin_amdgcn_raw_buffer_load_b128(r, voff, soff, AUX_NT); }
__device__ __forceinline__ v4u ldsc(rsrc_t r, int voff, int soff) { return __builtin_amdgcn_raw_buffer_load_b128(r, voff, soff, AUX_SC1); }
__device__ __forceinline__ void stsc(v4u v, rsrc_t r, int voff, int soff) { __builtin_amdgcn_raw_buffer_store_b128(v, r, voff, soff, AUX_SC1); }
__device__ __forceinline__ void dma16(v4u srd, int voff, int soff, int lds_dst) {
  asm volatile("s_mov_b32 m0, %3\n\ts_nop 0\n\tbuffer_load_dwordx4 %0, %1, %2 offen nt lds" :: "v"(voff), "s"(srd), "s"(soff), "s"(lds_dst) : "memory");
}
__device__ __forceinline__ void dma16p(v4u srd, int voff, int soff, int lds_dst) {
  asm volatile("s_mov_b32 m0, %3\n\ts_nop 0\n\tbuffer_load_dwordx4 %0, %1, %2 offen lds" :: "v"(voff), "s"(srd), "s"(soff), "s"(lds_dst) : "memory");
}
template <int NT> __device__ __forceinline__ void dmaw(v4u srd, int voff, int soff, int lds_dst) { if (NT) dma16(srd, voff, soff, lds_dst); else dma16p(srd, voff, soff, lds_dst); }
__device__ __forceinline__ void dma16sc(v4u srd, int voff, int soff, int lds_dst) {
  asm volatile("s_mov_b32 m0, %3\n\ts_nop 0\n\tbuffer_load_dwordx4 %0, %1, %2 offen sc1 lds" :: "v"(voff), "s"(srd), "s"(soff), "s"(lds_dst) : "memory");
}
template <typename T> __device__ __forceinline__ LAS T* lp(int off) { return (LAS T*)(uintptr_t)(unsigned)off; }
__device__ __forceinline__ v4u lds128(int off) { return *lp<v4u>(off); }
__device__ __forceinline__ v2u ldstr(int off) { return __builtin_bit_cast(v2u, __builtin_amdgcn_ds_read_tr16_b64_v4i16(lp<s4>(off))); }
__device__ __forceinline__ int swz(int r) { return ((r & 7) << 1) | ((r >> 3) & 1); }
__device__ __forceinline__ v4u xmask(unsigned xx, const unsigned (&mk)[4]) { return v4u{xx & mk[0], xx & mk[1], xx & mk[2], xx & mk[3]}; }
__device__ __forceinline__ bool all_ok(bool ok) { return __builtin_amdgcn_ballot_w64(!ok) == 0ull; }
__device__ __forceinline__ void wait_until(u64 t) { while ((long long)(__builtin_amdgcn_s_memrealtime() - t) < 0) __builtin_amdgcn_s_sleep(1); }

__device__ __forceinline__ void ln_stats(v4u xa, float& mb, float& ri) {
  float v[8] = {bflo(xa.x), bfhi(xa.x), bflo(xa.y), bfhi(xa.y), bflo(xa.z), bfhi(xa.z), bflo(xa.w), bfhi(xa.w)};
  const float mean = wave_sum(((v[0] + v[1]) + (v[2] + v[3])) + ((v[4] + v[5]) + (v[6] + v[7]))) * (1.0f / 512.0f);
  float q0 = 0.f, q1 = 0.f;
#pragma unroll
  for (int i = 0; i < 4; ++i) { const float d = v[i] - mean, e = v[i + 4] - mean; q0 += d * d; q1 += e * e; }
  const float var = wave_sum(q0 + q1) * (1.0f / 512.0f);
  mb = rbf(mean);
  ri = rcp(rbf(__builtin_amdgcn_sqrtf(rbf(rbf(var) + 1e-5f))));
}

__device__ __forceinline__ v4u layernorm8(v4u xa) {
  float mb, ri;
  ln_stats(xa, mb, ri);
  v4u o;
#pragma unroll
  for (int i = 0; i < 4; ++i) { const unsigned d = pk(bflo(xa[i]) - mb, bfhi(xa[i]) - mb); o[i] = pk(bflo(d) * ri, bfhi(d) * ri); }
  return o;
}

typedef u64 v8q __attribute__((ext_vector_type(8)));
typedef u64 v4q __attribute__((ext_vector_type(4)));
extern "C" __global__ void __launch_bounds__(512, 2) mla_decode(const void*, const void*, void*, const void*, const void*, const void*, const void*, const void*,
                                                      const void*, const void*, void*, const void*) {
  v8q ka0; v4q ka1; u64 t_start;
  asm volatile("s_load_dwordx16 %0, %3, 0x0\n\ts_load_dwordx8 %1, %3, 0x40\n\ts_memrealtime %2" : "=&s"(ka0), "=&s"(ka1), "=&s"(t_start) : "s"(__builtin_amdgcn_kernarg_segment_ptr()));
  const int b = blockIdx.x, tid = threadIdx.x, lane = tid & 63, w = __builtin_amdgcn_readfirstlane(tid >> 6);
  const int xcd = b & 7, rr = (b >> 3) & 15;
  const bool isA = (b & 1) == 0;
  const int r16 = lane & 15, g = lane >> 4;
  const int h = (xcd >> 1) * 8 + ((b >> 3) & 7), j = b >> 6;
  const int ai = b >> 1;
  {
    u64 pc; asm volatile("s_getpc_b64 %0" : "=s"(pc));
    asm volatile("s_mov_b32 m0, %2\n\ts_nop 0\n\tglobal_load_lds_dword %0, %1" :: "v"(min(((b >> 3) * 8 + w) * 128, CODE_BYTES - 128) + r16 * 8), "s"(pc & ~255ull), "s"(114688) : "memory");
  }
#define COMMON_CONSTS \
  const int dchunk = (r16 ^ swz(4 * (w & 3) + g)) * 16;     \
  const int trrow = 4 * g + (r16 >> 2); \
  const int trbase = trrow * 256 + ((((r16 >> 1) & 1) ^ swz(trrow)) * 16) + 8 * (r16 & 1);     \
  const int rbase = r16 * 256 + ((4 * g) ^ swz(r16)) * 16;                                      \
  const int trbaseK = trbase + trrow * 768, rbaseK = rbase + r16 * 768;                         \
  const int vrow0 = g * 256 + (r16 ^ (2 * w)) * 16, vrow1 = vrow0 ^ 16;

  const int cons = h * 4 + j;
  const int jj = 2 * j + (b & 1);
  v4u wqv[4], xa; v2u xb;
  int oW = (64 * jj + 16 * (w & 3) + 4 * g) * 8192 + h * 256 + r16 * 16, oW1 = oW + 8192, oW2 = oW + 16384, oW3 = oW + 24576,
      ox = (w == 2) ? lane * 16 : (w == 3) ? WS_QA + cons * 256 + lane * 4 : (64 * jj + 16 * (w & 3) + 4 * g) * 2;
  unsigned em = EMPTY;
  PIN(oW); PIN(oW1); PIN(oW2); PIN(oW3); PIN(ox); PIN(em);
  SCHED();
  asm volatile("s_waitcnt lgkmcnt(0)" : "+s"(ka0), "+s"(ka1), "+s"(t_start));
  const u16 *x = (const u16*)ka0[0], *wq = (const u16*)ka0[1], *wuk = (const u16*)ka0[3], *ckv = (const u16*)ka0[4], *wuv = (const u16*)ka0[5], *wo = (const u16*)ka0[6],
            *wg = (const u16*)ka0[7], *wu = (const u16*)ka1[0], *wd = (const u16*)ka1[1];
  unsigned char* ws = (unsigned char*)ka0[2];
  u16* out = (u16*)ka1[2];
  if (w >= 4) {
    asm volatile("global_load_dwordx2 %4, %5, %6\n\tglobal_load_dwordx4 %0, %7, %11" WQ_NT "\n\tglobal_load_dwordx4 %1, %8, %11" WQ_NT "\n\tglobal_load_dwordx4 %2, %9, %11" WQ_NT "\n\tglobal_load_dwordx4 %3, %10, %11" WQ_NT
                 : "=&v"(wqv[0]), "=&v"(wqv[1]), "=&v"(wqv[2]), "=&v"(wqv[3]), "=&v"(xb) : "v"(ox), "s"(x), "v"(oW), "v"(oW1), "v"(oW2), "v"(oW3), "s"(wq) : "memory");
  }
  if (w == 2) asm volatile("global_load_dwordx4 %0, %1, %2" : "=&v"(xa) : "v"(ox), "s"(x) : "memory");
  if (w == 3 && !isA) asm volatile("global_store_dword %0, %1, %2 sc1" :: "v"(ox), "v"(em), "s"(ws) : "memory");
  unsigned tag;
  asm volatile("s_load_dword %0, %1, 0x0\n\ts_barrier" : "=&s"(tag) : "s"(ws) : "memory");
  int po = lane * 16;
  u64 t_pub = 0;
  if (w < 2) {
    if (isA) {
      const v4u skv = mksrd(ckv, 16777216);
      const int so = (128 * ai + w) * 1024, vb = po ^ (w * 32);
#pragma unroll 1
      for (int i = 0; i < 8; ++i) {
        const int blk = ((2 * i) & 7) + (i >> 2);
        if (APRE == 8 && i == 1) { asm volatile("s_barrier\n\ts_barrier" ::: "memory"); t_pub = __builtin_amdgcn_s_memrealtime(); }
        if (APRE == 0 && i == 0) { asm volatile("s_barrier\n\ts_barrier" ::: "memory"); t_pub = __builtin_amdgcn_s_memrealtime(); }
        if (i == 6) asm volatile("s_waitcnt vmcnt(16)\n\ts_barrier" ::: "memory");
#pragma unroll
        for (int r = 0; r < 8; ++r) {
          if ((APRE & 7) && r == (APRE & 7) && i == (APRE >> 3)) { asm volatile("s_barrier\n\ts_barrier" ::: "memory"); t_pub = __builtin_amdgcn_s_memrealtime(); }
          dmaw<KV_NT>(skv, vb ^ ((r & 3) * 64 + (r >> 2) * 16), so + blk * 16384 + r * 2048, w * 1024 + blk * 16384 + r * 2048);
        }
      }
      STAMP(15);
    } else {
      const v4u sk = mksrd(wuk, 4194304);
      const int vk = g * 8192 + (r16 ^ (2 * g)) * 16;
#pragma unroll 1
      for (int i = 0; i < 4; ++i) {
        if (i == KPRE) { asm volatile("s_barrier\n\ts_barrier" ::: "memory"); t_pub = __builtin_amdgcn_s_memrealtime(); }
#pragma unroll
        for (int n = 0; n < 4; ++n) dmaw<WK_NT>(sk, vk ^ ((n & 1) * 128 + (n >> 1) * 16), (128 * j + 64 * w + 16 * i + 4 * n) * 8192 + h * 256, QAI + (16 * w + 4 * i + n) * 1024);
      }
      if (KPRE == 4) { asm volatile("s_barrier\n\ts_barrier" ::: "memory"); t_pub = __builtin_amdgcn_s_memrealtime(); }
    }
  }
  SCHED();
  const rsrc_t rs = mkrs(ws, WS_END);
  unsigned mk[4];
#pragma unroll
  for (int k = 0; k < 4; ++k) { mk[k] = ((r16 >> 1) == k) ? ((r16 & 1) ? 0xFFFF0000u : 0x0000FFFFu) : 0u; PIN(mk[k]); }
  if (w >= 4) {
    unsigned mk2[4];
    v4u xm[4];
#pragma unroll
    for (int k = 0; k < 4; ++k) { mk2[k] = mk[k] | dppu<0x128>(mk[k]); PIN(mk2[k]); }
    int a_w0 = 116736 + (w - 4) * 1024 + (g >> 1) * 512 + r16 * 32 + (lane & 16);
    PIN(a_w0);
    asm volatile("s_waitcnt vmcnt(4)" : "+v"(xb) :: "memory");
#pragma unroll
    for (int k = 0; k < 2; ++k) {
      const unsigned xp = (r16 >= 8) ? 0x3F803F80u : xb[k];
      xm[2 * k] = xmask(__builtin_amdgcn_perm(xp, xp, 0x01000100u), mk2); xm[2 * k + 1] = xmask(__builtin_amdgcn_perm(xp, xp, 0x03020302u), mk2);
      PIN(xm[2 * k]); PIN(xm[2 * k + 1]);
    }
    SCHED();
    f4 acc = {0.f, 0.f, 0.f, 0.f};
    asm volatile("s_waitcnt vmcnt(0)" : "+v"(wqv[0]), "+v"(wqv[1]), "+v"(wqv[2]), "+v"(wqv[3]) :: "memory");
#pragma unroll
    for (int i = 0; i < 4; ++i) acc = mfma(xm[i], wqv[i], acc);
    STAMP(12);
    *lp<f4>(a_w0) = acc;
    BAR();
    asm volatile("s_barrier" ::: "memory");
  }
  float mb = 0.f, ri = 0.f;
  if (w == 2) {
    __builtin_amdgcn_s_setprio(3);
    asm volatile("s_waitcnt vmcnt(0)" : "+v"(xa) :: "memory");
    ln_stats(xa, mb, ri);
    PIN(mb); PIN(ri);
  }
  asm volatile("s_waitcnt lgkmcnt(0)" : "+s"(tag));
  tag += 1u;
  unsigned tv = tag;
  PIN(tv);
  if (w == 2) {
    int a_qp = ((lane & 31) * 2 + (lane >> 5)) * 16, a_r0 = 116736 + po;
    PIN(a_qp); PIN(a_r0);
    BAR();
    f4 p[4];
#pragma unroll
    for (int i = 0; i < 4; ++i) p[i] = *lp<f4>(a_r0 + i * 1024);
    const f4 s = (p[0] + p[1]) + (p[2] + p[3]);
    const auto r0 = __builtin_amdgcn_permlane32_swap(asu(s[0]), asu(s[2]), false, false), r1 = __builtin_amdgcn_permlane32_swap(asu(s[1]), asu(s[3]), false, false);
    stsc(v4u{asu((asf(r0[0]) - mb * asf(r0[1])) * ri), tv, asu((asf(r1[0]) - mb * asf(r1[1])) * ri), tv}, rs, a_qp, WS_QP + (h * 8 + jj) * 1024);
    asm volatile("s_barrier" ::: "memory");
    STAMP(1);
    __builtin_amdgcn_s_setprio(0);
    if (!isA) {
      v4u xn;
#pragma unroll
      for (int i = 0; i < 4; ++i) { const unsigned d = pk(bflo(xa[i]) - mb, bfhi(xa[i]) - mb); xn[i] = pk(bflo(d) * ri, bfhi(d) * ri); }
      *lp<v4u>(XNI + po) = xn;
    }
  }
  if (w == 3) {
    VMWAIT(0);
    if (!isA && lane == 0) __builtin_amdgcn_raw_buffer_store_b64(v2u{tv, tv}, rs, 0, WS_FL + cons * 8, AUX_SC1);
    asm volatile("s_barrier\n\ts_barrier" ::: "memory");
  }
  if (w >= 2) t_pub = __builtin_amdgcn_s_memrealtime();
#define DLY_G DELAY_G
#define DLY_W DELAY_W
#define DLY_H DELAY_H
#define DLY_U DELAY_U
#define DLY_A DELAY_A
#define DLY_Q DELAY_Q
#define DLY_O DELAY_O
#define DLY_F DELAY_F
  if (!isA) {
    const int xrow0 = 128 * j + 16 * w;
    COMMON_CONSTS
    int a_wk = QAI + w * 4096 + rbase, a_qw = 73728 + lane * 4, a_qr = 73728 + g * 64, a_qa = 74240 + w * 32 + g * 8;
    int a_wk4[4];
#pragma unroll
    for (int s = 0; s < 4; ++s) { a_wk4[s] = a_wk ^ (s * 16); PIN(a_wk4[s]); }
    PIN(a_qw); PIN(a_qr); PIN(a_qa);
    v4u fw[4];
    const GAS char* pf = (const GAS char*)((r16 < 8) ? wg : wu) + (h < 28 ? (xrow0 + 4 * g + 1) * 3584 + h * 128 : 3584) + (r16 & 7) * 16;
#define GU4() asm volatile("global_load_dwordx4 %0, %4, off offset:-3584" GU_NT "\n\tglobal_load_dwordx4 %1, %4, off" GU_NT "\n\tglobal_load_dwordx4 %2, %4, off offset:3584" GU_NT "\n\tglobal_load_dwordx4 %3, %5, off" GU_NT \
                   : "=&v"(fw[0]), "=&v"(fw[1]), "=&v"(fw[2]), "=&v"(fw[3]) : "v"(pf), "v"(pf + 7168) : "memory")
    if (w == 4) {
      v4u p[8]; int spins = 0; unsigned bad;
      wait_until(t_pub + DLY_H);
      for (;;) {
        FENCE();
#pragma unroll
        for (int i = 0; i < 8; ++i) p[i] = ldsc(rs, po + (i & 3) * 1024, WS_QP + h * 8192 + (i >> 2) * 4096);
        bad = 0;
#pragma unroll
        for (int i = 0; i < 8; ++i) bad |= (p[i].y ^ tv) | (p[i].w ^ tv);
        PIN(bad);
        if (all_ok(bad == 0) || ++spins >= SPIN_CAP) break;
        __builtin_amdgcn_s_sleep(1);
      }
      const float q0 = ((asf(p[0].x) + asf(p[1].x)) + (asf(p[2].x) + asf(p[3].x))) + ((asf(p[4].x) + asf(p[5].x)) + (asf(p[6].x) + asf(p[7].x)));
      const float q1 = ((asf(p[0].z) + asf(p[1].z)) + (asf(p[2].z) + asf(p[3].z))) + ((asf(p[4].z) + asf(p[5].z)) + (asf(p[6].z) + asf(p[7].z)));
      *lp<unsigned>(a_qw) = pk(q0, q1);
    }
    const bool gu_early = (GUW >> w) & 1;
    if (gu_early) { wait_until(t_pub + DLY_O); GU4(); }
    if (w < 2) { VMWAIT(0); STAMP(13); }
    BAR();
    STAMP(2);
    v4u xm[4];
    {
      v4u qf[4], wkv[4];
#pragma unroll
      for (int s = 0; s < 4; ++s) { wkv[s] = lds128(a_wk4[s]); qf[s] = lds128(a_qr + s * 16); }
      SCHED();
      f4 acc = {0.f, 0.f, 0.f, 0.f};
#pragma unroll
      for (int s = 0; s < 4; ++s) acc = mfma(wkv[s], qf[s], acc);
      const float sc = 0.08838834764831845f * 1.4426950408889634f;
      if (r16 == 0) *lp<v2u>(a_qa) = v2u{pkc(acc[0] * sc, acc[1] * sc), pkc(acc[2] * sc, acc[3] * sc)};
    }
    BAR();
    if (w == 0 && lane < 16) stsc(lds128(74240 + po), rs, po, WS_QA + cons * 256);
    STAMP(3);
    {
      const v2u x4 = *lp<v2u>(XNI + (xrow0 + 4 * g) * 2);
      xm[0] = xmask(__builtin_amdgcn_perm(x4.x, x4.x, 0x01000100u), mk); xm[1] = xmask(__builtin_amdgcn_perm(x4.x, x4.x, 0x03020302u), mk);
      xm[2] = xmask(__builtin_amdgcn_perm(x4.y, x4.y, 0x01000100u), mk); xm[3] = xmask(__builtin_amdgcn_perm(x4.y, x4.y, 0x03020302u), mk);
#pragma unroll
      for (int i = 0; i < 4; ++i) PIN(xm[i]);
    }
    BAR();
    if (!gu_early) GU4();
    wait_until(t_pub + DLY_Q);
    v4u buv[4], bo[4][4], st;
    int tq[4];
#pragma unroll
    for (int t = 0; t < 4; ++t) tq[t] = (w >> 1) * 256 + (trbaseK ^ (32 * (4 * (w & 1) + t)));
    const v4u sws = mksrd(ws, WS_END);
    int a_st = (4 * g + (r16 & 3)) * 512, a_od = g * 1024 + r16 * 16, a_w1 = (g >> 1) * 12288 + w * 272 + r16 * 16 + (g & 1) * 8, a_r1 = (r16 & 7) * 272 + g * 16,
        a_w2 = 8192 + ((16 * w + r16 - 32 * j) & 127) * 2, a_r2 = 8192 + g * 16, a_o = g * 16384 + r16 * 8, a_sw = 4096 + w * 8, a_sr = 4096 + (g & 1) * 32;
    PIN(a_st); PIN(a_od); PIN(a_w1); PIN(a_r1); PIN(a_w2); PIN(a_r2); PIN(a_o); PIN(a_sw); PIN(a_sr);
    {
      const v4u suv = mksrd(wuv, 4194304), so = mksrd(wo, 4194304);
      const int so2 = (h * 128 + 8 * (w >> 2) + (w & 3)) * 1024;
#define WO4(n) _Pragma("unroll") for (int i = 0; i < 4; ++i) dmaw<(WO_NT >> (n)) & 1>(so, (i & 1) ? vrow1 : vrow0, so2 + ((((n) + j) & 3) * 32 + 16 * (i & 1) + 4 * (i >> 1)) * 1024, (32 * (n) + w + 8 * i) * 1024)
#define RD_UV() _Pragma("unroll") for (int ks = 0; ks < 4; ++ks) { const int a = QAI + (trbase ^ (32 * w)) + ks * 8192; const v2u lo = ldstr(a), hi = ldstr(a + 4096); buv[ks] = v4u{lo.x, lo.y, hi.x, hi.y}; }
#define RD_BO(n) _Pragma("unroll") for (int t = 0; t < 4; ++t) { const v2u lo = ldstr(tq[t] + (n) * 32768), hi = ldstr(tq[t] + (n) * 32768 + 16384); bo[t][n] = v4u{lo.x, lo.y, hi.x, hi.y}; }
      const int vo1 = g * 16 * 8192 + dchunk, so1 = (128 * j + 2 * (w & 3) + (w >> 2)) * 8192 + h * 256;
#define SB() asm volatile("s_barrier" ::: "memory")
#define WB(n) asm volatile("s_waitcnt vmcnt(" #n ") lgkmcnt(0)\n\ts_barrier" ::: "memory")
#define UV4() _Pragma("unroll 1") for (int n = 0; n < 4; ++n) dmaw<UV_NT>(suv, vo1, so1 + (64 * (n >> 1) + 8 * (n & 1)) * 8192, QAI + (32 * n + 4 * w) * 256)
#define S1(scr) { \
      asm volatile("s_waitcnt vmcnt(4)" : "+v"(fw[0]), "+v"(fw[1]), "+v"(fw[2]), "+v"(fw[3]) :: "memory"); \
      f4 acc = {0.f, 0.f, 0.f, 0.f}; \
      _Pragma("unroll") for (int i = 0; i < 4; ++i) acc = mfma(xm[i], fw[i], acc); \
      if (lane < 32) *lp<f4>((scr) + w * 512 + r16 * 32 + (lane & 16)) = acc; \
      BAR(); \
      if (w == 0) { \
        f2 p[8]; \
        _Pragma("unroll") for (int i = 0; i < 8; ++i) p[i] = *lp<f2>((scr) + lane * 8 + i * 512); \
        const f2 s = ((p[0] + p[1]) + (p[2] + p[3])) + ((p[4] + p[5]) + (p[6] + p[7])); \
        stsc(v4u{asu(s.x), tv, asu(s.y), tv}, rs, po, WS_FP + cons * 1024); \
        STAMP(15); \
      } \
      SB(); }
#define ST1() asm volatile("buffer_load_dwordx4 %0, %1, %2, %3 offen sc1" : "=&v"(st) : "v"(a_st), "s"(sws), "s"(WS_ST + w * 8192 + h * 16) : "memory")
      WO4(0); S1(102400)
      UV4(); SB(); WO4(1); WB(8); WO4(2); RD_BO(0) WB(8); WO4(3); RD_UV() WB(8); ST1(); RD_BO(1) WB(5); RD_BO(2)
      asm volatile("s_waitcnt vmcnt(0)" : "+v"(st) :: "memory");
      STAMP(14);
    }
    v4u od[4];
    {
      for (int spins = 0; !all_ok((st.y == tv) & (st.w == tv)) && spins < SPIN_CAP; ++spins)
        asm volatile("buffer_load_dwordx4 %0, %1, %2, %3 offen sc1\n\ts_waitcnt vmcnt(0)" : "=&v"(st) : "v"(a_st), "s"(sws), "s"(WS_ST + w * 8192 + h * 16) : "memory");
      STAMP(6);
#pragma unroll
      for (int i = 0; i < 4; ++i) od[i] = ldsc(rs, a_od + i * 256, WS_OP + (cons * 128 + 16 * w) * 256);
      SCHED();
      const float m = asf(st.x);
      float mw = fmx(m, dpp<0xB1>(m)); mw = fmx(mw, dpp<0x4E>(mw)); mw = max32(max16(mw));
      const float e = ex2(m - mw);
      const unsigned wb = pk(e, e);
      xm[0] = xmask(dppu<0x00>(wb), mk); xm[1] = xmask(dppu<0x55>(wb), mk); xm[2] = xmask(dppu<0xAA>(wb), mk); xm[3] = xmask(dppu<0xFF>(wb), mk);
      float lw = bflo(wb) * asf(st.z);
      lw += dpp<0xB1>(lw); lw += dpp<0x4E>(lw); lw = add32(add16(lw));
      *lp<f2>(a_sw) = f2{mw, lw};
#pragma unroll
      for (int i = 0; i < 4; ++i) PIN(xm[i]);
      SCHED();
    }
    {
      int spins = 0; f4 acc;
      for (;;) {
        unsigned bad = 0;
        acc = f4{0.f, 0.f, 0.f, 0.f};
#pragma unroll
        for (int i = 0; i < 4; ++i) { acc = mfma(xm[i], od[i], acc); bad = umax4(bad, od[i]); }
        if (all_ok(bad != EMPTY) || ++spins >= SPIN_CAP) break;
        FENCE();
#pragma unroll
        for (int i = 0; i < 4; ++i) od[i] = ldsc(rs, a_od + i * 256, WS_OP + (cons * 128 + 16 * w) * 256);
      }
      *lp<v2u>(a_w1) = v2u{pk(acc[0], acc[1]), pk(acc[2], acc[3])};
    }
    STAMP(7);
    BAR();
    STAMP(4);
    float ri;
    {
      v4u af[4];
      const f4 s0 = *lp<f4>(a_sr), s1 = *lp<f4>(a_sr + 16);
#pragma unroll
      for (int ks = 0; ks < 4; ++ks) af[ks] = lds128(a_r1 + ks * 64);
      RD_BO(3)
      SCHED();
      const float M = max16(fmx(mx3(s0[0], s0[2], s1[0]), s1[2]));
      f4 acc = {0.f, 0.f, 0.f, 0.f};
#pragma unroll
      for (int ks = 0; ks < 4; ++ks) acc = mfma(af[ks], buv[ks], acc);
      const float f0 = ex2(s0[0] - M), f1 = ex2(s0[2] - M), f2_ = ex2(s1[0] - M), f3 = ex2(s1[2] - M);
      const float c = add16((f0 * acc[0] + f1 * acc[1]) + (f2_ * acc[2] + f3 * acc[3]));
      *lp<u16>(a_w2) = (u16)pk(c, c);
      SCHED();
      ri = rcp(add16((f0 * s0[1] + f1 * s0[3]) + (f2_ * s1[1] + f3 * s1[3])));
      PIN(ri);
    }
    STAMP(9);
    BAR();
    STAMP(10);
    {
      v4u cf[4];
#pragma unroll
      for (int ks = 0; ks < 4; ++ks) cf[ks] = lds128(a_r2 + ks * 64);
      SCHED();
      f4 acc[4];
#pragma unroll
      for (int t = 0; t < 4; ++t) acc[t] = f4{0.f, 0.f, 0.f, 0.f};
#pragma unroll
      for (int ks = 0; ks < 4; ++ks)
#pragma unroll
        for (int t = 0; t < 4; ++t) acc[t] = mfma(cf[ks], bo[t][ks], acc[t]);
      const float v01 = (g & 1) ? acc[1][0] : acc[0][0], v23 = (g & 1) ? acc[3][0] : acc[2][0];
      float v = ((g & 2) ? v23 : v01) * ri;
      PIN(v); STAMP(11);
      __builtin_amdgcn_raw_buffer_store_b64(v2u{asu(v), tv}, rs, a_o, WS_SP + (4 * w * 128 + cons) * 128, AUX_SC1);
    }
    STAMP(8);
    STAMP_OUT();
    __builtin_amdgcn_endpgm();
  }
  {
    const v4u sws = mksrd(ws, WS_END);
    const int ht = w & 1, kt = w >> 1;
    v4u k0[16], k1[16];
    v2u v0[4][4];
    const rsrc_t rx = mkrs(x, 1024);
    COMMON_CONSTS
    const int mi = ai & 3, fi = (ai < 112) ? (ai >> 2) : 0;
    int ka[4], ta[4], qa_[4], vo0 = vrow0, vo1 = vrow1;
#pragma unroll
    for (int s = 0; s < 4; ++s) {
      ka[s] = kt * 32768 + (rbaseK ^ (s * 16)); ta[s] = (w >> 1) * 256 + (trbaseK ^ (32 * (4 * (w & 1) + s))); qa_[s] = ka[s] + (QAI + ht * 16384 - kt * 32768);
      PIN(ka[s]); PIN(ta[s]); PIN(qa_[s]);
    }
    int a_d = (2 * g + (r16 >> 3)) * 1024 + (r16 & 7) * 16, a_mw = (16 * ht + r16) * 16 + kt * 4,
        a_s = (lane < 16) ? (16 * ht + r16) * 16 : WS_END, a_o = (16 * g + (w >> 1)) * 32768 + (w & 1) * 128 + r16 * 8, a_pq = QAI + po,
        a_c = (16 * w + g) * 32768 + r16 * 16;
    int a_mr = a_mw & ~15, a_lw = 2048 + g * 512 + a_mw, a_pw = a_pq + w * 1024;
    PIN(a_mr); PIN(a_lw); PIN(a_pw);
    PIN(a_mw); PIN(a_s); PIN(a_o); PIN(a_pq); PIN(a_c); PIN(vo0); PIN(vo1);
#define RD_U0() _Pragma("unroll") for (int i = 0; i < 4; ++i) _Pragma("unroll") for (int s = 0; s < 4; ++s) { k0[4 * i + s] = lds128(ka[s] + i * 256); v0[i][s] = ldstr(ta[s] + i * 32768); }
#define RD_K1() _Pragma("unroll") for (int s = 0; s < 16; ++s) k1[s] = lds128(ka[s & 3] + (s >> 2) * 256 + 16384)
#define FLAGS_OK() all_ok(((fl.x ^ tv) | (fl.y ^ tv) | (fl.z ^ tv) | (fl.w ^ tv)) == 0)
#define QA4() _Pragma("unroll") for (int q = 0; q < 4; ++q) dma16sc(sws, (q & 1) ? vo1 : vo0, WS_QA + (w + 8 * q) * 1024, QAI + (w + 8 * q) * 1024)
    if (w >= 2) {
      v4u fl;
      if (w == 3) {
        __builtin_amdgcn_s_setprio(3);
      }
      asm volatile("s_barrier" ::: "memory");
      if (w == 3) asm volatile("buffer_load_dwordx4 %0, %1, %2, %3 offen sc1" : "=&v"(fl) : "v"(po), "s"(sws), "s"(WS_FL) : "memory");
      RD_U0()
      if (w == 3) {
        asm volatile("s_waitcnt vmcnt(0)" : "+v"(fl) :: "memory");
        int spins = 0;
        while (!FLAGS_OK() && ++spins < SPIN_CAP)
          asm volatile("buffer_load_dwordx4 %0, %1, %2, %3 offen sc1\n\ts_waitcnt vmcnt(0)" : "=&v"(fl) : "v"(po), "s"(sws), "s"(WS_FL) : "memory");
        __builtin_amdgcn_s_setprio(0);
      }
    }
    asm volatile("s_waitcnt vmcnt(16)\n\ts_barrier" ::: "memory");
    STAMP(2);
    const bool early = (long long)(__builtin_amdgcn_s_memrealtime() - (t_pub + DLY_G)) >= 0;
    if (early) { QA4(); }
    if (w < 2) { RD_U0() }
    if (kt < 2) { RD_K1(); }
    v4u vf[4][4];
#pragma unroll
    for (int i = 0; i < 8; ++i) { const v2u hi = ldstr(ta[i & 3] + (i >> 2) * 32768 + 16384); vf[i >> 2][i & 3] = v4u{v0[i >> 2][i & 3].x, v0[i >> 2][i & 3].y, hi.x, hi.y}; }
    if (!early) { wait_until(t_pub + DLY_G); QA4(); }
    VMWAIT(4);
    BAR();
    STAMP(3);
    if (kt >= 2) { RD_K1(); }
#define RD_V1(i) { const v2u hi = ldstr(ta[(i) & 3] + ((i) >> 2) * 32768 + 16384); vf[(i) >> 2][(i) & 3] = v4u{v0[(i) >> 2][(i) & 3].x, v0[(i) >> 2][(i) & 3].y, hi.x, hi.y}; }
    f4 S0 = {0.f, 0.f, 0.f, 0.f}, S1 = {0.f, 0.f, 0.f, 0.f};
    PIN(S0); PIN(S1);
    for (int spins = 0;; ++spins) {
      VMWAIT(0);
      unsigned bad = 0;
#pragma unroll
      for (int q = 0; q < 4; ++q) bad = umax4(bad, lds128(a_pw + q * 8192));
      if (all_ok(bad != EMPTY) || spins >= SPIN_CAP) break;
      QA4();
    }
    BAR();
    STAMP(4);
    {
      v4u qf[16];
#pragma unroll
      for (int s = 0; s < QD; ++s) qf[s] = lds128(qa_[s & 3] + (s >> 2) * 256);
      SCHED();
#pragma unroll
      for (int s = 0; s < 16; ++s) {
        mfma_acc(S0, k0[s], qf[s]); mfma_acc(S1, k1[s], qf[s]);
        if (s + QD < 16) qf[s + QD] = lds128(qa_[(s + QD) & 3] + ((s + QD) >> 2) * 256);
        if (s >= 8) RD_V1(s)
        if (s < 8 && !(s & 1)) stsc(v4u{EMPTY, EMPTY, EMPTY, EMPTY}, rs, a_c, WS_OP + (s >> 1) * 131072 + ai * 256);
        SCHED();
      }
      float m0, mt;
      asm volatile("s_nop 7\n\tv_max3_f32 %0, %2, %3, %4\n\tv_max3_f32 %0, %0, %5, %6\n\tv_max3_f32 %1, %7, %8, %9\n\tv_max_f32 %0, %0, %1\n\t"
                   "v_mov_b32 %1, %0\n\ts_nop 1\n\tv_permlane16_swap_b32 %0, %1\n\tv_max_f32 %0, %0, %1\n\t"
                   "v_mov_b32 %1, %0\n\ts_nop 1\n\tv_permlane32_swap_b32 %0, %1\n\tv_max_f32 %0, %0, %1"
                   : "=&v"(m0), "=&v"(mt) : "v"(S0[0]), "v"(S0[1]), "v"(S0[2]), "v"(S0[3]), "v"(S1[0]), "v"(S1[1]), "v"(S1[2]), "v"(S1[3]));
      *lp<float>(a_mw) = m0;
    }
    BAR();
    STAMP(5);
    float M;
    int a_f = (r16 & 3) * 1024 + (64 * ((r16 >> 2) & 1) + 8 * (r16 >> 3) + 2 * g) * 8;
    {
      const f4 m4 = *lp<f4>(a_mr);
      PIN(a_f); PIN(a_d);
      asm("v_max3_f32 %0, %1, %2, %3\n\tv_max_f32 %0, %0, %4" : "=&v"(M) : "v"(m4[0]), "v"(m4[1]), "v"(m4[2]), "v"(m4[3]));
      float p[8];
#pragma unroll
      for (int i = 0; i < 4; ++i) { p[i] = ex2(S0[i] - M); p[4 + i] = ex2(S1[i] - M); }
      *lp<v4u>(a_pw) = v4u{pk(p[0], p[1]), pk(p[2], p[3]), pk(p[4], p[5]), pk(p[6], p[7])};
      *lp<float>(a_lw) = ((p[0] + p[1]) + (p[2] + p[3])) + ((p[4] + p[5]) + (p[6] + p[7]));
    }
    VMWAIT(0);
    v4u dw[2], fp;
    asm volatile("buffer_load_dwordx4 %0, %1, %2, %3 offen sc1" : "=&v"(fp) : "v"(a_f), "s"(sws), "s"(WS_FP + fi * 4096 + mi * 128) : "memory");
    BAR();
    STAMP(6);
    {
      v4u pf_[8];
      pf_[0] = lds128(a_pq);
      const f4 l4 = *lp<f4>(a_lw & ~15);
#pragma unroll
      for (int i = 1; i < 8; ++i) pf_[i] = lds128(a_pq + ((i & 3) * 2 + (i >> 2)) * 1024);
      SCHED();
#pragma unroll
      for (int hh = 0; hh < 2; ++hh) {
        f4 O[4];
#pragma unroll
        for (int t = 0; t < 4; ++t) O[t] = f4{0.f, 0.f, 0.f, 0.f};
#pragma unroll
        for (int ks = 0; ks < 4; ++ks) {
#pragma unroll
          for (int t = 0; t < 4; ++t) O[t] = mfma(pf_[4 * hh + ks], vf[ks][t], O[t]);
          if (hh == 0 && ks == 0) { SCHED(); if (kt == 0) stsc(v4u{asu(M), tv, asu(add32(add16((l4[0] + l4[1]) + (l4[2] + l4[3])))), tv}, rs, a_s, WS_ST + ai * 512); SCHED(); }
        }
        if (hh == 1) { PIN(O[3]); STAMP(13); }
#pragma unroll
        for (int i = 0; i < 4; ++i)
          __builtin_amdgcn_raw_buffer_store_b64(v2u{pkc(O[0][i], O[1][i]), pkc(O[2][i], O[3][i])}, rs, a_o, WS_OP + (16 * hh + i) * 131072 + ai * 256, AUX_SC1);
      }
    }
    STAMP(7);
    const u64 t_pv = __builtin_amdgcn_s_memrealtime();
    asm volatile("s_waitcnt vmcnt(8)" : "+v"(fp) :: "memory");
    if (ai < 112) {
      wait_until(t_pv + DLY_U);
      const rsrc_t rd = mkrs(wd, 1835008);
#pragma unroll
      for (int i = 0; i < 2; ++i) dw[i] = ldnt(rd, a_d, (64 * fi + 16 * mi + 8 * i) * 1024 + w * 128);
      SCHED();
      const int gu = (r16 >> 2) & 1, hi = r16 >> 3;
      v4u p = fp;
      int spins = 0;
      for (; !all_ok((p.y == tv) & (p.w == tv)) && spins < SPIN_CAP; ++spins)
        asm volatile("buffer_load_dwordx4 %0, %1, %2, %3 offen sc1\n\ts_waitcnt vmcnt(0)" : "=&v"(p) : "v"(a_f), "s"(sws), "s"(WS_FP + fi * 4096 + mi * 128) : "memory");
      STAMP(14);
      float v0_ = asf(p.x), v1_ = asf(p.z);
      v0_ += dpp<0xB1>(v0_); v1_ += dpp<0xB1>(v1_);
      v0_ += dpp<0x4E>(v0_); v1_ += dpp<0x4E>(v1_);
      const float o0 = dpp<0x141>(v0_), o1 = dpp<0x141>(v1_);
      float hv[2];
#pragma unroll
      for (int e = 0; e < 2; ++e) {
        const float gt = rbf(gu ? (e ? o1 : o0) : (e ? v1_ : v0_)), up = rbf(gu ? (e ? v1_ : v0_) : (e ? o1 : o0));
        const float z2 = 2.0f * 0.7978845608028654f * 1.4426950408889634f * (gt + 0.044715f * gt * gt * gt);
        hv[e] = rbf(gt * rcp(1.0f + ex2(-z2))) * up;
      }
      const float t0 = dpp<0x128>(hv[0]), t1 = dpp<0x128>(hv[1]);
      unsigned mk2[4];
#pragma unroll
      for (int k = 0; k < 4; ++k) mk2[k] = mk[k] | dppu<0x128>(mk[k]);
      const float ha = hi ? t1 : hv[0], hb = hi ? hv[1] : t0;
      f4 acc = {0.f, 0.f, 0.f, 0.f};
      acc = mfma(xmask(pk(ha, ha), mk2), dw[0], acc);
      acc = mfma(xmask(pk(hb, hb), mk2), dw[1], acc);
      float y[4];
#pragma unroll
      for (int i = 0; i < 4; ++i) {
        const unsigned u = asu(dpp<0x128>(acc[i]));
        auto r = __builtin_amdgcn_permlane32_swap(u, u, false, false);
        y[i] = acc[i] + asf(r[1]);
      }
      const auto ya = __builtin_amdgcn_permlane32_swap(asu(y[0]), asu(y[2]), false, false), yb = __builtin_amdgcn_permlane32_swap(asu(y[1]), asu(y[3]), false, false);
      if (r16 < 8) stsc(v4u{ya[0], tv, yb[0], tv}, rs, (r16 >> 1) * 16384 + ((r16 & 1) * 8 + 4 * (g & 1) + 2 * (g >> 1)) * 8, WS_YP + (4 * w * 128 + ai) * 128);
    }
    STAMP(8);
    if (w >= 6) {
      const int off = (lane >> 1) * 128 + (lane & 1) * 16, so = ((w == 7) ? WS_SP : WS_YP) + h * 16384 + j * 32;
      const unsigned xo = __builtin_amdgcn_raw_buffer_load_b32(rx, (lane & 1) * 4, h * 32 + j * 8, 0);
      const bool skip3 = (w == 6) && (lane >= 32);
      v4u p[4]; int spins = 0; unsigned bad;
      {
        const int go = so + ((ai * 5 + 3) % 112) * 128;
        wait_until(t_pv + 80);
        for (int sp = 0; sp < SPIN_CAP; ++sp) {
          FENCE();
          const v2u t = __builtin_amdgcn_raw_buffer_load_b64(rs, 0, go, AUX_SC1);
          if (__builtin_amdgcn_readfirstlane(t.y) == tag) break;
          __builtin_amdgcn_s_sleep(2);
        }
        wait_until(__builtin_amdgcn_s_memrealtime() + ((w == 7) ? DLY_F : DLY_W));
      }
      do {
        FENCE();
#pragma unroll
        for (int n = 0; n < 4; ++n) p[n] = ldsc(rs, off, so + n * 4096);
        if (skip3) p[3] = v4u{0u, tv, 0u, tv};
        bad = 0;
#pragma unroll
        for (int n = 0; n < 4; ++n) bad |= (p[n].y ^ tv) | (p[n].w ^ tv);
        PIN(bad);
      } while (!all_ok(bad == 0) && ++spins < SPIN_CAP);
      float a0 = (asf(p[0].x) + asf(p[1].x)) + (asf(p[2].x) + asf(p[3].x));
      float a1 = (asf(p[0].z) + asf(p[1].z)) + (asf(p[2].z) + asf(p[3].z));
      a0 += dpp<0x128>(a0); a1 += dpp<0x128>(a1);
      a0 += dpp<0x124>(a0); a1 += dpp<0x124>(a1);
      a0 += dpp<0x122>(a0); a1 += dpp<0x122>(a1);
      a0 = rbf(add32(add16(a0))); a1 = rbf(add32(add16(a1)));
      if (w == 6) {
        a0 = rbf(a0 * rcp(1.0f + ex2(-1.4426950408889634f * a0)));
        a1 = rbf(a1 * rcp(1.0f + ex2(-1.4426950408889634f * a1)));
        if (lane < 2) *lp<f2>(1024 + lane * 8) = f2{a0, a1};
        STAMP(9);
        BAR();
      } else {
        BAR();
        const f2 gt = *lp<f2>(1024 + (lane & 1) * 8);
        const unsigned o = pk(bflo(xo) + rbf(a0 * gt.x), bfhi(xo) + rbf(a1 * gt.y));
        if (lane < 2) *(GAS unsigned*)(out + 16 * h + 4 * j + 2 * lane) = o;
        if (ai == 0 && lane == 0) *(GAS unsigned*)ws = tag;
        STAMP(10);
      }
    }
  }
  STAMP_OUT();
}

extern "C" void kernel_launch(void* const* d_in, const int* in_sizes, int n_in, void* d_out, int out_size, void* d_ws, size_t ws_size, hipStream_t stream) {
  (void)hipFuncSetAttribute((const void*)mla_decode, hipFuncAttributeMaxDynamicSharedMemorySize, LDS_BYTES);
  mla_decode<<<256, 512, LDS_BYTES, stream>>>((const u16*)d_in[0], (const u16*)d_in[2], (unsigned char*)d_ws + WS_BASE(ws_size), (const u16*)d_in[3], (const u16*)d_in[1], (const u16*)d_in[4], (const u16*)d_in[5],
                                              (const u16*)d_in[7], (const u16*)d_in[8], (const u16*)d_in[9], (u16*)d_out, d_in[0]);
}
```

```cpp
#include <hip/hip_runtime.h>
#include <stdint.h>

typedef unsigned short u16;
typedef unsigned long long u64;
typedef unsigned v4u __attribute__((ext_vector_type(4)));
typedef unsigned v2u __attribute__((ext_vector_type(2)));
typedef float f4 __attribute__((ext_vector_type(4)));
typedef float f2 __attribute__((ext_vector_type(2)));
typedef __bf16 bf8 __attribute__((ext_vector_type(8)));
typedef __bf16 bf2 __attribute__((ext_vector_type(2)));
typedef short s4 __attribute__((ext_vector_type(4)));
#define GAS __attribute__((address_space(1)))
#define LAS __attribute__((address_space(3)))

constexpr int DLY_H = 45;
constexpr int DLY_E = 55;
constexpr int DLY_G = 220;
constexpr int DLY_Q = 285;
constexpr int DLY_U = 30;
constexpr int DLY_F = 80;
constexpr int QD = 6;

constexpr int WS_FL = 1024;
constexpr int WS_QP = 4096;
constexpr int WS_QA = WS_QP + 262144;
constexpr int WS_FP = WS_QA + 32768;
constexpr int WS_ST = WS_FP + 131072;
constexpr int WS_SP = WS_ST + 262144;
constexpr int WS_YP = WS_SP + 524288;
constexpr int WS_OP = WS_YP + 524288;
constexpr int WS_END = WS_OP + 4194304;
constexpr int LDS_BYTES = 163840;
constexpr int CODE_BYTES = 13824;
constexpr int SPIN_CAP = 1 << 16;
constexpr int AUX_SC1 = 16, AUX_NT = 2;
constexpr int QAI = 131072;
constexpr int XNI = 65536;
constexpr unsigned EMPTY = 0xFFFFFFFFu;

#define BAR() asm volatile("s_waitcnt lgkmcnt(0)\n\ts_barrier" ::: "memory")
#define FENCE() asm volatile("" ::: "memory")
#define SCHED() __builtin_amdgcn_sched_barrier(0)
#define VMWAIT(n) asm volatile("s_waitcnt vmcnt(" #n ")" ::: "memory")
#define PIN(x) asm volatile("" : "+v"(x))

typedef __amdgpu_buffer_rsrc_t rsrc_t;
__device__ __forceinline__ float bflo(unsigned u) { return __uint_as_float(u << 16); }
__device__ __forceinline__ float bfhi(unsigned u) { return __uint_as_float(u & 0xFFFF0000u); }
__device__ __forceinline__ unsigned pk(float a, float b) { f2 f = {a, b}; return __builtin_bit_cast(unsigned, __builtin_convertvector(f, bf2)); }
__device__ __forceinline__ unsigned pkc(float a, float b) { return min(pk(a, b), EMPTY - 1u); }
__device__ __forceinline__ float rbf(float a) { return bflo(pk(a, a)); }
__device__ __forceinline__ float asf(unsigned u) { return __uint_as_float(u); }
__device__ __forceinline__ unsigned asu(float f) { return __float_as_uint(f); }
__device__ __forceinline__ float ex2(float x) { return __builtin_amdgcn_exp2f(x); }
__device__ __forceinline__ float rcp(float x) { return __builtin_amdgcn_rcpf(x); }
__device__ __forceinline__ f4 mfma(v4u a, v4u b, f4 c) {
  return __builtin_amdgcn_mfma_f32_16x16x32_bf16(__builtin_bit_cast(bf8, a), __builtin_bit_cast(bf8, b), c, 0, 0, 0);
}
__device__ __forceinline__ void mfma_acc(f4& c, v4u a, v4u b) { asm volatile("v_mfma_f32_16x16x32_bf16 %0, %1, %2, %0" : "+v"(c) : "v"(a), "v"(b)); }
template <int CTRL> __device__ __forceinline__ float dpp(float v) {
  return __builtin_bit_cast(float, __builtin_amdgcn_update_dpp(0, __builtin_bit_cast(int, v), CTRL, 0xf, 0xf, true));
}
template <int CTRL> __device__ __forceinline__ unsigned dppu(unsigned v) { return (unsigned)__builtin_amdgcn_update_dpp(0, (int)v, CTRL, 0xf, 0xf, true); }
__device__ __forceinline__ float add16(float v) { unsigned u = asu(v); auto r = __builtin_amdgcn_permlane16_swap(u, u, false, false); return asf(r[0]) + asf(r[1]); }
__device__ __forceinline__ float add32(float v) { unsigned u = asu(v); auto r = __builtin_amdgcn_permlane32_swap(u, u, false, false); return asf(r[0]) + asf(r[1]); }
__device__ __forceinline__ float fmx(float a, float b) { return __builtin_fmaxf(a, b); }
__device__ __forceinline__ float mx3(float a, float b, float c) { return fmx(fmx(a, b), c); }
__device__ __forceinline__ float max16(float v) { unsigned u = asu(v); auto r = __builtin_amdgcn_permlane16_swap(u, u, false, false); return fmx(asf(r[0]), asf(r[1])); }
__device__ __forceinline__ float max32(float v) { unsigned u = asu(v); auto r = __builtin_amdgcn_permlane32_swap(u, u, false, false); return fmx(asf(r[0]), asf(r[1])); }
__device__ __forceinline__ float wave_sum(float v) {
  v += dpp<0x128>(v); v += dpp<0x124>(v); v += dpp<0x122>(v); v += dpp<0x121>(v);
  return add32(add16(v));
}
__device__ __forceinline__ unsigned umax4(unsigned acc, v4u v) { return max(max(max(acc, v.x), v.y), max(v.z, v.w)); }
__device__ __forceinline__ rsrc_t mkrs(const void* p, int bytes) { return __builtin_amdgcn_make_buffer_rsrc((void*)p, 0, bytes, 0x00020000); }
__device__ __forceinline__ v4u mksrd(const void* p, unsigned bytes) { const u64 a = (u64)p; return v4u{(unsigned)a, (unsigned)(a >> 32) & 0xFFFFu, bytes, 0x00020000u}; }
__device__ __forceinline__ v4u ldnt(rsrc_t r, int voff, int soff) { return __builtin_amdgcn_raw_buffer_load_b128(r, voff, soff, AUX_NT); }
__device__ __forceinline__ v4u ldsc(rsrc_t r, int voff, int soff) { return __builtin_amdgcn_raw_buffer_load_b128(r, voff, soff, AUX_SC1); }
__device__ __forceinline__ void stsc(v4u v, rsrc_t r, int voff, int soff) { __builtin_amdgcn_raw_buffer_store_b128(v, r, voff, soff, AUX_SC1); }
__device__ __forceinline__ void dma16(v4u srd, int voff, int soff, int lds_dst) {
  asm volatile("s_mov_b32 m0, %3\n\ts_nop 0\n\tbuffer_load_dwordx4 %0, %1, %2 offen nt lds" :: "v"(voff), "s"(srd), "s"(soff), "s"(lds_dst) : "memory");
}
__device__ __forceinline__ void dma16p(v4u srd, int voff, int soff, int lds_dst) {
  asm volatile("s_mov_b32 m0, %3\n\ts_nop 0\n\tbuffer_load_dwordx4 %0, %1, %2 offen lds" :: "v"(voff), "s"(srd), "s"(soff), "s"(lds_dst) : "memory");
}
__device__ __forceinline__ void dma16sc(v4u srd, int voff, int soff, int lds_dst) {
  asm volatile("s_mov_b32 m0, %3\n\ts_nop 0\n\tbuffer_load_dwordx4 %0, %1, %2 offen sc1 lds" :: "v"(voff), "s"(srd), "s"(soff), "s"(lds_dst) : "memory");
}
template <typename T> __device__ __forceinline__ LAS T* lp(int off) { return (LAS T*)(uintptr_t)(unsigned)off; }
__device__ __forceinline__ v4u lds128(int off) { return *lp<v4u>(off); }
__device__ __forceinline__ v2u ldstr(int off) { return __builtin_bit_cast(v2u, __builtin_amdgcn_ds_read_tr16_b64_v4i16(lp<s4>(off))); }
__device__ __forceinline__ int swz(int r) { return ((r & 7) << 1) | ((r >> 3) & 1); }
__device__ __forceinline__ v4u xmask(unsigned xx, const unsigned (&mk)[4]) { return v4u{xx & mk[0], xx & mk[1], xx & mk[2], xx & mk[3]}; }
__device__ __forceinline__ bool all_ok(bool ok) { return __builtin_amdgcn_ballot_w64(!ok) == 0ull; }
__device__ __forceinline__ void wait_until(u64 t) { while ((long long)(__builtin_amdgcn_s_memrealtime() - t) < 0) __builtin_amdgcn_s_sleep(1); }

__device__ __forceinline__ void ln_stats(v4u xa, float& mb, float& ri) {
  float v[8] = {bflo(xa.x), bfhi(xa.x), bflo(xa.y), bfhi(xa.y), bflo(xa.z), bfhi(xa.z), bflo(xa.w), bfhi(xa.w)};
  const float mean = wave_sum(((v[0] + v[1]) + (v[2] + v[3])) + ((v[4] + v[5]) + (v[6] + v[7]))) * (1.0f / 512.0f);
  float q0 = 0.f, q1 = 0.f;
#pragma unroll
  for (int i = 0; i < 4; ++i) { const float d = v[i] - mean, e = v[i + 4] - mean; q0 += d * d; q1 += e * e; }
  const float var = wave_sum(q0 + q1) * (1.0f / 512.0f);
  mb = rbf(mean);
  ri = rcp(rbf(__builtin_amdgcn_sqrtf(rbf(rbf(var) + 1e-5f))));
}

typedef u64 v8q __attribute__((ext_vector_type(8)));
typedef u64 v4q __attribute__((ext_vector_type(4)));
extern "C" __global__ void __launch_bounds__(512, 2) mla_decode(const void*, const void*, void*, const void*, const void*, const void*, const void*, const void*,
                                                      const void*, const void*, void*, const void*) {
  v8q ka0; v4q ka1;
  asm volatile("s_load_dwordx16 %0, %2, 0x0\n\ts_load_dwordx8 %1, %2, 0x40" : "=&s"(ka0), "=&s"(ka1) : "s"(__builtin_amdgcn_kernarg_segment_ptr()));
  const int b = blockIdx.x, tid = threadIdx.x, lane = tid & 63, w = __builtin_amdgcn_readfirstlane(tid >> 6);
  const int xcd = b & 7;
  const bool isA = (b & 1) == 0;
  const int r16 = lane & 15, g = lane >> 4;
  const int h = (xcd >> 1) * 8 + ((b >> 3) & 7), j = b >> 6;
  const int ai = b >> 1;
  {
    u64 pc; asm volatile("s_getpc_b64 %0" : "=s"(pc));
    asm volatile("s_mov_b32 m0, %2\n\ts_nop 0\n\tglobal_load_lds_dword %0, %1" :: "v"(min(((b >> 3) * 8 + w) * 128, CODE_BYTES - 128) + r16 * 8), "s"(pc & ~255ull), "s"(114688) : "memory");
  }
#define COMMON_CONSTS \
  const int dchunk = (r16 ^ swz(4 * (w & 3) + g)) * 16;     \
  const int trrow = 4 * g + (r16 >> 2); \
  const int trbase = trrow * 256 + ((((r16 >> 1) & 1) ^ swz(trrow)) * 16) + 8 * (r16 & 1);     \
  const int rbase = r16 * 256 + ((4 * g) ^ swz(r16)) * 16;                                      \
  const int trbaseK = trbase + trrow * 768, rbaseK = rbase + r16 * 768;                         \
  const int vrow0 = g * 256 + (r16 ^ (2 * w)) * 16, vrow1 = vrow0 ^ 16;

  const int cons = h * 4 + j;
  const int jj = 2 * j + (b & 1);
  v4u wqv[4], xa; v2u xb;
  int oW = (64 * jj + 16 * (w & 3) + 4 * g) * 8192 + h * 256 + r16 * 16, oW1 = oW + 8192, oW2 = oW + 16384, oW3 = oW + 24576,
      ox = (w == 2) ? lane * 16 : (w == 3) ? WS_QA + cons * 256 + lane * 4 : (64 * jj + 16 * (w & 3) + 4 * g) * 2;
  unsigned em = EMPTY;
  PIN(oW); PIN(oW1); PIN(oW2); PIN(oW3); PIN(ox); PIN(em);
  SCHED();
  asm volatile("s_waitcnt lgkmcnt(0)" : "+s"(ka0), "+s"(ka1));
  const u16 *x = (const u16*)ka0[0], *wq = (const u16*)ka0[1], *wuk = (const u16*)ka0[3], *ckv = (const u16*)ka0[4], *wuv = (const u16*)ka0[5], *wo = (const u16*)ka0[6],
            *wg = (const u16*)ka0[7], *wu = (const u16*)ka1[0], *wd = (const u16*)ka1[1];
  unsigned char* ws = (unsigned char*)ka0[2];
  u16* out = (u16*)ka1[2];
  if (w >= 4) {
    asm volatile("global_load_dwordx2 %4, %5, %6\n\tglobal_load_dwordx4 %0, %7, %11 nt\n\tglobal_load_dwordx4 %1, %8, %11 nt\n\tglobal_load_dwordx4 %2, %9, %11 nt\n\tglobal_load_dwordx4 %3, %10, %11 nt"
                 : "=&v"(wqv[0]), "=&v"(wqv[1]), "=&v"(wqv[2]), "=&v"(wqv[3]), "=&v"(xb) : "v"(ox), "s"(x), "v"(oW), "v"(oW1), "v"(oW2), "v"(oW3), "s"(wq) : "memory");
  }
  if (w == 2) asm volatile("global_load_dwordx4 %0, %1, %2" : "=&v"(xa) : "v"(ox), "s"(x) : "memory");
  if (w == 3 && !isA) asm volatile("global_store_dword %0, %1, %2 sc1" :: "v"(ox), "v"(em), "s"(ws) : "memory");
  unsigned tag;
  asm volatile("s_load_dword %0, %1, 0x0\n\ts_barrier" : "=&s"(tag) : "s"(ws) : "memory");
  int po = lane * 16;
  u64 t_pub = 0;
  if (w < 2) {
    if (isA) {
      const v4u skv = mksrd(ckv, 16777216);
      const int so = (128 * ai + w) * 1024, vb = po ^ (w * 32);
#pragma unroll 1
      for (int i = 0; i < 8; ++i) {
        const int blk = ((2 * i) & 7) + (i >> 2);
        if (i == 1) { asm volatile("s_barrier\n\ts_barrier" ::: "memory"); t_pub = __builtin_amdgcn_s_memrealtime(); }
        if (i == 6) asm volatile("s_waitcnt vmcnt(16)\n\ts_barrier" ::: "memory");
#pragma unroll
        for (int r = 0; r < 8; ++r) dma16(skv, vb ^ ((r & 3) * 64 + (r >> 2) * 16), so + blk * 16384 + r * 2048, w * 1024 + blk * 16384 + r * 2048);
      }
    } else {
      const v4u sk = mksrd(wuk, 4194304);
      const int vk = g * 8192 + (r16 ^ (2 * g)) * 16;
#pragma unroll 1
      for (int i = 0; i < 4; ++i) {
        if (i == 3) { asm volatile("s_barrier\n\ts_barrier" ::: "memory"); t_pub = __builtin_amdgcn_s_memrealtime(); }
#pragma unroll
        for (int n = 0; n < 4; ++n) dma16(sk, vk ^ ((n & 1) * 128 + (n >> 1) * 16), (128 * j + 64 * w + 16 * i + 4 * n) * 8192 + h * 256, QAI + (16 * w + 4 * i + n) * 1024);
      }
    }
  }
  SCHED();
  const rsrc_t rs = mkrs(ws, WS_END);
  unsigned mk[4];
#pragma unroll
  for (int k = 0; k < 4; ++k) { mk[k] = ((r16 >> 1) == k) ? ((r16 & 1) ? 0xFFFF0000u : 0x0000FFFFu) : 0u; PIN(mk[k]); }
  if (w >= 4) {
    unsigned mk2[4];
    v4u xm[4];
#pragma unroll
    for (int k = 0; k < 4; ++k) { mk2[k] = mk[k] | dppu<0x128>(mk[k]); PIN(mk2[k]); }
    int a_w0 = 116736 + (w - 4) * 1024 + (g >> 1) * 512 + r16 * 32 + (lane & 16);
    PIN(a_w0);
    asm volatile("s_waitcnt vmcnt(4)" : "+v"(xb) :: "memory");
#pragma unroll
    for (int k = 0; k < 2; ++k) {
      const unsigned xp = (r16 >= 8) ? 0x3F803F80u : xb[k];
      xm[2 * k] = xmask(__builtin_amdgcn_perm(xp, xp, 0x01000100u), mk2); xm[2 * k + 1] = xmask(__builtin_amdgcn_perm(xp, xp, 0x03020302u), mk2);
      PIN(xm[2 * k]); PIN(xm[2 * k + 1]);
    }
    SCHED();
    f4 acc = {0.f, 0.f, 0.f, 0.f};
    asm volatile("s_waitcnt vmcnt(0)" : "+v"(wqv[0]), "+v"(wqv[1]), "+v"(wqv[2]), "+v"(wqv[3]) :: "memory");
#pragma unroll
    for (int i = 0; i < 4; ++i) acc = mfma(xm[i], wqv[i], acc);
    *lp<f4>(a_w0) = acc;
    BAR();
    asm volatile("s_barrier" ::: "memory");
  }
  float mb = 0.f, ri = 0.f;
  if (w == 2) {
    __builtin_amdgcn_s_setprio(3);
    asm volatile("s_waitcnt vmcnt(0)" : "+v"(xa) :: "memory");
    ln_stats(xa, mb, ri);
    PIN(mb); PIN(ri);
  }
  asm volatile("s_waitcnt lgkmcnt(0)" : "+s"(tag));
  tag += 1u;
  unsigned tv = tag;
  PIN(tv);
  if (w == 2) {
    int a_qp = ((lane & 31) * 2 + (lane >> 5)) * 16, a_r0 = 116736 + po;
    PIN(a_qp); PIN(a_r0);
    BAR();
    f4 p[4];
#pragma unroll
    for (int i = 0; i < 4; ++i) p[i] = *lp<f4>(a_r0 + i * 1024);
    const f4 s = (p[0] + p[1]) + (p[2] + p[3]);
    const auto r0 = __builtin_amdgcn_permlane32_swap(asu(s[0]), asu(s[2]), false, false), r1 = __builtin_amdgcn_permlane32_swap(asu(s[1]), asu(s[3]), false, false);
    stsc(v4u{asu((asf(r0[0]) - mb * asf(r0[1])) * ri), tv, asu((asf(r1[0]) - mb * asf(r1[1])) * ri), tv}, rs, a_qp, WS_QP + (h * 8 + jj) * 1024);
    asm volatile("s_barrier" ::: "memory");
    __builtin_amdgcn_s_setprio(0);
    if (!isA) {
      v4u xn;
#pragma unroll
      for (int i = 0; i < 4; ++i) { const unsigned d = pk(bflo(xa[i]) - mb, bfhi(xa[i]) - mb); xn[i] = pk(bflo(d) * ri, bfhi(d) * ri); }
      *lp<v4u>(XNI + po) = xn;
    }
  }
  if (w == 3) {
    VMWAIT(0);
    if (!isA && lane == 0) __builtin_amdgcn_raw_buffer_store_b64(v2u{tv, tv}, rs, 0, WS_FL + cons * 8, AUX_SC1);
    asm volatile("s_barrier\n\ts_barrier" ::: "memory");
  }
  if (w >= 2) t_pub = __builtin_amdgcn_s_memrealtime();
  if (!isA) {
    const int xrow0 = 128 * j + 16 * w;
    COMMON_CONSTS
    int a_wk = QAI + w * 4096 + rbase, a_qw = 73728 + lane * 4, a_qr = 73728 + g * 64, a_qa = 74240 + w * 32 + g * 8;
    int a_wk4[4];
#pragma unroll
    for (int s = 0; s < 4; ++s) { a_wk4[s] = a_wk ^ (s * 16); PIN(a_wk4[s]); }
    PIN(a_qw); PIN(a_qr); PIN(a_qa);
    v4u fw[4];
    const GAS char* pf = (const GAS char*)((r16 < 8) ? wg : wu) + (h < 28 ? (xrow0 + 4 * g + 1) * 3584 + h * 128 : 3584) + (r16 & 7) * 16;
#define GU4() asm volatile("global_load_dwordx4 %0, %4, off offset:-3584 nt\n\tglobal_load_dwordx4 %1, %4, off nt\n\tglobal_load_dwordx4 %2, %4, off offset:3584 nt\n\tglobal_load_dwordx4 %3, %5, off nt" \
                   : "=&v"(fw[0]), "=&v"(fw[1]), "=&v"(fw[2]), "=&v"(fw[3]) : "v"(pf), "v"(pf + 7168) : "memory")
    if (w == 4) {
      v4u p[8]; int spins = 0; unsigned bad;
      wait_until(t_pub + DLY_H);
      for (;;) {
        FENCE();
#pragma unroll
        for (int i = 0; i < 8; ++i) p[i] = ldsc(rs, po + (i & 3) * 1024, WS_QP + h * 8192 + (i >> 2) * 4096);
        bad = 0;
#pragma unroll
        for (int i = 0; i < 8; ++i) bad |= (p[i].y ^ tv) | (p[i].w ^ tv);
        PIN(bad);
        if (all_ok(bad == 0) || ++spins >= SPIN_CAP) break;
        __builtin_amdgcn_s_sleep(1);
      }
      const float q0 = ((asf(p[0].x) + asf(p[1].x)) + (asf(p[2].x) + asf(p[3].x))) + ((asf(p[4].x) + asf(p[5].x)) + (asf(p[6].x) + asf(p[7].x)));
      const float q1 = ((asf(p[0].z) + asf(p[1].z)) + (asf(p[2].z) + asf(p[3].z))) + ((asf(p[4].z) + asf(p[5].z)) + (asf(p[6].z) + asf(p[7].z)));
      *lp<unsigned>(a_qw) = pk(q0, q1);
    }
    const bool gu_early = (0xEC >> w) & 1;
    if (gu_early) { wait_until(t_pub + DLY_E); GU4(); }
    if (w < 2) { VMWAIT(0); }
    BAR();
    v4u xm[4];
    {
      v4u qf[4], wkv[4];
#pragma unroll
      for (int s = 0; s < 4; ++s) { wkv[s] = lds128(a_wk4[s]); qf[s] = lds128(a_qr + s * 16); }
      SCHED();
      f4 acc = {0.f, 0.f, 0.f, 0.f};
#pragma unroll
      for (int s = 0; s < 4; ++s) acc = mfma(wkv[s], qf[s], acc);
      const float sc = 0.08838834764831845f * 1.4426950408889634f;
      if (r16 == 0) *lp<v2u>(a_qa) = v2u{pkc(acc[0] * sc, acc[1] * sc), pkc(acc[2] * sc, acc[3] * sc)};
    }
    BAR();
    if (w == 0 && lane < 16) stsc(lds128(74240 + po), rs, po, WS_QA + cons * 256);
    {
      const v2u x4 = *lp<v2u>(XNI + (xrow0 + 4 * g) * 2);
      xm[0] = xmask(__builtin_amdgcn_perm(x4.x, x4.x, 0x01000100u), mk); xm[1] = xmask(__builtin_amdgcn_perm(x4.x, x4.x, 0x03020302u), mk);
      xm[2] = xmask(__builtin_amdgcn_perm(x4.y, x4.y, 0x01000100u), mk); xm[3] = xmask(__builtin_amdgcn_perm(x4.y, x4.y, 0x03020302u), mk);
#pragma unroll
      for (int i = 0; i < 4; ++i) PIN(xm[i]);
    }
    BAR();
    if (!gu_early) GU4();
    wait_until(t_pub + DLY_Q);
    v4u buv[4], bo[4][4], st;
    int tq[4];
#pragma unroll
    for (int t = 0; t < 4; ++t) tq[t] = (w >> 1) * 256 + (trbaseK ^ (32 * (4 * (w & 1) + t)));
    const v4u sws = mksrd(ws, WS_END);
    int a_st = (4 * g + (r16 & 3)) * 512, a_od = g * 1024 + r16 * 16, a_w1 = (g >> 1) * 12288 + w * 272 + r16 * 16 + (g & 1) * 8, a_r1 = (r16 & 7) * 272 + g * 16,
        a_w2 = 8192 + ((16 * w + r16 - 32 * j) & 127) * 2, a_r2 = 8192 + g * 16, a_o = g * 16384 + r16 * 8, a_sw = 4096 + w * 8, a_sr = 4096 + (g & 1) * 32;
    PIN(a_st); PIN(a_od); PIN(a_w1); PIN(a_r1); PIN(a_w2); PIN(a_r2); PIN(a_o); PIN(a_sw); PIN(a_sr);
    {
      const v4u suv = mksrd(wuv, 4194304), so = mksrd(wo, 4194304);
      const int so2 = (h * 128 + 8 * (w >> 2) + (w & 3)) * 1024;
#define WO4(n) _Pragma("unroll") for (int i = 0; i < 4; ++i) dma16p(so, (i & 1) ? vrow1 : vrow0, so2 + ((((n) + j) & 3) * 32 + 16 * (i & 1) + 4 * (i >> 1)) * 1024, (32 * (n) + w + 8 * i) * 1024)
#define RD_UV() _Pragma("unroll") for (int ks = 0; ks < 4; ++ks) { const int a = QAI + (trbase ^ (32 * w)) + ks * 8192; const v2u lo = ldstr(a), hi = ldstr(a + 4096); buv[ks] = v4u{lo.x, lo.y, hi.x, hi.y}; }
#define RD_BO(n) _Pragma("unroll") for (int t = 0; t < 4; ++t) { const v2u lo = ldstr(tq[t] + (n) * 32768), hi = ldstr(tq[t] + (n) * 32768 + 16384); bo[t][n] = v4u{lo.x, lo.y, hi.x, hi.y}; }
      const int vo1 = g * 16 * 8192 + dchunk, so1 = (128 * j + 2 * (w & 3) + (w >> 2)) * 8192 + h * 256;
#define SB() asm volatile("s_barrier" ::: "memory")
#define WB(n) asm volatile("s_waitcnt vmcnt(" #n ") lgkmcnt(0)\n\ts_barrier" ::: "memory")
#define UV4() _Pragma("unroll 1") for (int n = 0; n < 4; ++n) dma16(suv, vo1, so1 + (64 * (n >> 1) + 8 * (n & 1)) * 8192, QAI + (32 * n + 4 * w) * 256)
#define S1(scr) { \
      asm volatile("s_waitcnt vmcnt(4)" : "+v"(fw[0]), "+v"(fw[1]), "+v"(fw[2]), "+v"(fw[3]) :: "memory"); \
      f4 acc = {0.f, 0.f, 0.f, 0.f}; \
      _Pragma("unroll") for (int i = 0; i < 4; ++i) acc = mfma(xm[i], fw[i], acc); \
      if (lane < 32) *lp<f4>((scr) + w * 512 + r16 * 32 + (lane & 16)) = acc; \
      BAR(); \
      if (w == 0) { \
        f2 p[8]; \
        _Pragma("unroll") for (int i = 0; i < 8; ++i) p[i] = *lp<f2>((scr) + lane * 8 + i * 512); \
        const f2 s = ((p[0] + p[1]) + (p[2] + p[3])) + ((p[4] + p[5]) + (p[6] + p[7])); \
        stsc(v4u{asu(s.x), tv, asu(s.y), tv}, rs, po, WS_FP + cons * 1024); \
      } \
      SB(); }
#define ST1() asm volatile("buffer_load_dwordx4 %0, %1, %2, %3 offen sc1" : "=&v"(st) : "v"(a_st), "s"(sws), "s"(WS_ST + w * 8192 + h * 16) : "memory")
      WO4(0); S1(102400)
      UV4(); SB(); WO4(1); WB(8); WO4(2); RD_BO(0) WB(8); WO4(3); RD_UV() WB(8); ST1(); RD_BO(1) WB(5); RD_BO(2)
      asm volatile("s_waitcnt vmcnt(0)" : "+v"(st) :: "memory");
    }
    v4u od[4];
    {
      for (int spins = 0; !all_ok((st.y == tv) & (st.w == tv)) && spins < SPIN_CAP; ++spins)
        asm volatile("buffer_load_dwordx4 %0, %1, %2, %3 offen sc1\n\ts_waitcnt vmcnt(0)" : "=&v"(st) : "v"(a_st), "s"(sws), "s"(WS_ST + w * 8192 + h * 16) : "memory");
#pragma unroll
      for (int i = 0; i < 4; ++i) od[i] = __builtin_amdgcn_raw_buffer_load_b128(rs, a_od + i * 256, WS_OP + (cons * 128 + 16 * w) * 256, AUX_SC1 | AUX_NT);
      SCHED();
      const float m = asf(st.x);
      float mw = fmx(m, dpp<0xB1>(m)); mw = fmx(mw, dpp<0x4E>(mw)); mw = max32(max16(mw));
      const float e = ex2(m - mw);
      const unsigned wb = pk(e, e);
      xm[0] = xmask(dppu<0x00>(wb), mk); xm[1] = xmask(dppu<0x55>(wb), mk); xm[2] = xmask(dppu<0xAA>(wb), mk); xm[3] = xmask(dppu<0xFF>(wb), mk);
      float lw = bflo(wb) * asf(st.z);
      lw += dpp<0xB1>(lw); lw += dpp<0x4E>(lw); lw = add32(add16(lw));
      *lp<f2>(a_sw) = f2{mw, lw};
#pragma unroll
      for (int i = 0; i < 4; ++i) PIN(xm[i]);
      SCHED();
    }
    {
      int spins = 0; f4 acc;
      for (;;) {
        unsigned bad = 0;
        acc = f4{0.f, 0.f, 0.f, 0.f};
#pragma unroll
        for (int i = 0; i < 4; ++i) { acc = mfma(xm[i], od[i], acc); bad = umax4(bad, od[i]); }
        if (all_ok(bad != EMPTY) || ++spins >= SPIN_CAP) break;
        FENCE();
#pragma unroll
        for (int i = 0; i < 4; ++i) od[i] = __builtin_amdgcn_raw_buffer_load_b128(rs, a_od + i * 256, WS_OP + (cons * 128 + 16 * w) * 256, AUX_SC1 | AUX_NT);
      }
      *lp<v2u>(a_w1) = v2u{pk(acc[0], acc[1]), pk(acc[2], acc[3])};
    }
    BAR();
    float ri;
    {
      v4u af[4];
      const f4 s0 = *lp<f4>(a_sr), s1 = *lp<f4>(a_sr + 16);
#pragma unroll
      for (int ks = 0; ks < 4; ++ks) af[ks] = lds128(a_r1 + ks * 64);
      RD_BO(3)
      SCHED();
      const float M = max16(fmx(mx3(s0[0], s0[2], s1[0]), s1[2]));
      f4 acc = {0.f, 0.f, 0.f, 0.f};
#pragma unroll
      for (int ks = 0; ks < 4; ++ks) acc = mfma(af[ks], buv[ks], acc);
      const float f0 = ex2(s0[0] - M), f1 = ex2(s0[2] - M), f2_ = ex2(s1[0] - M), f3 = ex2(s1[2] - M);
      const float c = add16((f0 * acc[0] + f1 * acc[1]) + (f2_ * acc[2] + f3 * acc[3]));
      *lp<u16>(a_w2) = (u16)pk(c, c);
      SCHED();
      ri = rcp(add16((f0 * s0[1] + f1 * s0[3]) + (f2_ * s1[1] + f3 * s1[3])));
      PIN(ri);
    }
    BAR();
    {
      v4u cf[4];
#pragma unroll
      for (int ks = 0; ks < 4; ++ks) cf[ks] = lds128(a_r2 + ks * 64);
      SCHED();
      f4 acc[4];
#pragma unroll
      for (int t = 0; t < 4; ++t) acc[t] = f4{0.f, 0.f, 0.f, 0.f};
#pragma unroll
      for (int ks = 0; ks < 4; ++ks)
#pragma unroll
        for (int t = 0; t < 4; ++t) acc[t] = mfma(cf[ks], bo[t][ks], acc[t]);
      const float v01 = (g & 1) ? acc[1][0] : acc[0][0], v23 = (g & 1) ? acc[3][0] : acc[2][0];
      float v = ((g & 2) ? v23 : v01) * ri;
      PIN(v);
      __builtin_amdgcn_raw_buffer_store_b64(v2u{asu(v), tv}, rs, a_o, WS_SP + (4 * w * 128 + cons) * 128, AUX_SC1);
    }
    __builtin_amdgcn_endpgm();
  }
  {
    const v4u sws = mksrd(ws, WS_END);
    const int ht = w & 1, kt = w >> 1;
    v4u k0[16], k1[16];
    v2u v0[4][4];
    COMMON_CONSTS
    const int mi = ai & 3, fi = (ai < 112) ? (ai >> 2) : 0;
    int ka[4], ta[4], qa_[4], vo0 = vrow0, vo1 = vrow1;
#pragma unroll
    for (int s = 0; s < 4; ++s) {
      ka[s] = kt * 32768 + (rbaseK ^ (s * 16)); ta[s] = (w >> 1) * 256 + (trbaseK ^ (32 * (4 * (w & 1) + s))); qa_[s] = ka[s] + (QAI + ht * 16384 - kt * 32768);
      PIN(ka[s]); PIN(ta[s]); PIN(qa_[s]);
    }
    int a_d = (2 * g + (r16 >> 3)) * 1024 + (r16 & 7) * 16, a_mw = (16 * ht + r16) * 16 + kt * 4,
        a_s = (lane < 16) ? (16 * ht + r16) * 16 : WS_END, a_o = (16 * g + (w >> 1)) * 32768 + (w & 1) * 128 + r16 * 8, a_pq = QAI + po,
        a_c = (16 * w + g) * 32768 + r16 * 16;
    int a_mr = a_mw & ~15, a_lw = 2048 + g * 512 + a_mw, a_pw = a_pq + w * 1024;
    PIN(a_mr); PIN(a_lw); PIN(a_pw);
    PIN(a_mw); PIN(a_s); PIN(a_o); PIN(a_pq); PIN(a_c); PIN(vo0); PIN(vo1);
#define RD_U0() _Pragma("unroll") for (int i = 0; i < 4; ++i) _Pragma("unroll") for (int s = 0; s < 4; ++s) { k0[4 * i + s] = lds128(ka[s] + i * 256); v0[i][s] = ldstr(ta[s] + i * 32768); }
#define RD_K1() _Pragma("unroll") for (int s = 0; s < 16; ++s) k1[s] = lds128(ka[s & 3] + (s >> 2) * 256 + 16384)
#define FLAGS_OK() all_ok(((fl.x ^ tv) | (fl.y ^ tv) | (fl.z ^ tv) | (fl.w ^ tv)) == 0)
#define QA4() _Pragma("unroll") for (int q = 0; q < 4; ++q) dma16sc(sws, (q & 1) ? vo1 : vo0, WS_QA + (w + 8 * q) * 1024, QAI + (w + 8 * q) * 1024)
    if (w >= 2) {
      v4u fl;
      if (w == 3) {
        __builtin_amdgcn_s_setprio(3);
      }
      asm volatile("s_barrier" ::: "memory");
      if (w == 3) asm volatile("buffer_load_dwordx4 %0, %1, %2, %3 offen sc1" : "=&v"(fl) : "v"(po), "s"(sws), "s"(WS_FL) : "memory");
      RD_U0()
      if (w == 3) {
        asm volatile("s_waitcnt vmcnt(0)" : "+v"(fl) :: "memory");
        int spins = 0;
        while (!FLAGS_OK() && ++spins < SPIN_CAP)
          asm volatile("buffer_load_dwordx4 %0, %1, %2, %3 offen sc1\n\ts_waitcnt vmcnt(0)" : "=&v"(fl) : "v"(po), "s"(sws), "s"(WS_FL) : "memory");
        __builtin_amdgcn_s_setprio(0);
      }
    }
    asm volatile("s_waitcnt vmcnt(16)\n\ts_barrier" ::: "memory");
    const bool early = (long long)(__builtin_amdgcn_s_memrealtime() - (t_pub + DLY_G)) >= 0;
    if (early) { QA4(); }
    if (w < 2) { RD_U0() }
    if (kt < 2) { RD_K1(); }
    v4u vf[4][4];
#pragma unroll
    for (int i = 0; i < 8; ++i) { const v2u hi = ldstr(ta[i & 3] + (i >> 2) * 32768 + 16384); vf[i >> 2][i & 3] = v4u{v0[i >> 2][i & 3].x, v0[i >> 2][i & 3].y, hi.x, hi.y}; }
    if (!early) { wait_until(t_pub + DLY_G); QA4(); }
    VMWAIT(4);
    BAR();
    if (kt >= 2) { RD_K1(); }
#define RD_V1(i) { const v2u hi = ldstr(ta[(i) & 3] + ((i) >> 2) * 32768 + 16384); vf[(i) >> 2][(i) & 3] = v4u{v0[(i) >> 2][(i) & 3].x, v0[(i) >> 2][(i) & 3].y, hi.x, hi.y}; }
    f4 S0 = {0.f, 0.f, 0.f, 0.f}, S1 = {0.f, 0.f, 0.f, 0.f};
    PIN(S0); PIN(S1);
    for (int spins = 0;; ++spins) {
      VMWAIT(0);
      unsigned bad = 0;
#pragma unroll
      for (int q = 0; q < 4; ++q) bad = umax4(bad, lds128(a_pw + q * 8192));
      if (all_ok(bad != EMPTY) || spins >= SPIN_CAP) break;
      QA4();
    }
    BAR();
    {
      v4u qf[16];
#pragma unroll
      for (int s = 0; s < QD; ++s) qf[s] = lds128(qa_[s & 3] + (s >> 2) * 256);
      SCHED();
#pragma unroll
      for (int s = 0; s < 16; ++s) {
        mfma_acc(S0, k0[s], qf[s]); mfma_acc(S1, k1[s], qf[s]);
        if (s + QD < 16) qf[s + QD] = lds128(qa_[(s + QD) & 3] + ((s + QD) >> 2) * 256);
        if (s >= 8) RD_V1(s)
        if (s < 8 && !(s & 1)) stsc(v4u{EMPTY, EMPTY, EMPTY, EMPTY}, rs, a_c, WS_OP + (s >> 1) * 131072 + ai * 256);
        SCHED();
      }
      float m0, mt;
      asm volatile("s_nop 7\n\tv_max3_f32 %0, %2, %3, %4\n\tv_max3_f32 %0, %0, %5, %6\n\tv_max3_f32 %1, %7, %8, %9\n\tv_max_f32 %0, %0, %1\n\t"
                   "v_mov_b32 %1, %0\n\ts_nop 1\n\tv_permlane16_swap_b32 %0, %1\n\tv_max_f32 %0, %0, %1\n\t"
                   "v_mov_b32 %1, %0\n\ts_nop 1\n\tv_permlane32_swap_b32 %0, %1\n\tv_max_f32 %0, %0, %1"
                   : "=&v"(m0), "=&v"(mt) : "v"(S0[0]), "v"(S0[1]), "v"(S0[2]), "v"(S0[3]), "v"(S1[0]), "v"(S1[1]), "v"(S1[2]), "v"(S1[3]));
      *lp<float>(a_mw) = m0;
    }
    BAR();
    float M;
    {
      const f4 m4 = *lp<f4>(a_mr);
      asm("v_max3_f32 %0, %1, %2, %3\n\tv_max_f32 %0, %0, %4" : "=&v"(M) : "v"(m4[0]), "v"(m4[1]), "v"(m4[2]), "v"(m4[3]));
      float p[8];
#pragma unroll
      for (int i = 0; i < 4; ++i) { p[i] = ex2(S0[i] - M); p[4 + i] = ex2(S1[i] - M); }
      *lp<v4u>(a_pw) = v4u{pk(p[0], p[1]), pk(p[2], p[3]), pk(p[4], p[5]), pk(p[6], p[7])};
      *lp<float>(a_lw) = ((p[0] + p[1]) + (p[2] + p[3])) + ((p[4] + p[5]) + (p[6] + p[7]));
    }
    VMWAIT(0);
    BAR();
    {
      v4u pf_[8];
      pf_[0] = lds128(a_pq);
      const f4 l4 = *lp<f4>(a_lw & ~15);
#pragma unroll
      for (int i = 1; i < 8; ++i) pf_[i] = lds128(a_pq + ((i & 3) * 2 + (i >> 2)) * 1024);
      SCHED();
#pragma unroll
      for (int hh = 0; hh < 2; ++hh) {
        f4 O[4];
#pragma unroll
        for (int t = 0; t < 4; ++t) O[t] = f4{0.f, 0.f, 0.f, 0.f};
#pragma unroll
        for (int ks = 0; ks < 4; ++ks) {
#pragma unroll
          for (int t = 0; t < 4; ++t) O[t] = mfma(pf_[4 * hh + ks], vf[ks][t], O[t]);
          if (hh == 0 && ks == 0) { SCHED(); if (kt == 0) stsc(v4u{asu(M), tv, asu(add32(add16((l4[0] + l4[1]) + (l4[2] + l4[3])))), tv}, rs, a_s, WS_ST + ai * 512); SCHED(); }
        }
        if (hh == 0) SCHED();
        if (hh == 1) { PIN(O[3]); }
#pragma unroll
        for (int i = 0; i < 4; ++i)
          __builtin_amdgcn_raw_buffer_store_b64(v2u{pkc(O[0][i], O[1][i]), pkc(O[2][i], O[3][i])}, rs, a_o, WS_OP + (16 * hh + i) * 131072 + ai * 256, AUX_SC1);
      }
    }
    const u64 t_pv = __builtin_amdgcn_s_memrealtime();
    if (ai < 112) {
      wait_until(t_pv + DLY_U);
      const rsrc_t rd = mkrs(wd, 1835008);
      const int gu = (r16 >> 2) & 1, hi = r16 >> 3;
      const int a_f = (r16 & 3) * 1024 + (64 * gu + 8 * hi + 2 * g) * 8;
      v4u dw[2], p = ldsc(rs, a_f, WS_FP + fi * 4096 + mi * 128);
#pragma unroll
      for (int i = 0; i < 2; ++i) dw[i] = ldnt(rd, a_d, (64 * fi + 16 * mi + 8 * i) * 1024 + w * 128);
      SCHED();
      for (int spins = 0; !all_ok((p.y == tv) & (p.w == tv)) && spins < SPIN_CAP; ++spins) { FENCE(); p = ldsc(rs, a_f, WS_FP + fi * 4096 + mi * 128); }
      float v0_ = asf(p.x), v1_ = asf(p.z);
      v0_ += dpp<0xB1>(v0_); v1_ += dpp<0xB1>(v1_);
      v0_ += dpp<0x4E>(v0_); v1_ += dpp<0x4E>(v1_);
      const float o0 = dpp<0x141>(v0_), o1 = dpp<0x141>(v1_);
      float hv[2];
#pragma unroll
      for (int e = 0; e < 2; ++e) {
        const float gt = rbf(gu ? (e ? o1 : o0) : (e ? v1_ : v0_)), up = rbf(gu ? (e ? v1_ : v0_) : (e ? o1 : o0));
        const float z2 = 2.0f * 0.7978845608028654f * 1.4426950408889634f * (gt + 0.044715f * gt * gt * gt);
        hv[e] = rbf(gt * rcp(1.0f + ex2(-z2))) * up;
      }
      const float t0 = dpp<0x128>(hv[0]), t1 = dpp<0x128>(hv[1]);
      unsigned mk2[4];
#pragma unroll
      for (int k = 0; k < 4; ++k) mk2[k] = mk[k] | dppu<0x128>(mk[k]);
      const float ha = hi ? t1 : hv[0], hb = hi ? hv[1] : t0;
      f4 acc = {0.f, 0.f, 0.f, 0.f};
      acc = mfma(xmask(pk(ha, ha), mk2), dw[0], acc);
      acc = mfma(xmask(pk(hb, hb), mk2), dw[1], acc);
      float y[4];
#pragma unroll
      for (int i = 0; i < 4; ++i) {
        const unsigned u = asu(dpp<0x128>(acc[i]));
        auto r = __builtin_amdgcn_permlane32_swap(u, u, false, false);
        y[i] = acc[i] + asf(r[1]);
      }
      const auto ya = __builtin_amdgcn_permlane32_swap(asu(y[0]), asu(y[2]), false, false), yb = __builtin_amdgcn_permlane32_swap(asu(y[1]), asu(y[3]), false, false);
      if (r16 < 8) stsc(v4u{ya[0], tv, yb[0], tv}, rs, (r16 >> 1) * 16384 + ((r16 & 1) * 8 + 4 * (g & 1) + 2 * (g >> 1)) * 8, WS_YP + (4 * w * 128 + ai) * 128);
    }
    if (w >= 6) {
      const bool isy = w == 7;
      const int off = (lane >> 1) * 128 + (lane & 1) * 16, so = (isy ? WS_YP : WS_SP) + h * 16384 + j * 32;
      const int mo = 16 * h + 4 * j + 2 * (lane & 1) + (lane >> 5);
      int a_e = 1024 + ((lane & 1) * 2 + (lane >> 5)) * 4;
      float xo = bflo(*(const GAS u16*)(x + mo));
      if (!isy && (lane & 30) == 0) *(volatile GAS u16*)(out + mo) = (u16)(asu(xo) >> 16);
      PIN(xo); PIN(a_e);
      {
        const int go = ((ai * 5 + (lane & 3) * 7) & (isy ? 63 : 127)) * 128;
        wait_until(t_pv + DLY_F);
        for (int sp = 0; sp < SPIN_CAP; ++sp) {
          FENCE();
          const v2u t = __builtin_amdgcn_raw_buffer_load_b64(rs, go, so, AUX_SC1);
          if (!all_ok(t.y != tv)) break;
          __builtin_amdgcn_s_sleep(2);
        }
        if (ai == 0 && !isy && lane == 0) *(GAS unsigned*)ws = tag;
      }
      v4u p[4]; int spins = 0; unsigned bad;
      do {
        FENCE();
#pragma unroll
        for (int n = 0; n < 4; ++n) p[n] = ldsc(rs, off, so + n * 4096);
        if (isy && lane >= 32) p[3] = v4u{0u, tv, 0u, tv};
        bad = 0;
#pragma unroll
        for (int n = 0; n < 4; ++n) bad |= (p[n].y ^ tv) | (p[n].w ^ tv);
        PIN(bad);
      } while (!all_ok(bad == 0) && ++spins < SPIN_CAP);
      const float a0 = (asf(p[0].x) + asf(p[1].x)) + (asf(p[2].x) + asf(p[3].x));
      const float a1 = (asf(p[0].z) + asf(p[1].z)) + (asf(p[2].z) + asf(p[3].z));
      const auto hx = __builtin_amdgcn_permlane32_swap(asu(a0), asu(a1), false, false);
      float v = add16(asf(hx[0]) + asf(hx[1]));
      v += dpp<0x128>(v); v += dpp<0x124>(v); v += dpp<0x122>(v);
      v = rbf(v);
      if (isy) *lp<float>(a_e) = rbf(v * rcp(1.0f + ex2(-1.4426950408889634f * v)));
      BAR();
      if (!isy) {
        const unsigned o = pk(xo + rbf(v * *lp<float>(a_e)), 0.f);
        if ((lane & 30) == 0) *(GAS u16*)(out + mo) = (u16)o;
      }
    }
  }
}

extern "C" void kernel_launch(void* const* d_in, const int* in_sizes, int n_in, void* d_out, int out_size, void* d_ws, size_t ws_size, hipStream_t stream) {
  (void)hipFuncSetAttribute((const void*)mla_decode, hipFuncAttributeMaxDynamicSharedMemorySize, LDS_BYTES);
  mla_decode<<<256, 512, LDS_BYTES, stream>>>((const u16*)d_in[0], (const u16*)d_in[2], (unsigned char*)d_ws, (const u16*)d_in[3], (const u16*)d_in[1], (const u16*)d_in[4], (const u16*)d_in[5],
                                              (const u16*)d_in[7], (const u16*)d_in[8], (const u16*)d_in[9], (u16*)d_out, d_in[0]);
}
```

```cpp
#include <hip/hip_runtime.h>
#include <stdint.h>

typedef unsigned short u16;
typedef unsigned long long u64;
typedef unsigned v4u __attribute__((ext_vector_type(4)));
typedef unsigned v2u __attribute__((ext_vector_type(2)));
typedef float f4 __attribute__((ext_vector_type(4)));
typedef float f2 __attribute__((ext_vector_type(2)));
typedef __bf16 bf8 __attribute__((ext_vector_type(8)));
typedef __bf16 bf2 __attribute__((ext_vector_type(2)));
typedef short s4 __attribute__((ext_vector_type(4)));
#define GAS __attribute__((address_space(1)))
#define LAS __attribute__((address_space(3)))

constexpr int DLY_H = 45;
constexpr int DLY_E = 55;
constexpr int DLY_G = 220;
constexpr int DLY_Q = 285;
constexpr int DLY_U = 10;
constexpr int DLY_F = 80;
constexpr int QD = 6;

constexpr int WS_FL = 1024;
constexpr int WS_QP = 4096;
constexpr int WS_QA = WS_QP + 262144;
constexpr int WS_FP = WS_QA + 32768;
constexpr int WS_ST = WS_FP + 131072;
constexpr int WS_SP = WS_ST + 262144;
constexpr int WS_YP = WS_SP + 524288;
constexpr int WS_OP = WS_YP + 524288;
constexpr int WS_END = WS_OP + 4194304;
constexpr int LDS_BYTES = 163840;
constexpr int CODE_BYTES = 13824;
constexpr int SPIN_CAP = 1 << 16;
constexpr int AUX_SC1 = 16, AUX_NT = 2;
constexpr int QAI = 131072;
constexpr int XNI = 65536;
constexpr unsigned EMPTY = 0xFFFFFFFFu;

#define BAR() asm volatile("s_waitcnt lgkmcnt(0)\n\ts_barrier" ::: "memory")
#define FENCE() asm volatile("" ::: "memory")
#define SCHED() __builtin_amdgcn_sched_barrier(0)
#define VMWAIT(n) asm volatile("s_waitcnt vmcnt(" #n ")" ::: "memory")
#define PIN(x) asm volatile("" : "+v"(x))

typedef __amdgpu_buffer_rsrc_t rsrc_t;
__device__ __forceinline__ float bflo(unsigned u) { return __uint_as_float(u << 16); }
__device__ __forceinline__ float bfhi(unsigned u) { return __uint_as_float(u & 0xFFFF0000u); }
__device__ __forceinline__ unsigned pk(float a, float b) { f2 f = {a, b}; return __builtin_bit_cast(unsigned, __builtin_convertvector(f, bf2)); }
__device__ __forceinline__ unsigned pkc(float a, float b) { return min(pk(a, b), EMPTY - 1u); }
__device__ __forceinline__ float rbf(float a) { return bflo(pk(a, a)); }
__device__ __forceinline__ float asf(unsigned u) { return __uint_as_float(u); }
__device__ __forceinline__ unsigned asu(float f) { return __float_as_uint(f); }
__device__ __forceinline__ float ex2(float x) { return __builtin_amdgcn_exp2f(x); }
__device__ __forceinline__ float rcp(float x) { return __builtin_amdgcn_rcpf(x); }
__device__ __forceinline__ f4 mfma(v4u a, v4u b, f4 c) {
  return __builtin_amdgcn_mfma_f32_16x16x32_bf16(__builtin_bit_cast(bf8, a), __builtin_bit_cast(bf8, b), c, 0, 0, 0);
}
__device__ __forceinline__ void mfma_acc(f4& c, v4u a, v4u b) { asm volatile("v_mfma_f32_16x16x32_bf16 %0, %1, %2, %0" : "+v"(c) : "v"(a), "v"(b)); }
template <int CTRL> __device__ __forceinline__ float dpp(float v) {
  return __builtin_bit_cast(float, __builtin_amdgcn_update_dpp(0, __builtin_bit_cast(int, v), CTRL, 0xf, 0xf, true));
}
template <int CTRL> __device__ __forceinline__ unsigned dppu(unsigned v) { return (unsigned)__builtin_amdgcn_update_dpp(0, (int)v, CTRL, 0xf, 0xf, true); }
__device__ __forceinline__ float add16(float v) { unsigned u = asu(v); auto r = __builtin_amdgcn_permlane16_swap(u, u, false, false); return asf(r[0]) + asf(r[1]); }
__device__ __forceinline__ float add32(float v) { unsigned u = asu(v); auto r = __builtin_amdgcn_permlane32_swap(u, u, false, false); return asf(r[0]) + asf(r[1]); }
__device__ __forceinline__ float fmx(float a, float b) { return __builtin_fmaxf(a, b); }
__device__ __forceinline__ float mx3(float a, float b, float c) { return fmx(fmx(a, b), c); }
__device__ __forceinline__ float max16(float v) { unsigned u = asu(v); auto r = __builtin_amdgcn_permlane16_swap(u, u, false, false); return fmx(asf(r[0]), asf(r[1])); }
__device__ __forceinline__ float max32(float v) { unsigned u = asu(v); auto r = __builtin_amdgcn_permlane32_swap(u, u, false, false); return fmx(asf(r[0]), asf(r[1])); }
__device__ __forceinline__ float wave_sum(float v) {
  v += dpp<0x128>(v); v += dpp<0x124>(v); v += dpp<0x122>(v); v += dpp<0x121>(v);
  return add32(add16(v));
}
__device__ __forceinline__ unsigned umax4(unsigned acc, v4u v) { return max(max(max(acc, v.x), v.y), max(v.z, v.w)); }
__device__ __forceinline__ rsrc_t mkrs(const void* p, int bytes) { return __builtin_amdgcn_make_buffer_rsrc((void*)p, 0, bytes, 0x00020000); }
__device__ __forceinline__ v4u mksrd(const void* p, unsigned bytes) { const u64 a = (u64)p; return v4u{(unsigned)a, (unsigned)(a >> 32) & 0xFFFFu, bytes, 0x00020000u}; }
__device__ __forceinline__ v4u ldnt(rsrc_t r, int voff, int soff) { return __builtin_amdgcn_raw_buffer_load_b128(r, voff, soff, AUX_NT); }
__device__ __forceinline__ v4u ldsc(rsrc_t r, int voff, int soff) { return __builtin_amdgcn_raw_buffer_load_b128(r, voff, soff, AUX_SC1); }
__device__ __forceinline__ void stsc(v4u v, rsrc_t r, int voff, int soff) { __builtin_amdgcn_raw_buffer_store_b128(v, r, voff, soff, AUX_SC1); }
__device__ __forceinline__ void dma16(v4u srd, int voff, int soff, int lds_dst) {
  asm volatile("s_mov_b32 m0, %3\n\ts_nop 0\n\tbuffer_load_dwordx4 %0, %1, %2 offen nt lds" :: "v"(voff), "s"(srd), "s"(soff), "s"(lds_dst) : "memory");
}
__device__ __forceinline__ void dma16p(v4u srd, int voff, int soff, int lds_dst) {
  asm volatile("s_mov_b32 m0, %3\n\ts_nop 0\n\tbuffer_load_dwordx4 %0, %1, %2 offen lds" :: "v"(voff), "s"(srd), "s"(soff), "s"(lds_dst) : "memory");
}
__device__ __forceinline__ void dma16sc(v4u srd, int voff, int soff, int lds_dst) {
  asm volatile("s_mov_b32 m0, %3\n\ts_nop 0\n\tbuffer_load_dwordx4 %0, %1, %2 offen sc1 lds" :: "v"(voff), "s"(srd), "s"(soff), "s"(lds_dst) : "memory");
}
template <typename T> __device__ __forceinline__ LAS T* lp(int off) { return (LAS T*)(uintptr_t)(unsigned)off; }
__device__ __forceinline__ v4u lds128(int off) { return *lp<v4u>(off); }
__device__ __forceinline__ v2u ldstr(int off) { return __builtin_bit_cast(v2u, __builtin_amdgcn_ds_read_tr16_b64_v4i16(lp<s4>(off))); }
__device__ __forceinline__ int swz(int r) { return ((r & 7) << 1) | ((r >> 3) & 1); }
__device__ __forceinline__ v4u xmask(unsigned xx, const unsigned (&mk)[4]) { return v4u{xx & mk[0], xx & mk[1], xx & mk[2], xx & mk[3]}; }
__device__ __forceinline__ bool all_ok(bool ok) { return __builtin_amdgcn_ballot_w64(!ok) == 0ull; }
__device__ __forceinline__ void wait_until(u64 t) { while ((long long)(__builtin_amdgcn_s_memrealtime() - t) < 0) __builtin_amdgcn_s_sleep(1); }

__device__ __forceinline__ void ln_stats(v4u xa, float& mb, float& ri) {
  float v[8] = {bflo(xa.x), bfhi(xa.x), bflo(xa.y), bfhi(xa.y), bflo(xa.z), bfhi(xa.z), bflo(xa.w), bfhi(xa.w)};
  const float mean = wave_sum(((v[0] + v[1]) + (v[2] + v[3])) + ((v[4] + v[5]) + (v[6] + v[7]))) * (1.0f / 512.0f);
  float q0 = 0.f, q1 = 0.f;
#pragma unroll
  for (int i = 0; i < 4; ++i) { const float d = v[i] - mean, e = v[i + 4] - mean; q0 += d * d; q1 += e * e; }
  const float var = wave_sum(q0 + q1) * (1.0f / 512.0f);
  mb = rbf(mean);
  ri = rcp(rbf(__builtin_amdgcn_sqrtf(rbf(rbf(var) + 1e-5f))));
}

typedef u64 v8q __attribute__((ext_vector_type(8)));
typedef u64 v4q __attribute__((ext_vector_type(4)));
extern "C" __global__ void __launch_bounds__(512, 2) mla_decode(const void*, const void*, void*, const void*, const void*, const void*, const void*, const void*,
                                                      const void*, const void*, void*, const void*) {
  v8q ka0; v4q ka1;
  asm volatile("s_load_dwordx16 %0, %2, 0x0\n\ts_load_dwordx8 %1, %2, 0x40" : "=&s"(ka0), "=&s"(ka1) : "s"(__builtin_amdgcn_kernarg_segment_ptr()));
  const int b = blockIdx.x, tid = threadIdx.x, lane = tid & 63, w = __builtin_amdgcn_readfirstlane(tid >> 6);
  const int xcd = b & 7;
  const bool isA = (b & 1) == 0;
  const int r16 = lane & 15, g = lane >> 4;
  const int h = (xcd >> 1) * 8 + ((b >> 3) & 7), j = b >> 6;
  const int ai = b >> 1;
  {
    u64 pc; asm volatile("s_getpc_b64 %0" : "=s"(pc));
    asm volatile("s_mov_b32 m0, %2\n\ts_nop 0\n\tglobal_load_lds_dword %0, %1" :: "v"(min(((b >> 3) * 8 + w) * 128, CODE_BYTES - 128) + r16 * 8), "s"(pc & ~255ull), "s"(114688) : "memory");
  }
#define COMMON_CONSTS \
  const int dchunk = (r16 ^ swz(4 * (w & 3) + g)) * 16;     \
  const int trrow = 4 * g + (r16 >> 2); \
  const int trbase = trrow * 256 + ((((r16 >> 1) & 1) ^ swz(trrow)) * 16) + 8 * (r16 & 1);     \
  const int rbase = r16 * 256 + ((4 * g) ^ swz(r16)) * 16;                                      \
  const int trbaseK = trbase + trrow * 768, rbaseK = rbase + r16 * 768;                         \
  const int vrow0 = g * 256 + (r16 ^ (2 * w)) * 16, vrow1 = vrow0 ^ 16;

  const int cons = h * 4 + j;
  const int jj = 2 * j + (b & 1);
  v4u wqv[4], xa; v2u xb;
  int oW = (64 * jj + 16 * (w & 3) + 4 * g) * 8192 + h * 256 + r16 * 16, oW1 = oW + 8192, oW2 = oW + 16384, oW3 = oW + 24576,
      ox = (w == 2) ? lane * 16 : (w == 3) ? WS_QA + cons * 256 + lane * 4 : (64 * jj + 16 * (w & 3) + 4 * g) * 2;
  unsigned em = EMPTY;
  PIN(oW); PIN(oW1); PIN(oW2); PIN(oW3); PIN(ox); PIN(em);
  SCHED();
  asm volatile("s_waitcnt lgkmcnt(0)" : "+s"(ka0), "+s"(ka1));
  const u16 *x = (const u16*)ka0[0], *wq = (const u16*)ka0[1], *wuk = (const u16*)ka0[3], *ckv = (const u16*)ka0[4], *wuv = (const u16*)ka0[5], *wo = (const u16*)ka0[6],
            *wg = (const u16*)ka0[7], *wu = (const u16*)ka1[0], *wd = (const u16*)ka1[1];
  unsigned char* ws = (unsigned char*)ka0[2];
  u16* out = (u16*)ka1[2];
  if (w >= 4) {
    asm volatile("global_load_dwordx2 %4, %5, %6\n\tglobal_load_dwordx4 %0, %7, %11 nt\n\tglobal_load_dwordx4 %1, %8, %11 nt\n\tglobal_load_dwordx4 %2, %9, %11 nt\n\tglobal_load_dwordx4 %3, %10, %11 nt"
                 : "=&v"(wqv[0]), "=&v"(wqv[1]), "=&v"(wqv[2]), "=&v"(wqv[3]), "=&v"(xb) : "v"(ox), "s"(x), "v"(oW), "v"(oW1), "v"(oW2), "v"(oW3), "s"(wq) : "memory");
  }
  if (w == 2) asm volatile("global_load_dwordx4 %0, %1, %2" : "=&v"(xa) : "v"(ox), "s"(x) : "memory");
  if (w == 3 && !isA) asm volatile("global_store_dword %0, %1, %2 sc1" :: "v"(ox), "v"(em), "s"(ws) : "memory");
  unsigned tag;
  asm volatile("s_load_dword %0, %1, 0x0\n\ts_barrier" : "=&s"(tag) : "s"(ws) : "memory");
  int po = lane * 16;
  u64 t_pub = 0;
  if (w < 2) {
    if (isA) {
      const v4u skv = mksrd(ckv, 16777216);
      const int so = (128 * ai + w) * 1024, vb = po ^ (w * 32);
#pragma unroll 1
      for (int i = 0; i < 8; ++i) {
        const int blk = ((2 * i) & 7) + (i >> 2);
        if (i == 1) { asm volatile("s_barrier\n\ts_barrier" ::: "memory"); t_pub = __builtin_amdgcn_s_memrealtime(); }
        if (i == 6) asm volatile("s_waitcnt vmcnt(16)\n\ts_barrier" ::: "memory");
#pragma unroll
        for (int r = 0; r < 8; ++r) dma16(skv, vb ^ ((r & 3) * 64 + (r >> 2) * 16), so + blk * 16384 + r * 2048, w * 1024 + blk * 16384 + r * 2048);
      }
    } else {
      const v4u sk = mksrd(wuk, 4194304);
      const int vk = g * 8192 + (r16 ^ (2 * g)) * 16;
#pragma unroll 1
      for (int i = 0; i < 4; ++i) {
        if (i == 3) { asm volatile("s_barrier\n\ts_barrier" ::: "memory"); t_pub = __builtin_amdgcn_s_memrealtime(); }
#pragma unroll
        for (int n = 0; n < 4; ++n) dma16(sk, vk ^ ((n & 1) * 128 + (n >> 1) * 16), (128 * j + 64 * w + 16 * i + 4 * n) * 8192 + h * 256, QAI + (16 * w + 4 * i + n) * 1024);
      }
    }
  }
  SCHED();
  const rsrc_t rs = mkrs(ws, WS_END);
  unsigned mk[4];
#pragma unroll
  for (int k = 0; k < 4; ++k) { mk[k] = ((r16 >> 1) == k) ? ((r16 & 1) ? 0xFFFF0000u : 0x0000FFFFu) : 0u; PIN(mk[k]); }
  if (w >= 4) {
    unsigned mk2[4];
    v4u xm[4];
#pragma unroll
    for (int k = 0; k < 4; ++k) { mk2[k] = mk[k] | dppu<0x128>(mk[k]); PIN(mk2[k]); }
    int a_w0 = 116736 + (w - 4) * 1024 + (g >> 1) * 512 + r16 * 32 + (lane & 16);
    PIN(a_w0);
    asm volatile("s_waitcnt vmcnt(4)" : "+v"(xb) :: "memory");
#pragma unroll
    for (int k = 0; k < 2; ++k) {
      const unsigned xp = (r16 >= 8) ? 0x3F803F80u : xb[k];
      xm[2 * k] = xmask(__builtin_amdgcn_perm(xp, xp, 0x01000100u), mk2); xm[2 * k + 1] = xmask(__builtin_amdgcn_perm(xp, xp, 0x03020302u), mk2);
      PIN(xm[2 * k]); PIN(xm[2 * k + 1]);
    }
    SCHED();
    f4 acc = {0.f, 0.f, 0.f, 0.f};
    asm volatile("s_waitcnt vmcnt(0)" : "+v"(wqv[0]), "+v"(wqv[1]), "+v"(wqv[2]), "+v"(wqv[3]) :: "memory");
#pragma unroll
    for (int i = 0; i < 4; ++i) acc = mfma(xm[i], wqv[i], acc);
    *lp<f4>(a_w0) = acc;
    BAR();
    asm volatile("s_barrier" ::: "memory");
  }
  float mb = 0.f, ri = 0.f;
  if (w == 2) {
    __builtin_amdgcn_s_setprio(3);
    asm volatile("s_waitcnt vmcnt(0)" : "+v"(xa) :: "memory");
    ln_stats(xa, mb, ri);
    PIN(mb); PIN(ri);
  }
  asm volatile("s_waitcnt lgkmcnt(0)" : "+s"(tag));
  tag += 1u;
  unsigned tv = tag;
  PIN(tv);
  if (w == 2) {
    int a_qp = ((lane & 31) * 2 + (lane >> 5)) * 16, a_r0 = 116736 + po;
    PIN(a_qp); PIN(a_r0);
    BAR();
    f4 p[4];
#pragma unroll
    for (int i = 0; i < 4; ++i) p[i] = *lp<f4>(a_r0 + i * 1024);
    const f4 s = (p[0] + p[1]) + (p[2] + p[3]);
    const auto r0 = __builtin_amdgcn_permlane32_swap(asu(s[0]), asu(s[2]), false, false), r1 = __builtin_amdgcn_permlane32_swap(asu(s[1]), asu(s[3]), false, false);
    stsc(v4u{asu((asf(r0[0]) - mb * asf(r0[1])) * ri), tv, asu((asf(r1[0]) - mb * asf(r1[1])) * ri), tv}, rs, a_qp, WS_QP + (h * 8 + jj) * 1024);
    asm volatile("s_barrier" ::: "memory");
    __builtin_amdgcn_s_setprio(0);
    if (!isA) {
      v4u xn;
#pragma unroll
      for (int i = 0; i < 4; ++i) { const unsigned d = pk(bflo(xa[i]) - mb, bfhi(xa[i]) - mb); xn[i] = pk(bflo(d) * ri, bfhi(d) * ri); }
      *lp<v4u>(XNI + po) = xn;
    }
  }
  if (w == 3) {
    VMWAIT(0);
    if (!isA && lane == 0) __builtin_amdgcn_raw_buffer_store_b64(v2u{tv, tv}, rs, 0, WS_FL + cons * 8, AUX_SC1);
    asm volatile("s_barrier\n\ts_barrier" ::: "memory");
  }
  if (w >= 2) t_pub = __builtin_amdgcn_s_memrealtime();
  if (!isA) {
    const int xrow0 = 128 * j + 16 * w;
    COMMON_CONSTS
    int a_wk = QAI + w * 4096 + rbase, a_qw = 73728 + lane * 4, a_qr = 73728 + g * 64, a_qa = 74240 + w * 32 + g * 8;
    int a_wk4[4];
#pragma unroll
    for (int s = 0; s < 4; ++s) { a_wk4[s] = a_wk ^ (s * 16); PIN(a_wk4[s]); }
    PIN(a_qw); PIN(a_qr); PIN(a_qa);
    v4u fw[4];
    const GAS char* pf = (const GAS char*)((r16 < 8) ? wg : wu) + (h < 28 ? (xrow0 + 4 * g + 1) * 3584 + h * 128 : 3584) + (r16 & 7) * 16;
#define GU4() asm volatile("global_load_dwordx4 %0, %4, off offset:-3584 nt\n\tglobal_load_dwordx4 %1, %4, off nt\n\tglobal_load_dwordx4 %2, %4, off offset:3584 nt\n\tglobal_load_dwordx4 %3, %5, off nt" \
                   : "=&v"(fw[0]), "=&v"(fw[1]), "=&v"(fw[2]), "=&v"(fw[3]) : "v"(pf), "v"(pf + 7168) : "memory")
    if (w == 4) {
      v4u p[8]; int spins = 0; unsigned bad;
      wait_until(t_pub + DLY_H);
      for (;;) {
        FENCE();
#pragma unroll
        for (int i = 0; i < 8; ++i) p[i] = ldsc(rs, po + (i & 3) * 1024, WS_QP + h * 8192 + (i >> 2) * 4096);
        bad = 0;
#pragma unroll
        for (int i = 0; i < 8; ++i) bad |= (p[i].y ^ tv) | (p[i].w ^ tv);
        PIN(bad);
        if (all_ok(bad == 0) || ++spins >= SPIN_CAP) break;
        __builtin_amdgcn_s_sleep(1);
      }
      const float q0 = ((asf(p[0].x) + asf(p[1].x)) + (asf(p[2].x) + asf(p[3].x))) + ((asf(p[4].x) + asf(p[5].x)) + (asf(p[6].x) + asf(p[7].x)));
      const float q1 = ((asf(p[0].z) + asf(p[1].z)) + (asf(p[2].z) + asf(p[3].z))) + ((asf(p[4].z) + asf(p[5].z)) + (asf(p[6].z) + asf(p[7].z)));
      *lp<unsigned>(a_qw) = pk(q0, q1);
    }
    const bool gu_early = (0xEC >> w) & 1;
    if (gu_early) { wait_until(t_pub + DLY_E); GU4(); }
    if (w < 2) { VMWAIT(0); }
    BAR();
    v4u xm[4];
    {
      v4u qf[4], wkv[4];
#pragma unroll
      for (int s = 0; s < 4; ++s) { wkv[s] = lds128(a_wk4[s]); qf[s] = lds128(a_qr + s * 16); }
      SCHED();
      f4 acc = {0.f, 0.f, 0.f, 0.f};
#pragma unroll
      for (int s = 0; s < 4; ++s) acc = mfma(wkv[s], qf[s], acc);
      const float sc = 0.08838834764831845f * 1.4426950408889634f;
      if (r16 == 0) *lp<v2u>(a_qa) = v2u{pkc(acc[0] * sc, acc[1] * sc), pkc(acc[2] * sc, acc[3] * sc)};
    }
    BAR();
    if (w == 0 && lane < 16) stsc(lds128(74240 + po), rs, po, WS_QA + cons * 256);
    {
      const v2u x4 = *lp<v2u>(XNI + (xrow0 + 4 * g) * 2);
      xm[0] = xmask(__builtin_amdgcn_perm(x4.x, x4.x, 0x01000100u), mk); xm[1] = xmask(__builtin_amdgcn_perm(x4.x, x4.x, 0x03020302u), mk);
      xm[2] = xmask(__builtin_amdgcn_perm(x4.y, x4.y, 0x01000100u), mk); xm[3] = xmask(__builtin_amdgcn_perm(x4.y, x4.y, 0x03020302u), mk);
#pragma unroll
      for (int i = 0; i < 4; ++i) PIN(xm[i]);
    }
    BAR();
    if (!gu_early) GU4();
    wait_until(t_pub + DLY_Q);
    v4u buv[4], bo[4][4], st;
    int tq[4];
#pragma unroll
    for (int t = 0; t < 4; ++t) tq[t] = (w >> 1) * 256 + (trbaseK ^ (32 * (4 * (w & 1) + t)));
    const v4u sws = mksrd(ws, WS_END);
    int a_st = (4 * g + (r16 & 3)) * 512, a_od = g * 1024 + r16 * 16, a_w1 = (g >> 1) * 12288 + w * 272 + r16 * 16 + (g & 1) * 8, a_r1 = (r16 & 7) * 272 + g * 16,
        a_w2 = 8192 + ((16 * w + r16 - 32 * j) & 127) * 2, a_r2 = 8192 + g * 16, a_o = g * 16384 + r16 * 8, a_sw = 4096 + w * 8, a_sr = 4096 + (g & 1) * 32;
    PIN(a_st); PIN(a_od); PIN(a_w1); PIN(a_r1); PIN(a_w2); PIN(a_r2); PIN(a_o); PIN(a_sw); PIN(a_sr);
    {
      const v4u suv = mksrd(wuv, 4194304), so = mksrd(wo, 4194304);
      const int so2 = (h * 128 + 8 * (w >> 2) + (w & 3)) * 1024;
#define WO4(n) _Pragma("unroll") for (int i = 0; i < 4; ++i) dma16p(so, (i & 1) ? vrow1 : vrow0, so2 + ((((n) + j) & 3) * 32 + 16 * (i & 1) + 4 * (i >> 1)) * 1024, (32 * (n) + w + 8 * i) * 1024)
#define RD_UV() _Pragma("unroll") for (int ks = 0; ks < 4; ++ks) { const int a = QAI + (trbase ^ (32 * w)) + ks * 8192; const v2u lo = ldstr(a), hi = ldstr(a + 4096); buv[ks] = v4u{lo.x, lo.y, hi.x, hi.y}; }
#define RD_BO(n) _Pragma("unroll") for (int t = 0; t < 4; ++t) { const v2u lo = ldstr(tq[t] + (n) * 32768), hi = ldstr(tq[t] + (n) * 32768 + 16384); bo[t][n] = v4u{lo.x, lo.y, hi.x, hi.y}; }
      const int vo1 = g * 16 * 8192 + dchunk, so1 = (128 * j + 2 * (w & 3) + (w >> 2)) * 8192 + h * 256;
#define SB() asm volatile("s_barrier" ::: "memory")
#define WB(n) asm volatile("s_waitcnt vmcnt(" #n ") lgkmcnt(0)\n\ts_barrier" ::: "memory")
#define UV4() _Pragma("unroll 1") for (int n = 0; n < 4; ++n) dma16(suv, vo1, so1 + (64 * (n >> 1) + 8 * (n & 1)) * 8192, QAI + (32 * n + 4 * w) * 256)
#define S1(scr) { \
      asm volatile("s_waitcnt vmcnt(4)" : "+v"(fw[0]), "+v"(fw[1]), "+v"(fw[2]), "+v"(fw[3]) :: "memory"); \
      f4 acc = {0.f, 0.f, 0.f, 0.f}; \
      _Pragma("unroll") for (int i = 0; i < 4; ++i) acc = mfma(xm[i], fw[i], acc); \
      if (lane < 32) *lp<f4>((scr) + w * 512 + r16 * 32 + (lane & 16)) = acc; \
      BAR(); \
      if (w == 0) { \
        f2 p[8]; \
        _Pragma("unroll") for (int i = 0; i < 8; ++i) p[i] = *lp<f2>((scr) + lane * 8 + i * 512); \
        const f2 s = ((p[0] + p[1]) + (p[2] + p[3])) + ((p[4] + p[5]) + (p[6] + p[7])); \
        stsc(v4u{asu(s.x), tv, asu(s.y), tv}, rs, po, WS_FP + cons * 1024); \
      } \
      SB(); }
#define ST1() asm volatile("buffer_load_dwordx4 %0, %1, %2, %3 offen sc1" : "=&v"(st) : "v"(a_st), "s"(sws), "s"(WS_ST + w * 8192 + h * 16) : "memory")
      WO4(0); S1(102400)
      UV4(); SB(); WO4(1); WB(8); WO4(2); RD_BO(0) WB(8); WO4(3); RD_UV() WB(8); ST1(); RD_BO(1) WB(5); RD_BO(2)
      asm volatile("s_waitcnt vmcnt(0)" : "+v"(st) :: "memory");
    }
    v4u od[4];
    {
      for (int spins = 0; !all_ok((st.y == tv) & (st.w == tv)) && spins < SPIN_CAP; ++spins)
        asm volatile("buffer_load_dwordx4 %0, %1, %2, %3 offen sc1\n\ts_waitcnt vmcnt(0)" : "=&v"(st) : "v"(a_st), "s"(sws), "s"(WS_ST + w * 8192 + h * 16) : "memory");
#pragma unroll
      for (int i = 0; i < 4; ++i) od[i] = __builtin_amdgcn_raw_buffer_load_b128(rs, a_od + i * 256, WS_OP + (cons * 128 + 16 * w) * 256, AUX_SC1 | AUX_NT);
      SCHED();
      const float m = asf(st.x);
      float mw = fmx(m, dpp<0xB1>(m)); mw = fmx(mw, dpp<0x4E>(mw)); mw = max32(max16(mw));
      const float e = ex2(m - mw);
      const unsigned wb = pk(e, e);
      xm[0] = xmask(dppu<0x00>(wb), mk); xm[1] = xmask(dppu<0x55>(wb), mk); xm[2] = xmask(dppu<0xAA>(wb), mk); xm[3] = xmask(dppu<0xFF>(wb), mk);
      float lw = bflo(wb) * asf(st.z);
      lw += dpp<0xB1>(lw); lw += dpp<0x4E>(lw); lw = add32(add16(lw));
      *lp<f2>(a_sw) = f2{mw, lw};
#pragma unroll
      for (int i = 0; i < 4; ++i) PIN(xm[i]);
      SCHED();
    }
    {
      int spins = 0; f4 acc;
      for (;;) {
        unsigned bad = 0;
        acc = f4{0.f, 0.f, 0.f, 0.f};
#pragma unroll
        for (int i = 0; i < 4; ++i) { acc = mfma(xm[i], od[i], acc); bad = umax4(bad, od[i]); }
        if (all_ok(bad != EMPTY) || ++spins >= SPIN_CAP) break;
        FENCE();
#pragma unroll
        for (int i = 0; i < 4; ++i) od[i] = __builtin_amdgcn_raw_buffer_load_b128(rs, a_od + i * 256, WS_OP + (cons * 128 + 16 * w) * 256, AUX_SC1 | AUX_NT);
      }
      *lp<v2u>(a_w1) = v2u{pk(acc[0], acc[1]), pk(acc[2], acc[3])};
    }
    BAR();
    float ri;
    {
      v4u af[4];
      const f4 s0 = *lp<f4>(a_sr), s1 = *lp<f4>(a_sr + 16);
#pragma unroll
      for (int ks = 0; ks < 4; ++ks) af[ks] = lds128(a_r1 + ks * 64);
      RD_BO(3)
      SCHED();
      const float M = max16(fmx(mx3(s0[0], s0[2], s1[0]), s1[2]));
      f4 acc = {0.f, 0.f, 0.f, 0.f};
#pragma unroll
      for (int ks = 0; ks < 4; ++ks) acc = mfma(af[ks], buv[ks], acc);
      const float f0 = ex2(s0[0] - M), f1 = ex2(s0[2] - M), f2_ = ex2(s1[0] - M), f3 = ex2(s1[2] - M);
      const float c = add16((f0 * acc[0] + f1 * acc[1]) + (f2_ * acc[2] + f3 * acc[3]));
      *lp<u16>(a_w2) = (u16)pk(c, c);
      SCHED();
      ri = rcp(add16((f0 * s0[1] + f1 * s0[3]) + (f2_ * s1[1] + f3 * s1[3])));
      PIN(ri);
    }
    BAR();
    {
      v4u cf[4];
#pragma unroll
      for (int ks = 0; ks < 4; ++ks) cf[ks] = lds128(a_r2 + ks * 64);
      SCHED();
      f4 acc[4];
#pragma unroll
      for (int t = 0; t < 4; ++t) acc[t] = f4{0.f, 0.f, 0.f, 0.f};
#pragma unroll
      for (int ks = 0; ks < 4; ++ks)
#pragma unroll
        for (int t = 0; t < 4; ++t) acc[t] = mfma(cf[ks], bo[t][ks], acc[t]);
      const float v01 = (g & 1) ? acc[1][0] : acc[0][0], v23 = (g & 1) ? acc[3][0] : acc[2][0];
      float v = ((g & 2) ? v23 : v01) * ri;
      PIN(v);
      __builtin_amdgcn_raw_buffer_store_b64(v2u{asu(v), tv}, rs, a_o, WS_SP + (4 * w * 128 + cons) * 128, AUX_SC1);
    }
    if (b == 1 && tid == 0) __builtin_amdgcn_raw_buffer_store_b32(tv, rs, 0, 0, AUX_SC1);
    __builtin_amdgcn_endpgm();
  }
  {
    const v4u sws = mksrd(ws, WS_END);
    const int ht = w & 1, kt = w >> 1;
    v4u k0[16], k1[16];
    v2u v0[4][4];
    COMMON_CONSTS
    const int mi = ai & 3, fi = (ai < 112) ? (ai >> 2) : 0;
    int ka[4], ta[4], qa_[4], vo0 = vrow0, vo1 = vrow1;
#pragma unroll
    for (int s = 0; s < 4; ++s) {
      ka[s] = kt * 32768 + (rbaseK ^ (s * 16)); ta[s] = (w >> 1) * 256 + (trbaseK ^ (32 * (4 * (w & 1) + s))); qa_[s] = ka[s] + (QAI + ht * 16384 - kt * 32768);
      PIN(ka[s]); PIN(ta[s]); PIN(qa_[s]);
    }
    int a_d = (2 * g + (r16 >> 3)) * 1024 + (r16 & 7) * 16, a_mw = (16 * ht + r16) * 16 + kt * 4,
        a_s = (lane < 16) ? (16 * ht + r16) * 16 : WS_END, a_o = (16 * g + (w >> 1)) * 32768 + (w & 1) * 128 + r16 * 8, a_pq = QAI + po,
        a_c = (16 * w + g) * 32768 + r16 * 16;
    int a_mr = a_mw & ~15, a_lw = 2048 + g * 512 + a_mw, a_pw = a_pq + w * 1024;
    PIN(a_mr); PIN(a_lw); PIN(a_pw);
    PIN(a_mw); PIN(a_s); PIN(a_o); PIN(a_pq); PIN(a_c); PIN(vo0); PIN(vo1);
#define RD_U0() _Pragma("unroll") for (int i = 0; i < 4; ++i) _Pragma("unroll") for (int s = 0; s < 4; ++s) { k0[4 * i + s] = lds128(ka[s] + i * 256); v0[i][s] = ldstr(ta[s] + i * 32768); }
#define RD_K1() _Pragma("unroll") for (int s = 0; s < 16; ++s) k1[s] = lds128(ka[s & 3] + (s >> 2) * 256 + 16384)
#define FLAGS_OK() all_ok(((fl.x ^ tv) | (fl.y ^ tv) | (fl.z ^ tv) | (fl.w ^ tv)) == 0)
#define QA4() _Pragma("unroll") for (int q = 0; q < 4; ++q) dma16sc(sws, (q & 1) ? vo1 : vo0, WS_QA + (w + 8 * q) * 1024, QAI + (w + 8 * q) * 1024)
    if (w >= 2) {
      v4u fl;
      if (w == 3) {
        __builtin_amdgcn_s_setprio(3);
      }
      asm volatile("s_barrier" ::: "memory");
      if (w == 3) asm volatile("buffer_load_dwordx4 %0, %1, %2, %3 offen sc1" : "=&v"(fl) : "v"(po), "s"(sws), "s"(WS_FL) : "memory");
      RD_U0()
      if (w == 3) {
        asm volatile("s_waitcnt vmcnt(0)" : "+v"(fl) :: "memory");
        int spins = 0;
        while (!FLAGS_OK() && ++spins < SPIN_CAP)
          asm volatile("buffer_load_dwordx4 %0, %1, %2, %3 offen sc1\n\ts_waitcnt vmcnt(0)" : "=&v"(fl) : "v"(po), "s"(sws), "s"(WS_FL) : "memory");
        __builtin_amdgcn_s_setprio(0);
      }
    }
    asm volatile("s_waitcnt vmcnt(16)\n\ts_barrier" ::: "memory");
    const bool early = (long long)(__builtin_amdgcn_s_memrealtime() - (t_pub + DLY_G)) >= 0;
    if (early) { QA4(); }
    if (w < 2) { RD_U0() }
    if (kt < 2) { RD_K1(); }
    v4u vf[4][4];
#pragma unroll
    for (int i = 0; i < 8; ++i) { const v2u hi = ldstr(ta[i & 3] + (i >> 2) * 32768 + 16384); vf[i >> 2][i & 3] = v4u{v0[i >> 2][i & 3].x, v0[i >> 2][i & 3].y, hi.x, hi.y}; }
    if (!early) { wait_until(t_pub + DLY_G); QA4(); }
    VMWAIT(4);
    BAR();
    if (kt >= 2) { RD_K1(); }
#define RD_V1(i) { const v2u hi = ldstr(ta[(i) & 3] + ((i) >> 2) * 32768 + 16384); vf[(i) >> 2][(i) & 3] = v4u{v0[(i) >> 2][(i) & 3].x, v0[(i) >> 2][(i) & 3].y, hi.x, hi.y}; }
    f4 S0 = {0.f, 0.f, 0.f, 0.f}, S1 = {0.f, 0.f, 0.f, 0.f};
    PIN(S0); PIN(S1);
    for (int spins = 0;; ++spins) {
      VMWAIT(0);
      unsigned bad = 0;
#pragma unroll
      for (int q = 0; q < 4; ++q) bad = umax4(bad, lds128(a_pw + q * 8192));
      if (all_ok(bad != EMPTY) || spins >= SPIN_CAP) break;
      QA4();
    }
    BAR();
    {
      v4u qf[16];
#pragma unroll
      for (int s = 0; s < QD; ++s) qf[s] = lds128(qa_[s & 3] + (s >> 2) * 256);
      SCHED();
#pragma unroll
      for (int s = 0; s < 16; ++s) {
        mfma_acc(S0, k0[s], qf[s]); mfma_acc(S1, k1[s], qf[s]);
        if (s + QD < 16) qf[s + QD] = lds128(qa_[(s + QD) & 3] + ((s + QD) >> 2) * 256);
        if (s >= 8) RD_V1(s)
        if (s < 8 && !(s & 1)) stsc(v4u{EMPTY, EMPTY, EMPTY, EMPTY}, rs, a_c, WS_OP + (s >> 1) * 131072 + ai * 256);
        SCHED();
      }
      float m0, mt;
      asm volatile("s_nop 7\n\tv_max3_f32 %0, %2, %3, %4\n\tv_max3_f32 %0, %0, %5, %6\n\tv_max3_f32 %1, %7, %8, %9\n\tv_max_f32 %0, %0, %1\n\t"
                   "v_mov_b32 %1, %0\n\ts_nop 1\n\tv_permlane16_swap_b32 %0, %1\n\tv_max_f32 %0, %0, %1\n\t"
                   "v_mov_b32 %1, %0\n\ts_nop 1\n\tv_permlane32_swap_b32 %0, %1\n\tv_max_f32 %0, %0, %1"
                   : "=&v"(m0), "=&v"(mt) : "v"(S0[0]), "v"(S0[1]), "v"(S0[2]), "v"(S0[3]), "v"(S1[0]), "v"(S1[1]), "v"(S1[2]), "v"(S1[3]));
      *lp<float>(a_mw) = m0;
    }
    BAR();
    float M;
    {
      const f4 m4 = *lp<f4>(a_mr);
      asm("v_max3_f32 %0, %1, %2, %3\n\tv_max_f32 %0, %0, %4" : "=&v"(M) : "v"(m4[0]), "v"(m4[1]), "v"(m4[2]), "v"(m4[3]));
      float p[8];
#pragma unroll
      for (int i = 0; i < 4; ++i) { p[i] = ex2(S0[i] - M); p[4 + i] = ex2(S1[i] - M); }
      *lp<v4u>(a_pw) = v4u{pk(p[0], p[1]), pk(p[2], p[3]), pk(p[4], p[5]), pk(p[6], p[7])};
      *lp<float>(a_lw) = ((p[0] + p[1]) + (p[2] + p[3])) + ((p[4] + p[5]) + (p[6] + p[7]));
    }
    VMWAIT(0);
    __builtin_amdgcn_s_setprio(2);
    BAR();
    {
      v4u pf_[8];
      pf_[0] = lds128(a_pq);
      const f4 l4 = *lp<f4>(a_lw & ~15);
#pragma unroll
      for (int i = 1; i < 8; ++i) pf_[i] = lds128(a_pq + ((i & 3) * 2 + (i >> 2)) * 1024);
      SCHED();
#pragma unroll
      for (int hh = 0; hh < 2; ++hh) {
        f4 O[4];
#pragma unroll
        for (int t = 0; t < 4; ++t) O[t] = f4{0.f, 0.f, 0.f, 0.f};
#pragma unroll
        for (int ks = 0; ks < 4; ++ks) {
#pragma unroll
          for (int t = 0; t < 4; ++t) O[t] = mfma(pf_[4 * hh + ks], vf[ks][t], O[t]);
          if (hh == 0 && ks == 0) { SCHED(); if (kt == 0) stsc(v4u{asu(M), tv, asu(add32(add16((l4[0] + l4[1]) + (l4[2] + l4[3])))), tv}, rs, a_s, WS_ST + ai * 512); SCHED(); }
        }
        if (hh == 0) SCHED();
        if (hh == 1) { PIN(O[3]); }
#pragma unroll
        for (int i = 0; i < 4; ++i)
          __builtin_amdgcn_raw_buffer_store_b64(v2u{pkc(O[0][i], O[1][i]), pkc(O[2][i], O[3][i])}, rs, a_o, WS_OP + (16 * hh + i) * 131072 + ai * 256, AUX_SC1);
      }
    }
    __builtin_amdgcn_s_setprio(0);
    const u64 t_pv = __builtin_amdgcn_s_memrealtime();
    const int mo = 16 * h + 4 * j + 2 * (lane & 1) + (lane >> 5);
    unsigned xo = 0;
    if (w == 6) {
      xo = *(const GAS u16*)(x + mo);
      if ((lane & 30) == 0) __builtin_nontemporal_store((u16)0, (GAS u16*)(out + mo));
    }
    if (ai < 112) {
      wait_until(t_pv + DLY_U);
      const rsrc_t rd = mkrs(wd, 1835008);
      const int gu = (r16 >> 2) & 1, hi = r16 >> 3;
      const int a_f = (r16 & 3) * 1024 + (64 * gu + 8 * hi + 2 * g) * 8;
      v4u dw[2], p = ldsc(rs, a_f, WS_FP + fi * 4096 + mi * 128);
#pragma unroll
      for (int i = 0; i < 2; ++i) dw[i] = ldnt(rd, a_d, (64 * fi + 16 * mi + 8 * i) * 1024 + w * 128);
      SCHED();
      for (int spins = 0; !all_ok((p.y == tv) & (p.w == tv)) && spins < SPIN_CAP; ++spins) { FENCE(); p = ldsc(rs, a_f, WS_FP + fi * 4096 + mi * 128); }
      float v0_ = asf(p.x), v1_ = asf(p.z);
      v0_ += dpp<0xB1>(v0_); v1_ += dpp<0xB1>(v1_);
      v0_ += dpp<0x4E>(v0_); v1_ += dpp<0x4E>(v1_);
      const float o0 = dpp<0x141>(v0_), o1 = dpp<0x141>(v1_);
      float hv[2];
#pragma unroll
      for (int e = 0; e < 2; ++e) {
        const float gt = rbf(gu ? (e ? o1 : o0) : (e ? v1_ : v0_)), up = rbf(gu ? (e ? v1_ : v0_) : (e ? o1 : o0));
        const float z2 = 2.0f * 0.7978845608028654f * 1.4426950408889634f * (gt + 0.044715f * gt * gt * gt);
        hv[e] = rbf(gt * rcp(1.0f + ex2(-z2))) * up;
      }
      const float t0 = dpp<0x128>(hv[0]), t1 = dpp<0x128>(hv[1]);
      unsigned mk2[4];
#pragma unroll
      for (int k = 0; k < 4; ++k) mk2[k] = mk[k] | dppu<0x128>(mk[k]);
      const float ha = hi ? t1 : hv[0], hb = hi ? hv[1] : t0;
      f4 acc = {0.f, 0.f, 0.f, 0.f};
      acc = mfma(xmask(pk(ha, ha), mk2), dw[0], acc);
      acc = mfma(xmask(pk(hb, hb), mk2), dw[1], acc);
      float y[4];
#pragma unroll
      for (int i = 0; i < 4; ++i) {
        const unsigned u = asu(dpp<0x128>(acc[i]));
        auto r = __builtin_amdgcn_permlane32_swap(u, u, false, false);
        y[i] = acc[i] + asf(r[1]);
      }
      const auto ya = __builtin_amdgcn_permlane32_swap(asu(y[0]), asu(y[2]), false, false), yb = __builtin_amdgcn_permlane32_swap(asu(y[1]), asu(y[3]), false, false);
      if (r16 < 8) stsc(v4u{ya[0], tv, yb[0], tv}, rs, (r16 >> 1) * 16384 + ((r16 & 1) * 8 + 4 * (g & 1) + 2 * (g >> 1)) * 8, WS_YP + (4 * w * 128 + ai) * 128);
    }
    if (w >= 6) {
      const bool isy = w == 7;
      const int off = (lane >> 1) * 128 + (lane & 1) * 16, so = (isy ? WS_YP : WS_SP) + h * 16384 + j * 32;
      int a_e = 1024 + ((lane & 1) * 2 + (lane >> 5)) * 4;
      PIN(a_e);
      {
        const int go = ((ai * 5 + (lane & 3) * 7) & (isy ? 63 : 127)) * 128;
        wait_until(t_pv + DLY_F);
        VMWAIT(0);
        for (int sp = 0; sp < SPIN_CAP; ++sp) {
          FENCE();
          const v2u t = __builtin_amdgcn_raw_buffer_load_b64(rs, go, so, AUX_SC1);
          if (!all_ok(t.y != tv)) break;
          __builtin_amdgcn_s_sleep(2);
        }
      }
      v4u p[4]; int spins = 0; unsigned bad;
      do {
        FENCE();
#pragma unroll
        for (int n = 0; n < 4; ++n) p[n] = ldsc(rs, off, so + n * 4096);
        if (isy && lane >= 32) p[3] = v4u{0u, tv, 0u, tv};
        bad = 0;
#pragma unroll
        for (int n = 0; n < 4; ++n) bad |= (p[n].y ^ tv) | (p[n].w ^ tv);
        PIN(bad);
      } while (!all_ok(bad == 0) && ++spins < SPIN_CAP);
      const float a0 = (asf(p[0].x) + asf(p[1].x)) + (asf(p[2].x) + asf(p[3].x));
      const float a1 = (asf(p[0].z) + asf(p[1].z)) + (asf(p[2].z) + asf(p[3].z));
      const auto pr2 = __builtin_amdgcn_permlane32_swap(asu(a0), asu(a1), false, false);
      float v = add16(asf(pr2[0]) + asf(pr2[1]));
      v += dpp<0x128>(v); v += dpp<0x124>(v); v += dpp<0x122>(v);
      v = rbf(v);
      if (isy) *lp<float>(a_e) = rbf(v * rcp(1.0f + ex2(-1.4426950408889634f * v)));
      BAR();
      if (!isy) {
        const unsigned o = pk(bflo(xo) + rbf(v * *lp<float>(a_e)), 0.f);
        if ((lane & 30) == 0) *(GAS u16*)(out + mo) = (u16)o;
      }
    }
  }
}

extern "C" void kernel_launch(void* const* d_in, const int* in_sizes, int n_in, void* d_out, int out_size, void* d_ws, size_t ws_size, hipStream_t stream) {
  (void)hipFuncSetAttribute((const void*)mla_decode, hipFuncAttributeMaxDynamicSharedMemorySize, LDS_BYTES);
  mla_decode<<<256, 512, LDS_BYTES, stream>>>((const u16*)d_in[0], (const u16*)d_in[2], (unsigned char*)d_ws, (const u16*)d_in[3], (const u16*)d_in[1], (const u16*)d_in[4], (const u16*)d_in[5],
                                              (const u16*)d_in[7], (const u16*)d_in[8], (const u16*)d_in[9], (u16*)d_out, d_in[0]);
}
```
